```python
import torch

def _softmax(x, axis=-1):
    return torch.softmax(x, dim=axis)
def _gelu(x):
    return torch.nn.functional.gelu(x, approximate="tanh")
def _silu(x):
    return torch.nn.functional.silu(x)
def _sigmoid(x):
    return torch.sigmoid(x)
def _layernorm(x, axis=-1, eps=1e-5):
    m = x.mean(dim=axis, keepdim=True)
    v = x.var(dim=axis, keepdim=True, unbiased=False)
    return (x - m) / torch.sqrt(v + eps)
def _scatter_add(tgt, idx, src, axis=0):
    return tgt.clone().index_add_(axis, idx, src)
def _segment_sum(src, seg, n):
    out = torch.zeros((n,) + src.shape[1:], dtype=src.dtype)
    return out.index_add_(0, seg, src)
def _pad(x, pw):
    flat = tuple(v for pair in reversed(pw) for v in pair)
    return torch.nn.functional.pad(x, flat)

kernel_name = 'recurrent_scan__grouped_kv_mqa__moe_arch_mod__prompt'

def setup_inputs():

    _w = (lambda a: (lambda t: t.to(torch.bfloat16) if t.is_floating_point() else t)(torch.as_tensor(a)))
    def _A_setup():
        B, S, D, H = 1, 2048, 1024, 32
        Dh = D // H
        return {
            "x": _w((lambda *s: torch.randn(*s, dtype=torch.bfloat16) * 0.05)(B, S, D)),
            "wq": _w((lambda *s: torch.randn(*s, dtype=torch.bfloat16) * 0.05)(D, H * Dh)),
            "wk": _w((lambda *s: torch.randn(*s, dtype=torch.bfloat16) * 0.05)(D, Dh)),
            "wv": _w((lambda *s: torch.randn(*s, dtype=torch.bfloat16) * 0.05)(D, Dh)),
            "wo": _w((lambda *s: torch.randn(*s, dtype=torch.bfloat16) * 0.05)(H * Dh, D)),
        }
    _A_in = _A_setup()

    def _B_setup():
        B, S, D, F, C = 1, 2048, 1024, 3584, 1
        return {
            "x": _w((lambda *s: torch.randn(*s, dtype=torch.bfloat16) * 0.05)(B, S, D)),
            "w_r": _w((lambda *s: torch.randn(*s, dtype=torch.bfloat16) * 0.05)(D)),
            "w_gate": _w((lambda *s: torch.randn(*s, dtype=torch.bfloat16) * 0.05)(D, F)),
            "w_up": _w((lambda *s: torch.randn(*s, dtype=torch.bfloat16) * 0.05)(D, F)),
            "w_down": _w((lambda *s: torch.randn(*s, dtype=torch.bfloat16) * 0.05)(F, D)),
        }
    _B_in = _B_setup()

    return {
        "A_x": _A_in["x"],
        "A_wq": _A_in["wq"],
        "A_wk": _A_in["wk"],
        "A_wv": _A_in["wv"],
        "A_wo": _A_in["wo"],
        "B_x": _B_in["x"],
        "B_w_r": _B_in["w_r"],
        "B_w_gate": _B_in["w_gate"],
        "B_w_up": _B_in["w_up"],
        "B_w_down": _B_in["w_down"],
    }

def reference(A_x, A_wq, A_wk, A_wv, A_wo, B_x, B_w_r, B_w_gate, B_w_up, B_w_down):
    def _A_ref(x, wq, wk, wv, wo):
        B, S, D = x.shape
        H = 32
        Dh = D // H
        q = (lambda x, *d: x.permute(*d))((x @ wq).reshape(B, S, H, Dh), 0, 2, 1, 3)
        k = x @ wk
        v = x @ wv
        scores = torch.einsum("bhqd,bkd->bhqk", q, k) * (Dh ** -0.5)
        p = _softmax(scores, axis=-1)
        o = torch.einsum("bhqk,bkd->bhqd", p, v)
        return (lambda x, *d: x.permute(*d))(o, 0, 2, 1, 3).reshape(B, S, H * Dh) @ wo

    def _B_ref(x, w_r, w_gate, w_up, w_down):
        B, S, D = x.shape
        C = 1
        r = x @ w_r
        idx = torch.argsort(-r, axis=-1)[:, :C]
        idx_e = (lambda x, n, axis: torch.repeat_interleave(x, n, dim=axis))(idx.reshape(B, C, 1), D, axis=2)
        x_sel = (lambda x, i, axis: torch.take_along_dim(x, i, dim=axis))(x, idx_e, axis=1)
        g = _silu(x_sel @ w_gate)
        u = x_sel @ w_up
        y = (g * u) @ w_down
        r_sel = _sigmoid((lambda x, i, axis: torch.take_along_dim(x, i, dim=axis))(r, idx, axis=1))
        y = y * r_sel.reshape(B, C, 1)
        b_off = (torch.arange(B) * S).reshape(B, 1)
        flat_idx = (idx + b_off).reshape(B * C)
        zero = (x * 0.0).reshape(B * S, D)
        upd = _scatter_add(zero, flat_idx, y.reshape(B * C, D), axis=0)
        return x + upd.reshape(B, S, D)

    x = A_x
    xn = _layernorm(x, axis=-1)
    s = _A_ref(xn, A_wq, A_wk, A_wv, A_wo)
    g = _silu(_B_ref(xn, B_w_r, B_w_gate, B_w_up, B_w_down))
    return x + s * g
```

```cpp
#include <hip/hip_runtime.h>
#include <hip/hip_bf16.h>
#include <stdint.h>
namespace naive {
typedef unsigned short u16;
__device__ __forceinline__ float b2f(u16 b) { return __uint_as_float((unsigned)b << 16); }
__device__ __forceinline__ u16 f2b(float f) { unsigned u = __float_as_uint(f); if ((u & 0x7fffffffu) > 0x7f800000u) return 0x7fc0; u += 0x7fffu + ((u >> 16) & 1u); return (u16)(u >> 16); }
__device__ __forceinline__ float rb(float f) { return b2f(f2b(f)); }
constexpr int S = 2048, D = 1024, H = 32, DH = 32, F = 3584;

__global__ void k_ln(const u16* x, const u16* wr, u16* xn, float* r) {
  int row = blockIdx.x, t = threadIdx.x; __shared__ float red[256];
  float v[4], s = 0; for (int i = 0; i < 4; i++) { v[i] = b2f(x[row * D + t * 4 + i]); s += v[i]; }
  red[t] = s; __syncthreads(); for (int o = 128; o; o >>= 1) { if (t < o) red[t] += red[t + o]; __syncthreads(); }
  float mean = red[0] / D; __syncthreads();
  float q = 0; for (int i = 0; i < 4; i++) q += (v[i] - mean) * (v[i] - mean);
  red[t] = q; __syncthreads(); for (int o = 128; o; o >>= 1) { if (t < o) red[t] += red[t + o]; __syncthreads(); }
  float var = rb(red[0] / D); __syncthreads();
  float m = rb(mean), d = rb(sqrtf(rb(var + 1e-5f)));
  float rr = 0;
  for (int i = 0; i < 4; i++) { float z = rb(rb(v[i] - m) / d); xn[row * D + t * 4 + i] = f2b(z); rr += z * b2f(wr[t * 4 + i]); }
  red[t] = rr; __syncthreads(); for (int o = 128; o; o >>= 1) { if (t < o) red[t] += red[t + o]; __syncthreads(); }
  if (t == 0) r[row] = rb(red[0]);
}
__global__ void k_gemm(const u16* A, const u16* B, u16* C, int M, int N, int K) {
  int n = blockIdx.x * 32 + (threadIdx.x & 31), m = blockIdx.y * 8 + (threadIdx.x >> 5);
  float acc = 0; for (int k = 0; k < K; k++) acc += b2f(A[(size_t)m * K + k]) * b2f(B[(size_t)k * N + n]);
  C[(size_t)m * N + n] = f2b(acc);
}
__global__ void k_attn(const u16* q, const u16* k, const u16* v, u16* o) {
  int row = blockIdx.x, h = blockIdx.y, t = threadIdx.x;
  __shared__ float sc[S]; __shared__ float red[256]; __shared__ float qs[DH];
  if (t < DH) qs[t] = b2f(q[row * D + h * DH + t]); __syncthreads();
  float mx = -INFINITY;
  for (int j = t; j < S; j += 256) { float a = 0; for (int d = 0; d < DH; d++) a += qs[d] * b2f(k[j * DH + d]); a = rb(rb(a) * 0.17677669529663687f); sc[j] = a; mx = fmaxf(mx, a); }
  red[t] = mx; __syncthreads(); for (int o2 = 128; o2; o2 >>= 1) { if (t < o2) red[t] = fmaxf(red[t], red[t + o2]); __syncthreads(); }
  mx = red[0]; __syncthreads();
  float s = 0; for (int j = t; j < S; j += 256) { float e = expf(sc[j] - mx); sc[j] = e; s += e; }
  red[t] = s; __syncthreads(); for (int o2 = 128; o2; o2 >>= 1) { if (t < o2) red[t] += red[t + o2]; __syncthreads(); }
  s = red[0]; __syncthreads();
  int d = t & 31, part = t >> 5; float a = 0;
  for (int j = part; j < S; j += 8) a += rb(sc[j] / s) * b2f(v[j * DH + d]);
  red[t] = a; __syncthreads();
  if (t < 32) { float z = 0; for (int p = 0; p < 8; p++) z += red[p * 32 + t]; o[row * D + h * DH + t] = f2b(z); }
}
__global__ void k_argmax(const float* r, int* idx) { if (threadIdx.x == 0) { int b = 0; for (int i = 1; i < S; i++) if (r[i] > r[b]) b = i; *idx = b; } }
__global__ void k_gu(const u16* xn, const int* idx, const u16* wg, const u16* wu, float* h) {
  int f = blockIdx.x * 64 + threadIdx.x; const u16* xs = xn + (size_t)(*idx) * D; float g = 0, u = 0;
  for (int k = 0; k < D; k++) { float xv = b2f(xs[k]); g += xv * b2f(wg[(size_t)k * F + f]); u += xv * b2f(wu[(size_t)k * F + f]); }
  g = rb(g); u = rb(u); g = rb(g / (1.f + expf(-g))); h[f] = rb(g * u);
}
__global__ void k_down(const float* h, const u16* wd, const float* r, const int* idx, float* y) {
  int n = blockIdx.x * 64 + threadIdx.x; float a = 0; for (int f = 0; f < F; f++) a += h[f] * b2f(wd[(size_t)f * D + n]);
  float rs = rb(1.f / (1.f + expf(-r[*idx]))); y[n] = rb(rb(a) * rs);
}
__global__ void k_final(const u16* x, const u16* xn, const u16* s, const float* y, const int* idx, u16* out) {
  int i = blockIdx.x * 256 + threadIdx.x; int row = i / D, c = i % D;
  float b = b2f(xn[i]); if (row == *idx) b = rb(b + y[c]);
  float g = rb(b / (1.f + expf(-b)));
  out[i] = f2b(b2f(x[i]) + rb(b2f(s[i]) * g));
}
inline void launch(void* const* d_in, void* d_out, void* d_ws, hipStream_t st) {
  const u16 *x = (const u16*)d_in[0], *wq = (const u16*)d_in[1], *wk = (const u16*)d_in[2], *wv = (const u16*)d_in[3], *wo = (const u16*)d_in[4];
  const u16 *wr = (const u16*)d_in[6], *wg = (const u16*)d_in[7], *wu = (const u16*)d_in[8], *wd = (const u16*)d_in[9];
  char* w = (char*)d_ws; u16* xn = (u16*)w; w += S * D * 2; u16* q = (u16*)w; w += S * D * 2; u16* o = (u16*)w; w += S * D * 2; u16* s = (u16*)w; w += S * D * 2;
  u16* k = (u16*)w; w += S * DH * 2; u16* v = (u16*)w; w += S * DH * 2; float* r = (float*)w; w += S * 4; float* h = (float*)w; w += F * 4; float* y = (float*)w; w += D * 4; int* idx = (int*)w;
  k_ln<<<S, 256, 0, st>>>(x, wr, xn, r);
  k_gemm<<<dim3(D / 32, S / 8), 256, 0, st>>>(xn, wq, q, S, D, D);
  k_gemm<<<dim3(1, S / 8), 256, 0, st>>>(xn, wk, k, S, DH, D);
  k_gemm<<<dim3(1, S / 8), 256, 0, st>>>(xn, wv, v, S, DH, D);
  k_attn<<<dim3(S, H), 256, 0, st>>>(q, k, v, o);
  k_gemm<<<dim3(D / 32, S / 8), 256, 0, st>>>(o, wo, s, S, D, D);
  k_argmax<<<1, 64, 0, st>>>(r, idx);
  k_gu<<<F / 64, 64, 0, st>>>(xn, idx, wg, wu, h);
  k_down<<<D / 64, 64, 0, st>>>(h, wd, r, idx, y);
  k_final<<<S * D / 256, 256, 0, st>>>(x, xn, s, y, idx, (u16*)d_out);
}
}
extern "C" void kernel_launch(void* const* d_in, const int* in_sizes, int n_in, void* d_out, int out_size, void* d_ws, size_t ws_size, hipStream_t stream) {
  naive::launch(d_in, d_out, d_ws, stream);
}
```

```cpp
#include <hip/hip_runtime.h>
#include <hip/hip_bf16.h>
#pragma clang diagnostic ignored "-Winline-asm"

__global__ __launch_bounds__(512, 2) void fused_kernel(const void* x, const void* wq, const void* wk, const void* wv, const void* wo,
                                                       const void* wr, const void* wg, const void* wu, const void* wd, void* out, void* ws) {
  __shared__ __attribute__((aligned(16))) char lds[163840];
  unsigned tid = threadIdx.x;
  asm volatile(
      ".Lkstart:\n"
      "s_load_dwordx16 s[8:23], s[0:1], 0\n"
      "s_load_dwordx4 s[24:27], s[0:1], 64\n"
      "s_load_dwordx2 s[28:29], s[0:1], 80\n"
      "s_getpc_b64 s[68:69]\n"
      ".Lpc0:\n"
      "v_lshrrev_b32 v2, 6, v0\n"
      "v_lshlrev_b32 v14, 3, v0\n"
      "s_lshr_b32 s81, s2, 3\n"
      "s_lshl_b32 s82, s81, 11\n"
      "v_readfirstlane_b32 s83, v2\n"
      "s_and_b32 s69, s69, 0xffff\n"
      "s_mov_b32 s70, .Lkend-.Lpc0\n"
      "s_mov_b32 s71, 0x20000\n"
      "v_add_u32 v14, s82, v14\n"
      "v_and_b32 v1, 63, v0\n"
      "s_mov_b32 s39, s2\n"
      "s_and_b32 s80, s2, 6\n"
      "s_and_b32 s82, s81, 7\n"
      "s_lshl_b32 s31, s80, 2\n"
      "s_add_u32 s31, s31, s82\n"
      "s_and_b32 s80, s2, 1\n"
      "s_lshl_b32 s80, s80, 2\n"
      "s_lshr_b32 s79, s81, 3\n"
      "s_add_u32 s79, s79, s80\n"
      "s_and_b32 s30, s83, 3\n"
      "s_and_b32 s33, s30, 1\n"
      "s_lshr_b32 s34, s30, 1\n"
      "s_cmp_lt_u32 s83, 4\n"
      "s_cbranch_scc0 .Lhelper\n"
      "s_setprio 1\n"
      "buffer_load_dwordx2 v[158:159], v14, s[68:71], 0 offen\n"
      "v_and_b32 v10, 31, v1\n"
      "v_lshrrev_b32 v11, 5, v1\n"
      "v_and_b32 v12, 15, v1\n"
      "v_xor_b32 v12, v12, v11\n"
      "v_lshlrev_b32 v12, 4, v12\n"
      "s_lshl_b32 s80, s33, 13\n"
      "v_lshl_add_u32 v13, v10, 8, s80\n"
      "v_add_u32 v146, v13, v12\n"
      "v_bfe_u32 v12, v1, 2, 2\n"
      "v_and_b32 v13, 3, v1\n"
      "v_bfe_u32 v14, v1, 4, 1\n"
      "v_lshlrev_b32 v14, 5, v14\n"
      "v_lshlrev_b32 v15, 1, v13\n"
      "v_lshrrev_b32 v13, 1, v13\n"
      "v_and_b32 v15, 2, v15\n"
      "v_or_b32 v13, v13, v15\n"
      "v_lshl_add_u32 v13, v13, 3, v14\n"
      "v_lshl_add_u32 v14, v11, 3, v12\n"
      "s_lshl_b32 s82, s34, 1\n"
      "v_lshl_add_u32 v13, v14, 8, v13\n"
      "v_xor_b32 v12, s82, v12\n"
      "v_lshl_add_u32 v147, v12, 6, v13\n"
      "v_xor_b32 v150, 64, v147\n"
      "v_add_u32 v148, 65536, v147\n"
      "v_add_u32 v149, 131072, v147\n"
      "v_add_u32 v151, 65536, v150\n"
      "v_add_u32 v152, 131072, v150\n"
      "v_lshlrev_b32 v145, 4, v1\n"
      "s_lshl_b32 s80, s79, 8\n"
      "s_lshl_b32 s81, s30, 6\n"
      "s_add_u32 s94, s80, s81\n"
      "s_lshl_b32 s80, s33, 8\n"
      "s_add_u32 s80, s80, 163328\n"
      "v_lshl_add_u32 v154, v10, 3, s80\n"
      "s_lshl_b32 s80, s34, 8\n"
      "s_add_u32 s80, s80, 157824\n"
      "v_lshl_add_u32 v155, v11, 4, s80\n"
      "s_waitcnt lgkmcnt(0)\n"
      "s_load_dword s36, s[28:29], 0\n"
      "s_add_u32 s35, s31, s79\n"
      "s_and_b32 s35, s35, 7\n"
      "s_lshl_b32 s80, s35, 8\n"
      "v_lshl_add_u32 v12, v1, 2, s80\n"
      "global_load_dword v128, v12, s[18:19]\n"
      "s_mov_b32 s48, s28\n"
      "s_and_b32 s49, s29, 0xffff\n"
      "s_mov_b32 s50, 6553600\n"
      "s_mov_b32 s51, 0x20000\n"
      "s_lshl_b32 s87, s31, 17\n"
      "s_add_u32 s40, s8, s87\n"
      "s_addc_u32 s41, s9, 0\n"
      "s_and_b32 s41, s41, 0xffff\n"
      "s_mov_b32 s42, 131072\n"
      "s_mov_b32 s43, 0x20000\n"
      "s_add_u32 s72, s26, s87\n"
      "s_addc_u32 s73, s27, 0\n"
      "s_and_b32 s73, s73, 0xffff\n"
      "s_mov_b32 s74, 131072\n"
      "s_mov_b32 s75, 0x20000\n"
      "s_add_u32 s22, s87, 2359296\n"
      "s_lshl_b32 s81, s79, 14\n"
      "s_add_u32 s22, s22, s81\n"
      "s_lshl_b32 s81, s34, 13\n"
      "s_add_u32 s22, s22, s81\n"
      "s_lshl_b32 s81, s33, 10\n"
      "s_add_u32 s22, s22, s81\n"
      "s_add_u32 s23, s22, 4096\n"
      "s_lshl_b32 s24, s31, 11\n"
      "s_add_u32 s24, s24, 1572864\n"
      "s_add_u32 s24, s24, s94\n"
      "v_add_u32 v2, 0, v146\n"
      "v_xor_b32 v3, 32, v2\n"
      "v_xor_b32 v4, 64, v2\n"
      "v_xor_b32 v5, 96, v2\n"
      "v_xor_b32 v6, 128, v2\n"
      "v_xor_b32 v7, 160, v2\n"
      "v_xor_b32 v8, 192, v2\n"
      "v_xor_b32 v9, 224, v2\n"
      "v_bfe_u32 v12, v1, 4, 1\n"
      "v_and_b32 v13, 15, v1\n"
      "v_cmp_eq_u32 vcc, v12, v13\n"
      "v_mov_b32 v12, 0x3f803f80\n"
      "v_and_b32 v13, 14, v1\n"
      "v_cndmask_b32 v156, 0, v12, vcc\n"
      "v_or_b32 v12, s33, v13\n"
      "v_min_u32 v12, 1, v12\n"
      "v_lshlrev_b32 v12, 31, v12\n"
      "v_and_b32 v13, 1, v1\n"
      "v_lshl_add_u32 v12, v13, 6, v12\n"
      "v_lshrrev_b32 v13, 4, v1\n"
      "v_lshl_add_u32 v14, v13, 4, v12\n"
      "s_mov_b32 s76, 0xffffff00\n"
      "v_lshlrev_b32 v125, 2, v1\n"
      "v_add_u32 v125, 147456, v125\n"
      "v_mov_b32 v12, 0x3f803f80\n"
      "v_mov_b32 v13, 0\n"
      "ds_write_b32 v125, v12\n"
      "ds_write_b32 v125, v13 offset:544\n"
      "v_and_b32 v12, 3, v1\n"
      "v_bfe_u32 v13, v1, 4, 1\n"
      "v_lshlrev_b32 v13, 5, v13\n"
      "v_lshl_add_u32 v12, v12, 3, v13\n"
      "v_bfe_u32 v13, v1, 2, 2\n"
      "v_lshl_add_u32 v13, v11, 3, v13\n"
      "v_lshl_add_u32 v12, v13, 6, v12\n"
      "s_lshl_b32 s80, s34, 13\n"
      "s_add_u32 s80, s80, 16384\n"
      "v_add_u32 v124, s80, v12\n"
      "v_min_u32 v12, 2, v10\n"
      "v_mul_u32_u24 v12, 272, v12\n"
      "v_lshl_add_u32 v12, v11, 4, v12\n"
      "v_add_u32 v12, 147456, v12\n"
      "s_sub_u32 s56, 0, s34\n"
      "s_mov_b32 s57, s56\n"
      "v_add_u32 v13, 0, v12\n"
      "v_cndmask_b32 v116, v13, v2, s[56:57]\n"
      "v_add_u32 v13, 32, v12\n"
      "v_cndmask_b32 v117, v13, v3, s[56:57]\n"
      "v_add_u32 v13, 64, v12\n"
      "v_cndmask_b32 v118, v13, v4, s[56:57]\n"
      "v_add_u32 v13, 96, v12\n"
      "v_cndmask_b32 v119, v13, v5, s[56:57]\n"
      "v_add_u32 v13, 128, v12\n"
      "v_cndmask_b32 v120, v13, v6, s[56:57]\n"
      "v_add_u32 v13, 160, v12\n"
      "v_cndmask_b32 v121, v13, v7, s[56:57]\n"
      "v_add_u32 v13, 192, v12\n"
      "v_cndmask_b32 v122, v13, v8, s[56:57]\n"
      "v_add_u32 v13, 224, v12\n"
      "v_cndmask_b32 v123, v13, v9, s[56:57]\n"
      "s_lshl_b32 s66, s31, 17\n"
      "s_lshl_b32 s81, s34, 16\n"
      "s_add_u32 s66, s66, s81\n"
      "s_lshl_b32 s81, s79, 11\n"
      "s_add_u32 s66, s66, s81\n"
      "s_lshl_b32 s81, s33, 10\n"
      "s_add_u32 s66, s66, s81\n"
      "s_add_u32 s66, s66, 2359296\n"
      "s_lshl_b32 s67, s31, 13\n"
      "s_lshl_b32 s81, s79, 10\n"
      "s_add_u32 s67, s67, s81\n"
      "s_lshl_b32 s81, s34, 9\n"
      "s_add_u32 s67, s67, s81\n"
      "s_lshl_b32 s81, s33, 8\n"
      "s_add_u32 s67, s67, s81\n"
      "s_add_u32 s67, s67, 786432\n"
      "s_lshl_b32 s68, s31, 11\n"
      "s_lshl_b32 s81, s79, 8\n"
      "s_add_u32 s68, s68, s81\n"
      "s_lshl_b32 s81, s34, 7\n"
      "s_add_u32 s68, s68, s81\n"
      "s_add_u32 s68, s68, 1048576\n"
      "v_and_b32 v12, 1, v1\n"
      "v_cmp_ne_u32 s[58:59], 0, v12\n"
      "v_and_b32 v12, 2, v1\n"
      "v_cmp_ne_u32 s[60:61], 0, v12\n"
      "v_and_b32 v12, 8, v1\n"
      "v_cmp_ne_u32 s[62:63], 0, v12\n"
      "v_and_b32 v12, 16, v1\n"
      "v_cmp_ne_u32 s[64:65], 0, v12\n"
      "v_bfe_u32 v12, v1, 2, 1\n"
      "v_and_b32 v12, s34, v12\n"
      "v_xor_b32 v12, v12, v11\n"
      "v_lshlrev_b32 v12, 31, v12\n"
      "v_lshl_add_u32 v127, v10, 3, v12\n"
      "s_waitcnt vmcnt(0)\n"
      "ds_write_b32 v125, v128 offset:272\n"
      "s_mov_b32 s80, 0x3f803f80\n"
      "v_dot2_f32_bf16 v129, v128, s80, 0\n"
      "s_nop 2\n"
      "v_add_f32_dpp v129, v129, v129 row_shr:1 row_mask:0xf bank_mask:0xf bound_ctrl:0\n"
      "s_nop 1\n"
      "v_add_f32_dpp v129, v129, v129 row_shr:2 row_mask:0xf bank_mask:0xf bound_ctrl:0\n"
      "s_nop 1\n"
      "v_add_f32_dpp v129, v129, v129 row_shr:4 row_mask:0xf bank_mask:0xf bound_ctrl:0\n"
      "s_nop 1\n"
      "v_add_f32_dpp v129, v129, v129 row_shr:8 row_mask:0xf bank_mask:0xf bound_ctrl:0\n"
      "s_nop 1\n"
      "v_add_f32_dpp v129, v129, v129 row_bcast:15 row_mask:0xa bank_mask:0xf bound_ctrl:0\n"
      "s_nop 1\n"
      "v_add_f32_dpp v129, v129, v129 row_bcast:31 row_mask:0xc bank_mask:0xf bound_ctrl:0\n"
      "s_nop 0\n"
      "v_readlane_b32 s69, v129, 63\n"
      "s_waitcnt lgkmcnt(0)\n"
      "s_add_u32 s36, s36, 1\n"
      "s_and_b32 s80, s36, 0xff\n"
      "v_mov_b32 v15, s80\n"
      "v_mov_b32 v157, v156\n"
      "v_mov_b32 v158, v156\n"
      "v_mov_b32 v159, v156\n"
      "v_mov_b32 v144, 148288\n"
      "s_lshl_b32 s80, s30, 2\n"
      "v_add_u32 v153, s80, v144\n"
      "v_mov_b32 v12, 0\n"
      "v_mov_b32 v13, 0\n"
      "ds_write_b32 v153, v12\n"
      "ds_write_b32 v153, v12 offset:16\n"
      "s_waitcnt lgkmcnt(0)\n"
      "s_lshl_b32 s80, s34, 8\n"
      "s_add_u32 s80, s80, 151552\n"
      "v_and_b32 v13, 0xff, v14\n"
      "v_add_u32 v13, s80, v13\n"
      "s_barrier\n"
      "ds_read_b128 a[0:3], v2\n"
      "ds_read_b64_tr_b16 v[16:17], v124 offset:0\n"
      "ds_read_b64_tr_b16 v[18:19], v124 offset:256\n"
      "ds_read_b128 a[4:7], v3\n"
      "ds_read_b64_tr_b16 v[20:21], v124 offset:1024\n"
      "ds_read_b64_tr_b16 v[22:23], v124 offset:1280\n"
      "ds_read_b128 a[8:11], v4\n"
      "ds_read_b64_tr_b16 v[24:25], v124 offset:2048\n"
      "ds_read_b64_tr_b16 v[26:27], v124 offset:2304\n"
      "ds_read_b128 a[12:15], v5\n"
      "ds_read_b64_tr_b16 v[28:29], v124 offset:3072\n"
      "ds_read_b64_tr_b16 v[30:31], v124 offset:3328\n"
      "ds_read_b128 a[16:19], v6\n"
      "ds_read_b64_tr_b16 v[32:33], v124 offset:4096\n"
      "ds_read_b64_tr_b16 v[34:35], v124 offset:4352\n"
      "s_waitcnt lgkmcnt(12)\n"
      "v_mfma_f32_32x32x16_bf16 v[80:95], v[16:19], a[0:3], 0\n"
      "ds_read_b128 a[20:23], v7\n"
      "ds_read_b64_tr_b16 v[36:37], v124 offset:5120\n"
      "ds_read_b64_tr_b16 v[38:39], v124 offset:5376\n"
      "s_waitcnt lgkmcnt(12)\n"
      "v_mfma_f32_32x32x16_bf16 v[80:95], v[20:23], a[4:7], v[80:95]\n"
      "ds_read_b128 a[24:27], v8\n"
      "ds_read_b64_tr_b16 v[40:41], v124 offset:6144\n"
      "ds_read_b64_tr_b16 v[42:43], v124 offset:6400\n"
      "s_waitcnt lgkmcnt(12)\n"
      "v_mfma_f32_32x32x16_bf16 v[80:95], v[24:27], a[8:11], v[80:95]\n"
      "ds_read_b128 a[28:31], v9\n"
      "ds_read_b64_tr_b16 v[44:45], v124 offset:7168\n"
      "ds_read_b64_tr_b16 v[46:47], v124 offset:7424\n"
      "s_waitcnt lgkmcnt(12)\n"
      "v_mfma_f32_32x32x16_bf16 v[80:95], v[28:31], a[12:15], v[80:95]\n"
      "ds_read_b128 v[48:51], v116\n"
      "ds_read_b128 v[52:55], v117\n"
      "ds_read_b128 v[56:59], v118\n"
      "s_waitcnt lgkmcnt(12)\n"
      "v_mfma_f32_32x32x16_bf16 v[80:95], v[32:35], a[16:19], v[80:95]\n"
      "ds_read_b128 v[60:63], v119\n"
      "ds_read_b128 v[64:67], v120\n"
      "ds_read_b128 v[68:71], v121\n"
      "s_waitcnt lgkmcnt(12)\n"
      "v_mfma_f32_32x32x16_bf16 v[80:95], v[36:39], a[20:23], v[80:95]\n"
      "ds_read_b128 v[72:75], v122\n"
      "ds_read_b128 v[76:79], v123\n"
      "s_waitcnt lgkmcnt(11)\n"
      "v_mfma_f32_32x32x16_bf16 v[80:95], v[40:43], a[24:27], v[80:95]\n"
      "s_waitcnt lgkmcnt(8)\n"
      "v_mfma_f32_32x32x16_bf16 v[80:95], v[44:47], a[28:31], v[80:95]\n"
      "s_waitcnt lgkmcnt(0)\n"
      "v_mfma_f32_32x32x16_bf16 v[96:111], v[48:51], a[0:3], 0\n"
      "v_mfma_f32_32x32x16_bf16 v[96:111], v[52:55], a[4:7], v[96:111]\n"
      "v_mfma_f32_32x32x16_bf16 v[96:111], v[56:59], a[8:11], v[96:111]\n"
      "s_nop 7\n"
      "v_and_or_b32 v80, v80, s76, v15\n"
      "v_and_or_b32 v81, v81, s76, v15\n"
      "v_and_or_b32 v82, v82, s76, v15\n"
      "v_and_or_b32 v83, v83, s76, v15\n"
      "v_mfma_f32_32x32x16_bf16 v[96:111], v[60:63], a[12:15], v[96:111]\n"
      "buffer_store_dwordx4 v[80:83], v145, s[48:51], s66 offen sc1\n"
      "v_and_or_b32 v84, v84, s76, v15\n"
      "v_and_or_b32 v85, v85, s76, v15\n"
      "v_and_or_b32 v86, v86, s76, v15\n"
      "v_mfma_f32_32x32x16_bf16 v[96:111], v[64:67], a[16:19], v[96:111]\n"
      "v_and_or_b32 v87, v87, s76, v15\n"
      "s_add_u32 s66, s66, 16384\n"
      "buffer_store_dwordx4 v[84:87], v145, s[48:51], s66 offen sc1\n"
      "v_and_or_b32 v88, v88, s76, v15\n"
      "v_mfma_f32_32x32x16_bf16 v[96:111], v[68:71], a[20:23], v[96:111]\n"
      "v_and_or_b32 v89, v89, s76, v15\n"
      "v_and_or_b32 v90, v90, s76, v15\n"
      "v_and_or_b32 v91, v91, s76, v15\n"
      "s_add_u32 s66, s66, 16384\n"
      "v_mfma_f32_32x32x16_bf16 v[96:111], v[72:75], a[24:27], v[96:111]\n"
      "buffer_store_dwordx4 v[88:91], v145, s[48:51], s66 offen sc1\n"
      "v_and_or_b32 v92, v92, s76, v15\n"
      "v_and_or_b32 v93, v93, s76, v15\n"
      "v_and_or_b32 v94, v94, s76, v15\n"
      "v_mfma_f32_32x32x16_bf16 v[96:111], v[76:79], a[28:31], v[96:111]\n"
      "v_and_or_b32 v95, v95, s76, v15\n"
      "s_add_u32 s66, s66, 16384\n"
      "buffer_store_dwordx4 v[92:95], v145, s[48:51], s66 offen sc1\n"
      "v_mfma_f32_16x16x32_bf16 v[112:115], v[16:19], v[156:159], 0\n"
      "v_mfma_f32_16x16x32_bf16 v[112:115], v[20:23], v[156:159], v[112:115]\n"
      "v_mfma_f32_16x16x32_bf16 v[112:115], v[24:27], v[156:159], v[112:115]\n"
      "s_nop 5\n"
      "v_cndmask_b32 v128, v96, v97, s[58:59]\n"
      "v_cndmask_b32 v129, v98, v99, s[58:59]\n"
      "v_cndmask_b32 v130, v100, v101, s[58:59]\n"
      "v_cndmask_b32 v131, v102, v103, s[58:59]\n"
      "v_mfma_f32_16x16x32_bf16 v[112:115], v[28:31], v[156:159], v[112:115]\n"
      "v_cndmask_b32 v132, v104, v105, s[58:59]\n"
      "v_cndmask_b32 v133, v106, v107, s[58:59]\n"
      "v_cndmask_b32 v134, v108, v109, s[58:59]\n"
      "v_cndmask_b32 v135, v110, v111, s[58:59]\n"
      "v_mfma_f32_16x16x32_bf16 v[112:115], v[32:35], v[156:159], v[112:115]\n"
      "v_cndmask_b32 v128, v128, v129, s[60:61]\n"
      "v_cndmask_b32 v129, v130, v131, s[60:61]\n"
      "v_cndmask_b32 v130, v132, v133, s[60:61]\n"
      "v_cndmask_b32 v131, v134, v135, s[60:61]\n"
      "v_mfma_f32_16x16x32_bf16 v[112:115], v[36:39], v[156:159], v[112:115]\n"
      "v_cndmask_b32 v128, v128, v129, s[62:63]\n"
      "v_cndmask_b32 v129, v130, v131, s[62:63]\n"
      "v_cndmask_b32 v128, v128, v129, s[64:65]\n"
      "v_mov_b32 v129, s69\n"
      "v_mfma_f32_16x16x32_bf16 v[112:115], v[40:43], v[156:159], v[112:115]\n"
      "v_cndmask_b32 v128, v96, v128, s[56:57]\n"
      "v_cndmask_b32 v129, v97, v129, s[56:57]\n"
      "v_and_or_b32 v136, v128, s76, v15\n"
      "v_and_or_b32 v137, v129, s76, v15\n"
      "v_mfma_f32_16x16x32_bf16 v[112:115], v[44:47], v[156:159], v[112:115]\n"
      "buffer_store_dwordx2 v[136:137], v127, s[48:51], s67 offen sc1\n"
      "s_nop 6\n"
      "v_and_or_b32 v112, v112, s76, v15\n"
      "v_and_or_b32 v113, v113, s76, v15\n"
      "v_and_or_b32 v114, v114, s76, v15\n"
      "v_and_or_b32 v115, v115, s76, v15\n"
      "buffer_store_dwordx4 v[112:115], v14, s[48:51], s68 offen sc1\n"
      "s_barrier\n"
      "v_add_u32 v2, 65536, v146\n"
      "v_xor_b32 v3, 32, v2\n"
      "v_xor_b32 v4, 64, v2\n"
      "v_xor_b32 v5, 96, v2\n"
      "v_xor_b32 v6, 128, v2\n"
      "v_xor_b32 v7, 160, v2\n"
      "v_xor_b32 v8, 192, v2\n"
      "v_xor_b32 v9, 224, v2\n"
      "ds_read_b64_tr_b16 v[16:17], v147 offset:32768\n"
      "ds_read_b64_tr_b16 v[18:19], v147 offset:33792\n"
      "ds_read_b64_tr_b16 v[32:33], v150 offset:32768\n"
      "ds_read_b64_tr_b16 v[34:35], v150 offset:33792\n"
      "ds_read_b64_tr_b16 v[20:21], v147 offset:36864\n"
      "ds_read_b64_tr_b16 v[22:23], v147 offset:37888\n"
      "ds_read_b64_tr_b16 v[36:37], v150 offset:36864\n"
      "ds_read_b64_tr_b16 v[38:39], v150 offset:37888\n"
      "ds_read_b64_tr_b16 v[24:25], v147 offset:40960\n"
      "ds_read_b64_tr_b16 v[26:27], v147 offset:41984\n"
      "ds_read_b64_tr_b16 v[40:41], v150 offset:40960\n"
      "ds_read_b64_tr_b16 v[42:43], v150 offset:41984\n"
      "ds_read_b64_tr_b16 v[28:29], v147 offset:45056\n"
      "ds_read_b64_tr_b16 v[30:31], v147 offset:46080\n"
      "ds_read_b64_tr_b16 v[44:45], v150 offset:45056\n"
      "ds_read_b64_tr_b16 v[46:47], v150 offset:46080\n"
      "ds_read_b64_tr_b16 v[48:49], v147 offset:49152\n"
      "ds_read_b64_tr_b16 v[50:51], v147 offset:50176\n"
      "ds_read_b64_tr_b16 v[64:65], v150 offset:49152\n"
      "ds_read_b64_tr_b16 v[66:67], v150 offset:50176\n"
      "ds_read_b64_tr_b16 v[52:53], v147 offset:53248\n"
      "ds_read_b64_tr_b16 v[54:55], v147 offset:54272\n"
      "ds_read_b64_tr_b16 v[68:69], v150 offset:53248\n"
      "ds_read_b64_tr_b16 v[70:71], v150 offset:54272\n"
      "ds_read_b64_tr_b16 v[56:57], v147 offset:57344\n"
      "ds_read_b64_tr_b16 v[58:59], v147 offset:58368\n"
      "ds_read_b64_tr_b16 v[72:73], v150 offset:57344\n"
      "ds_read_b64_tr_b16 v[74:75], v150 offset:58368\n"
      "ds_read_b64_tr_b16 v[60:61], v147 offset:61440\n"
      "ds_read_b64_tr_b16 v[62:63], v147 offset:62464\n"
      "ds_read_b64_tr_b16 v[76:77], v150 offset:61440\n"
      "ds_read_b64_tr_b16 v[78:79], v150 offset:62464\n"
      "v_mov_b32 v12, 1\n"
      "ds_write_b32 v153, v12\n"
      "s_waitcnt lgkmcnt(0)\n"
      "ds_read_b64 v[10:11], v144 offset:16\n"
      "s_waitcnt lgkmcnt(0)\n"
      "v_min_u32 v12, v10, v11\n"
      "v_cmp_gt_u32 vcc, 1, v12\n"
      "s_nop 0\n"
      "s_cbranch_vccz .Lsc1_ok\n"
      "s_nop 2\n"
      "s_mov_b32 s90, 0x100000\n"
      ".Lsc1:\n"
      "s_sleep 1\n"
      "ds_read_b64 v[10:11], v144 offset:16\n"
      "s_waitcnt lgkmcnt(0)\n"
      "v_min_u32 v12, v10, v11\n"
      "v_cmp_gt_u32 vcc, 1, v12\n"
      "s_sub_u32 s90, s90, 1\n"
      "s_nop 3\n"
      "s_cbranch_vccz .Lsc1_ok\n"
      "s_cbranch_scc0 .Lsc1\n"
      ".Lsc1_ok:\n"
      "v_mfma_f32_32x32x16_bf16 v[112:127], v[16:19], a[0:3], 0\n"
      "ds_read_b128 a[32:35], v2\n"
      "ds_read_b128 a[36:39], v3\n"
      "ds_read_b128 a[40:43], v4\n"
      "ds_read_b128 a[44:47], v5\n"
      "v_mfma_f32_32x32x16_bf16 v[128:143], v[32:35], a[0:3], 0\n"
      "ds_read_b128 a[48:51], v6\n"
      "ds_read_b128 a[52:55], v7\n"
      "ds_read_b128 a[56:59], v8\n"
      "ds_read_b128 a[60:63], v9\n"
      "v_mfma_f32_32x32x16_bf16 v[112:127], v[20:23], a[4:7], v[112:127]\n"
      "ds_read_b64_tr_b16 v[80:81], v148 offset:16384\n"
      "ds_read_b64_tr_b16 v[82:83], v148 offset:17408\n"
      "ds_read_b64_tr_b16 v[96:97], v151 offset:16384\n"
      "ds_read_b64_tr_b16 v[98:99], v151 offset:17408\n"
      "v_mfma_f32_32x32x16_bf16 v[128:143], v[36:39], a[4:7], v[128:143]\n"
      "ds_read_b64_tr_b16 v[84:85], v148 offset:20480\n"
      "ds_read_b64_tr_b16 v[86:87], v148 offset:21504\n"
      "ds_read_b64_tr_b16 v[100:101], v151 offset:20480\n"
      "ds_read_b64_tr_b16 v[102:103], v151 offset:21504\n"
      "v_mfma_f32_32x32x16_bf16 v[112:127], v[24:27], a[8:11], v[112:127]\n"
      "ds_read_b64_tr_b16 v[88:89], v148 offset:24576\n"
      "ds_read_b64_tr_b16 v[90:91], v148 offset:25600\n"
      "ds_read_b64_tr_b16 v[104:105], v151 offset:24576\n"
      "ds_read_b64_tr_b16 v[106:107], v151 offset:25600\n"
      "v_mfma_f32_32x32x16_bf16 v[128:143], v[40:43], a[8:11], v[128:143]\n"
      "ds_read_b64_tr_b16 v[92:93], v148 offset:28672\n"
      "ds_read_b64_tr_b16 v[94:95], v148 offset:29696\n"
      "ds_read_b64_tr_b16 v[108:109], v151 offset:28672\n"
      "ds_read_b64_tr_b16 v[110:111], v151 offset:29696\n"
      "v_mfma_f32_32x32x16_bf16 v[112:127], v[28:31], a[12:15], v[112:127]\n"
      "ds_read_b64_tr_b16 a[64:65], v148 offset:32768\n"
      "ds_read_b64_tr_b16 a[66:67], v148 offset:33792\n"
      "ds_read_b64_tr_b16 a[80:81], v151 offset:32768\n"
      "ds_read_b64_tr_b16 a[82:83], v151 offset:33792\n"
      "v_mfma_f32_32x32x16_bf16 v[128:143], v[44:47], a[12:15], v[128:143]\n"
      "ds_read_b64_tr_b16 a[68:69], v148 offset:36864\n"
      "ds_read_b64_tr_b16 a[70:71], v148 offset:37888\n"
      "ds_read_b64_tr_b16 a[84:85], v151 offset:36864\n"
      "ds_read_b64_tr_b16 a[86:87], v151 offset:37888\n"
      "v_mfma_f32_32x32x16_bf16 v[112:127], v[48:51], a[16:19], v[112:127]\n"
      "ds_read_b64_tr_b16 a[72:73], v148 offset:40960\n"
      "ds_read_b64_tr_b16 a[74:75], v148 offset:41984\n"
      "ds_read_b64_tr_b16 a[88:89], v151 offset:40960\n"
      "ds_read_b64_tr_b16 a[90:91], v151 offset:41984\n"
      "v_mfma_f32_32x32x16_bf16 v[128:143], v[64:67], a[16:19], v[128:143]\n"
      "ds_read_b64_tr_b16 a[76:77], v148 offset:45056\n"
      "ds_read_b64_tr_b16 a[78:79], v148 offset:46080\n"
      "ds_read_b64_tr_b16 a[92:93], v151 offset:45056\n"
      "ds_read_b64_tr_b16 a[94:95], v151 offset:46080\n"
      "v_mfma_f32_32x32x16_bf16 v[112:127], v[52:55], a[20:23], v[112:127]\n"
      "v_mfma_f32_32x32x16_bf16 v[128:143], v[68:71], a[20:23], v[128:143]\n"
      "v_mfma_f32_32x32x16_bf16 v[112:127], v[56:59], a[24:27], v[112:127]\n"
      "v_mfma_f32_32x32x16_bf16 v[128:143], v[72:75], a[24:27], v[128:143]\n"
      "v_mfma_f32_32x32x16_bf16 v[112:127], v[60:63], a[28:31], v[112:127]\n"
      "v_mfma_f32_32x32x16_bf16 v[128:143], v[76:79], a[28:31], v[128:143]\n"
      "v_mfma_f32_16x16x32_bf16 v[2:5], v[16:19], v[156:159], 0\n"
      "v_mfma_f32_16x16x32_bf16 v[6:9], v[32:35], v[156:159], 0\n"
      "v_mfma_f32_16x16x32_bf16 v[2:5], v[20:23], v[156:159], v[2:5]\n"
      "v_mfma_f32_16x16x32_bf16 v[6:9], v[36:39], v[156:159], v[6:9]\n"
      "v_mfma_f32_16x16x32_bf16 v[2:5], v[24:27], v[156:159], v[2:5]\n"
      "v_mfma_f32_16x16x32_bf16 v[6:9], v[40:43], v[156:159], v[6:9]\n"
      "v_mfma_f32_16x16x32_bf16 v[2:5], v[28:31], v[156:159], v[2:5]\n"
      "v_mfma_f32_16x16x32_bf16 v[6:9], v[44:47], v[156:159], v[6:9]\n"
      "v_mfma_f32_16x16x32_bf16 v[2:5], v[48:51], v[156:159], v[2:5]\n"
      "v_mfma_f32_16x16x32_bf16 v[6:9], v[64:67], v[156:159], v[6:9]\n"
      "v_mfma_f32_16x16x32_bf16 v[2:5], v[52:55], v[156:159], v[2:5]\n"
      "v_mfma_f32_16x16x32_bf16 v[6:9], v[68:71], v[156:159], v[6:9]\n"
      "v_mfma_f32_16x16x32_bf16 v[2:5], v[56:59], v[156:159], v[2:5]\n"
      "v_mfma_f32_16x16x32_bf16 v[6:9], v[72:75], v[156:159], v[6:9]\n"
      "v_mfma_f32_16x16x32_bf16 v[2:5], v[60:63], v[156:159], v[2:5]\n"
      "v_mfma_f32_16x16x32_bf16 v[6:9], v[76:79], v[156:159], v[6:9]\n"
      "s_mov_b32 s82, 0x00030003\n"
      "s_mov_b32 s83, 0x00030003\n"
      "s_nop 5\n"
      "s_mov_b64 exec, s[82:83]\n"
      "ds_write_b128 v13, v[2:5]\n"
      "ds_write_b128 v13, v[6:9] offset:128\n"
      "s_mov_b64 exec, -1\n"
      "v_mov_b32 v12, 2\n"
      "ds_write_b32 v153, v12\n"
      "ds_read_b64 v[10:11], v144 offset:16\n"
      "v_add_u32 v2, 114688, v146\n"
      "v_xor_b32 v3, 32, v2\n"
      "v_xor_b32 v4, 64, v2\n"
      "v_xor_b32 v5, 96, v2\n"
      "v_xor_b32 v6, 128, v2\n"
      "v_xor_b32 v7, 160, v2\n"
      "v_xor_b32 v8, 192, v2\n"
      "v_xor_b32 v9, 224, v2\n"
      "s_waitcnt lgkmcnt(0)\n"
      "v_min_u32 v12, v10, v11\n"
      "v_cmp_gt_u32 vcc, 2, v12\n"
      "s_nop 0\n"
      "s_cbranch_vccz .Lsc2_ok\n"
      "s_nop 2\n"
      "s_mov_b32 s90, 0x100000\n"
      ".Lsc2:\n"
      "s_sleep 1\n"
      "ds_read_b64 v[10:11], v144 offset:16\n"
      "s_waitcnt lgkmcnt(0)\n"
      "v_min_u32 v12, v10, v11\n"
      "v_cmp_gt_u32 vcc, 2, v12\n"
      "s_sub_u32 s90, s90, 1\n"
      "s_nop 3\n"
      "s_cbranch_vccz .Lsc2_ok\n"
      "s_cbranch_scc0 .Lsc2\n"
      ".Lsc2_ok:\n"
      "v_mfma_f32_32x32x16_bf16 v[112:127], v[80:83], a[32:35], v[112:127]\n"
      "ds_read_b128 a[0:3], v2\n"
      "ds_read_b128 a[4:7], v3\n"
      "ds_read_b128 a[8:11], v4\n"
      "ds_read_b128 a[12:15], v5\n"
      "v_mfma_f32_32x32x16_bf16 v[128:143], v[96:99], a[32:35], v[128:143]\n"
      "ds_read_b128 a[16:19], v6\n"
      "ds_read_b128 a[20:23], v7\n"
      "ds_read_b128 a[24:27], v8\n"
      "ds_read_b128 a[28:31], v9\n"
      "v_mfma_f32_32x32x16_bf16 v[112:127], v[84:87], a[36:39], v[112:127]\n"
      "v_add_u32 v2, 16384, v146\n"
      "v_xor_b32 v3, 32, v2\n"
      "v_xor_b32 v4, 64, v2\n"
      "v_xor_b32 v5, 96, v2\n"
      "v_mfma_f32_32x32x16_bf16 v[128:143], v[100:103], a[36:39], v[128:143]\n"
      "v_xor_b32 v6, 128, v2\n"
      "v_xor_b32 v7, 160, v2\n"
      "v_xor_b32 v8, 192, v2\n"
      "v_xor_b32 v9, 224, v2\n"
      "v_mfma_f32_32x32x16_bf16 v[112:127], v[88:91], a[40:43], v[112:127]\n"
      "ds_read_b64_tr_b16 v[16:17], v149 offset:0\n"
      "ds_read_b64_tr_b16 v[18:19], v149 offset:1024\n"
      "ds_read_b64_tr_b16 v[32:33], v152 offset:0\n"
      "ds_read_b64_tr_b16 v[34:35], v152 offset:1024\n"
      "v_mfma_f32_32x32x16_bf16 v[128:143], v[104:107], a[40:43], v[128:143]\n"
      "ds_read_b64_tr_b16 v[20:21], v149 offset:4096\n"
      "ds_read_b64_tr_b16 v[22:23], v149 offset:5120\n"
      "ds_read_b64_tr_b16 v[36:37], v152 offset:4096\n"
      "ds_read_b64_tr_b16 v[38:39], v152 offset:5120\n"
      "v_mfma_f32_32x32x16_bf16 v[112:127], v[92:95], a[44:47], v[112:127]\n"
      "ds_read_b64_tr_b16 v[24:25], v149 offset:8192\n"
      "ds_read_b64_tr_b16 v[26:27], v149 offset:9216\n"
      "ds_read_b64_tr_b16 v[40:41], v152 offset:8192\n"
      "ds_read_b64_tr_b16 v[42:43], v152 offset:9216\n"
      "v_mfma_f32_32x32x16_bf16 v[128:143], v[108:111], a[44:47], v[128:143]\n"
      "ds_read_b64_tr_b16 v[28:29], v149 offset:12288\n"
      "ds_read_b64_tr_b16 v[30:31], v149 offset:13312\n"
      "ds_read_b64_tr_b16 v[44:45], v152 offset:12288\n"
      "ds_read_b64_tr_b16 v[46:47], v152 offset:13312\n"
      "v_mfma_f32_32x32x16_bf16 v[112:127], a[64:67], a[48:51], v[112:127]\n"
      "ds_read_b64_tr_b16 v[48:49], v147 offset:0\n"
      "ds_read_b64_tr_b16 v[50:51], v147 offset:1024\n"
      "ds_read_b64_tr_b16 v[64:65], v150 offset:0\n"
      "ds_read_b64_tr_b16 v[66:67], v150 offset:1024\n"
      "v_mfma_f32_32x32x16_bf16 v[128:143], a[80:83], a[48:51], v[128:143]\n"
      "ds_read_b64_tr_b16 v[52:53], v147 offset:4096\n"
      "ds_read_b64_tr_b16 v[54:55], v147 offset:5120\n"
      "ds_read_b64_tr_b16 v[68:69], v150 offset:4096\n"
      "ds_read_b64_tr_b16 v[70:71], v150 offset:5120\n"
      "v_mfma_f32_32x32x16_bf16 v[112:127], a[68:71], a[52:55], v[112:127]\n"
      "ds_read_b64_tr_b16 v[56:57], v147 offset:8192\n"
      "ds_read_b64_tr_b16 v[58:59], v147 offset:9216\n"
      "ds_read_b64_tr_b16 v[72:73], v150 offset:8192\n"
      "ds_read_b64_tr_b16 v[74:75], v150 offset:9216\n"
      "v_mfma_f32_32x32x16_bf16 v[128:143], a[84:87], a[52:55], v[128:143]\n"
      "ds_read_b64_tr_b16 v[60:61], v147 offset:12288\n"
      "ds_read_b64_tr_b16 v[62:63], v147 offset:13312\n"
      "ds_read_b64_tr_b16 v[76:77], v150 offset:12288\n"
      "ds_read_b64_tr_b16 v[78:79], v150 offset:13312\n"
      "v_mov_b32 v12, 3\n"
      "ds_write_b32 v153, v12\n"
      "v_mfma_f32_32x32x16_bf16 v[112:127], a[72:75], a[56:59], v[112:127]\n"
      "ds_read_b64 v[10:11], v144 offset:16\n"
      "v_mfma_f32_32x32x16_bf16 v[128:143], a[88:91], a[56:59], v[128:143]\n"
      "v_mfma_f32_32x32x16_bf16 v[112:127], a[76:79], a[60:63], v[112:127]\n"
      "v_mfma_f32_32x32x16_bf16 v[128:143], a[92:95], a[60:63], v[128:143]\n"
      "s_waitcnt lgkmcnt(0)\n"
      "v_min_u32 v12, v10, v11\n"
      "v_cmp_gt_u32 vcc, 3, v12\n"
      "s_nop 0\n"
      "s_cbranch_vccz .Lsc3_ok\n"
      "s_nop 6\n"
      "s_mov_b32 s90, 0x100000\n"
      ".Lsc3:\n"
      "s_sleep 1\n"
      "ds_read_b64 v[10:11], v144 offset:16\n"
      "s_waitcnt lgkmcnt(0)\n"
      "v_min_u32 v12, v10, v11\n"
      "v_cmp_gt_u32 vcc, 3, v12\n"
      "s_sub_u32 s90, s90, 1\n"
      "s_nop 3\n"
      "s_cbranch_vccz .Lsc3_ok\n"
      "s_cbranch_scc0 .Lsc3\n"
      ".Lsc3_ok:\n"
      "v_mfma_f32_32x32x16_bf16 v[112:127], v[16:19], a[0:3], v[112:127]\n"
      "ds_read_b128 a[32:35], v2\n"
      "ds_read_b128 a[36:39], v3\n"
      "ds_read_b128 a[40:43], v4\n"
      "ds_read_b128 a[44:47], v5\n"
      "v_mfma_f32_32x32x16_bf16 v[128:143], v[32:35], a[0:3], v[128:143]\n"
      "ds_read_b128 a[48:51], v6\n"
      "ds_read_b128 a[52:55], v7\n"
      "ds_read_b128 a[56:59], v8\n"
      "ds_read_b128 a[60:63], v9\n"
      "v_mfma_f32_32x32x16_bf16 v[112:127], v[20:23], a[4:7], v[112:127]\n"
      "v_add_u32 v2, 65536, v146\n"
      "v_xor_b32 v3, 32, v2\n"
      "v_xor_b32 v4, 64, v2\n"
      "v_xor_b32 v5, 96, v2\n"
      "v_mfma_f32_32x32x16_bf16 v[128:143], v[36:39], a[4:7], v[128:143]\n"
      "v_xor_b32 v6, 128, v2\n"
      "v_xor_b32 v7, 160, v2\n"
      "v_xor_b32 v8, 192, v2\n"
      "v_xor_b32 v9, 224, v2\n"
      "v_mfma_f32_32x32x16_bf16 v[112:127], v[24:27], a[8:11], v[112:127]\n"
      "ds_read_b64_tr_b16 v[80:81], v147 offset:32768\n"
      "ds_read_b64_tr_b16 v[82:83], v147 offset:33792\n"
      "ds_read_b64_tr_b16 v[96:97], v150 offset:32768\n"
      "ds_read_b64_tr_b16 v[98:99], v150 offset:33792\n"
      "v_mfma_f32_32x32x16_bf16 v[128:143], v[40:43], a[8:11], v[128:143]\n"
      "ds_read_b64_tr_b16 v[84:85], v147 offset:36864\n"
      "ds_read_b64_tr_b16 v[86:87], v147 offset:37888\n"
      "ds_read_b64_tr_b16 v[100:101], v150 offset:36864\n"
      "ds_read_b64_tr_b16 v[102:103], v150 offset:37888\n"
      "v_mfma_f32_32x32x16_bf16 v[112:127], v[28:31], a[12:15], v[112:127]\n"
      "ds_read_b64_tr_b16 v[88:89], v147 offset:40960\n"
      "ds_read_b64_tr_b16 v[90:91], v147 offset:41984\n"
      "ds_read_b64_tr_b16 v[104:105], v150 offset:40960\n"
      "ds_read_b64_tr_b16 v[106:107], v150 offset:41984\n"
      "v_mfma_f32_32x32x16_bf16 v[128:143], v[44:47], a[12:15], v[128:143]\n"
      "ds_read_b64_tr_b16 v[92:93], v147 offset:45056\n"
      "ds_read_b64_tr_b16 v[94:95], v147 offset:46080\n"
      "ds_read_b64_tr_b16 v[108:109], v150 offset:45056\n"
      "ds_read_b64_tr_b16 v[110:111], v150 offset:46080\n"
      "v_mfma_f32_32x32x16_bf16 v[112:127], v[48:51], a[16:19], v[112:127]\n"
      "ds_read_b64_tr_b16 a[64:65], v147 offset:49152\n"
      "ds_read_b64_tr_b16 a[66:67], v147 offset:50176\n"
      "ds_read_b64_tr_b16 a[80:81], v150 offset:49152\n"
      "ds_read_b64_tr_b16 a[82:83], v150 offset:50176\n"
      "v_mfma_f32_32x32x16_bf16 v[128:143], v[64:67], a[16:19], v[128:143]\n"
      "ds_read_b64_tr_b16 a[68:69], v147 offset:53248\n"
      "ds_read_b64_tr_b16 a[70:71], v147 offset:54272\n"
      "ds_read_b64_tr_b16 a[84:85], v150 offset:53248\n"
      "ds_read_b64_tr_b16 a[86:87], v150 offset:54272\n"
      "v_mfma_f32_32x32x16_bf16 v[112:127], v[52:55], a[20:23], v[112:127]\n"
      "ds_read_b64_tr_b16 a[72:73], v147 offset:57344\n"
      "ds_read_b64_tr_b16 a[74:75], v147 offset:58368\n"
      "ds_read_b64_tr_b16 a[88:89], v150 offset:57344\n"
      "ds_read_b64_tr_b16 a[90:91], v150 offset:58368\n"
      "v_mfma_f32_32x32x16_bf16 v[128:143], v[68:71], a[20:23], v[128:143]\n"
      "ds_read_b64_tr_b16 a[76:77], v147 offset:61440\n"
      "ds_read_b64_tr_b16 a[78:79], v147 offset:62464\n"
      "ds_read_b64_tr_b16 a[92:93], v150 offset:61440\n"
      "ds_read_b64_tr_b16 a[94:95], v150 offset:62464\n"
      "v_mov_b32 v12, 4\n"
      "ds_write_b32 v153, v12\n"
      "v_mfma_f32_32x32x16_bf16 v[112:127], v[56:59], a[24:27], v[112:127]\n"
      "ds_read_b64 v[10:11], v144 offset:16\n"
      "v_mfma_f32_32x32x16_bf16 v[128:143], v[72:75], a[24:27], v[128:143]\n"
      "v_mfma_f32_32x32x16_bf16 v[112:127], v[60:63], a[28:31], v[112:127]\n"
      "v_mfma_f32_32x32x16_bf16 v[128:143], v[76:79], a[28:31], v[128:143]\n"
      "s_waitcnt lgkmcnt(0)\n"
      "v_min_u32 v12, v10, v11\n"
      "v_cmp_gt_u32 vcc, 4, v12\n"
      "s_nop 0\n"
      "s_cbranch_vccz .Lsc4_ok\n"
      "s_nop 6\n"
      "s_mov_b32 s90, 0x100000\n"
      ".Lsc4:\n"
      "s_sleep 1\n"
      "ds_read_b64 v[10:11], v144 offset:16\n"
      "s_waitcnt lgkmcnt(0)\n"
      "v_min_u32 v12, v10, v11\n"
      "v_cmp_gt_u32 vcc, 4, v12\n"
      "s_sub_u32 s90, s90, 1\n"
      "s_nop 3\n"
      "s_cbranch_vccz .Lsc4_ok\n"
      "s_cbranch_scc0 .Lsc4\n"
      ".Lsc4_ok:\n"
      "v_mfma_f32_32x32x16_bf16 v[112:127], v[80:83], a[32:35], v[112:127]\n"
      "ds_read_b128 a[0:3], v2\n"
      "ds_read_b128 a[4:7], v3\n"
      "ds_read_b128 a[8:11], v4\n"
      "ds_read_b128 a[12:15], v5\n"
      "v_mfma_f32_32x32x16_bf16 v[128:143], v[96:99], a[32:35], v[128:143]\n"
      "ds_read_b128 a[16:19], v6\n"
      "ds_read_b128 a[20:23], v7\n"
      "ds_read_b128 a[24:27], v8\n"
      "ds_read_b128 a[28:31], v9\n"
      "v_mfma_f32_32x32x16_bf16 v[112:127], v[84:87], a[36:39], v[112:127]\n"
      "v_add_u32 v2, 114688, v146\n"
      "v_xor_b32 v3, 32, v2\n"
      "v_xor_b32 v4, 64, v2\n"
      "v_xor_b32 v5, 96, v2\n"
      "v_mfma_f32_32x32x16_bf16 v[128:143], v[100:103], a[36:39], v[128:143]\n"
      "v_xor_b32 v6, 128, v2\n"
      "v_xor_b32 v7, 160, v2\n"
      "v_xor_b32 v8, 192, v2\n"
      "v_xor_b32 v9, 224, v2\n"
      "v_mfma_f32_32x32x16_bf16 v[112:127], v[88:91], a[40:43], v[112:127]\n"
      "ds_read_b64_tr_b16 v[16:17], v148 offset:16384\n"
      "ds_read_b64_tr_b16 v[18:19], v148 offset:17408\n"
      "ds_read_b64_tr_b16 v[32:33], v151 offset:16384\n"
      "ds_read_b64_tr_b16 v[34:35], v151 offset:17408\n"
      "v_mfma_f32_32x32x16_bf16 v[128:143], v[104:107], a[40:43], v[128:143]\n"
      "ds_read_b64_tr_b16 v[20:21], v148 offset:20480\n"
      "ds_read_b64_tr_b16 v[22:23], v148 offset:21504\n"
      "ds_read_b64_tr_b16 v[36:37], v151 offset:20480\n"
      "ds_read_b64_tr_b16 v[38:39], v151 offset:21504\n"
      "v_mfma_f32_32x32x16_bf16 v[112:127], v[92:95], a[44:47], v[112:127]\n"
      "ds_read_b64_tr_b16 v[24:25], v148 offset:24576\n"
      "ds_read_b64_tr_b16 v[26:27], v148 offset:25600\n"
      "ds_read_b64_tr_b16 v[40:41], v151 offset:24576\n"
      "ds_read_b64_tr_b16 v[42:43], v151 offset:25600\n"
      "v_mfma_f32_32x32x16_bf16 v[128:143], v[108:111], a[44:47], v[128:143]\n"
      "ds_read_b64_tr_b16 v[28:29], v148 offset:28672\n"
      "ds_read_b64_tr_b16 v[30:31], v148 offset:29696\n"
      "ds_read_b64_tr_b16 v[44:45], v151 offset:28672\n"
      "ds_read_b64_tr_b16 v[46:47], v151 offset:29696\n"
      "v_mfma_f32_32x32x16_bf16 v[112:127], a[64:67], a[48:51], v[112:127]\n"
      "ds_read_b64_tr_b16 v[48:49], v148 offset:32768\n"
      "ds_read_b64_tr_b16 v[50:51], v148 offset:33792\n"
      "ds_read_b64_tr_b16 v[64:65], v151 offset:32768\n"
      "ds_read_b64_tr_b16 v[66:67], v151 offset:33792\n"
      "v_mfma_f32_32x32x16_bf16 v[128:143], a[80:83], a[48:51], v[128:143]\n"
      "ds_read_b64_tr_b16 v[52:53], v148 offset:36864\n"
      "ds_read_b64_tr_b16 v[54:55], v148 offset:37888\n"
      "ds_read_b64_tr_b16 v[68:69], v151 offset:36864\n"
      "ds_read_b64_tr_b16 v[70:71], v151 offset:37888\n"
      "v_mfma_f32_32x32x16_bf16 v[112:127], a[68:71], a[52:55], v[112:127]\n"
      "ds_read_b64_tr_b16 v[56:57], v148 offset:40960\n"
      "ds_read_b64_tr_b16 v[58:59], v148 offset:41984\n"
      "ds_read_b64_tr_b16 v[72:73], v151 offset:40960\n"
      "ds_read_b64_tr_b16 v[74:75], v151 offset:41984\n"
      "v_mfma_f32_32x32x16_bf16 v[128:143], a[84:87], a[52:55], v[128:143]\n"
      "ds_read_b64_tr_b16 v[60:61], v148 offset:45056\n"
      "ds_read_b64_tr_b16 v[62:63], v148 offset:46080\n"
      "ds_read_b64_tr_b16 v[76:77], v151 offset:45056\n"
      "ds_read_b64_tr_b16 v[78:79], v151 offset:46080\n"
      "v_mov_b32 v12, 5\n"
      "ds_write_b32 v153, v12\n"
      "v_mfma_f32_32x32x16_bf16 v[112:127], a[72:75], a[56:59], v[112:127]\n"
      "ds_read_b64 v[10:11], v144 offset:16\n"
      "v_mfma_f32_32x32x16_bf16 v[128:143], a[88:91], a[56:59], v[128:143]\n"
      "v_mfma_f32_32x32x16_bf16 v[112:127], a[76:79], a[60:63], v[112:127]\n"
      "v_mfma_f32_32x32x16_bf16 v[128:143], a[92:95], a[60:63], v[128:143]\n"
      "s_waitcnt lgkmcnt(0)\n"
      "v_min_u32 v12, v10, v11\n"
      "v_cmp_gt_u32 vcc, 5, v12\n"
      "s_nop 0\n"
      "s_cbranch_vccz .Lsc5_ok\n"
      "s_nop 6\n"
      "s_mov_b32 s90, 0x100000\n"
      ".Lsc5:\n"
      "s_sleep 1\n"
      "ds_read_b64 v[10:11], v144 offset:16\n"
      "s_waitcnt lgkmcnt(0)\n"
      "v_min_u32 v12, v10, v11\n"
      "v_cmp_gt_u32 vcc, 5, v12\n"
      "s_sub_u32 s90, s90, 1\n"
      "s_nop 3\n"
      "s_cbranch_vccz .Lsc5_ok\n"
      "s_cbranch_scc0 .Lsc5\n"
      ".Lsc5_ok:\n"
      "v_mfma_f32_32x32x16_bf16 v[112:127], v[16:19], a[0:3], v[112:127]\n"
      "ds_read_b128 a[32:35], v2\n"
      "ds_read_b128 a[36:39], v3\n"
      "ds_read_b128 a[40:43], v4\n"
      "ds_read_b128 a[44:47], v5\n"
      "v_mfma_f32_32x32x16_bf16 v[128:143], v[32:35], a[0:3], v[128:143]\n"
      "ds_read_b128 a[48:51], v6\n"
      "ds_read_b128 a[52:55], v7\n"
      "ds_read_b128 a[56:59], v8\n"
      "ds_read_b128 a[60:63], v9\n"
      "v_mfma_f32_32x32x16_bf16 v[112:127], v[20:23], a[4:7], v[112:127]\n"
      "v_add_u32 v2, 16384, v146\n"
      "v_xor_b32 v3, 32, v2\n"
      "v_xor_b32 v4, 64, v2\n"
      "v_xor_b32 v5, 96, v2\n"
      "v_mfma_f32_32x32x16_bf16 v[128:143], v[36:39], a[4:7], v[128:143]\n"
      "v_xor_b32 v6, 128, v2\n"
      "v_xor_b32 v7, 160, v2\n"
      "v_xor_b32 v8, 192, v2\n"
      "v_xor_b32 v9, 224, v2\n"
      "v_mfma_f32_32x32x16_bf16 v[112:127], v[24:27], a[8:11], v[112:127]\n"
      "ds_read_b64_tr_b16 v[80:81], v149 offset:0\n"
      "ds_read_b64_tr_b16 v[82:83], v149 offset:1024\n"
      "ds_read_b64_tr_b16 v[96:97], v152 offset:0\n"
      "ds_read_b64_tr_b16 v[98:99], v152 offset:1024\n"
      "v_mfma_f32_32x32x16_bf16 v[128:143], v[40:43], a[8:11], v[128:143]\n"
      "ds_read_b64_tr_b16 v[84:85], v149 offset:4096\n"
      "ds_read_b64_tr_b16 v[86:87], v149 offset:5120\n"
      "ds_read_b64_tr_b16 v[100:101], v152 offset:4096\n"
      "ds_read_b64_tr_b16 v[102:103], v152 offset:5120\n"
      "v_mfma_f32_32x32x16_bf16 v[112:127], v[28:31], a[12:15], v[112:127]\n"
      "ds_read_b64_tr_b16 v[88:89], v149 offset:8192\n"
      "ds_read_b64_tr_b16 v[90:91], v149 offset:9216\n"
      "ds_read_b64_tr_b16 v[104:105], v152 offset:8192\n"
      "ds_read_b64_tr_b16 v[106:107], v152 offset:9216\n"
      "v_mfma_f32_32x32x16_bf16 v[128:143], v[44:47], a[12:15], v[128:143]\n"
      "ds_read_b64_tr_b16 v[92:93], v149 offset:12288\n"
      "ds_read_b64_tr_b16 v[94:95], v149 offset:13312\n"
      "ds_read_b64_tr_b16 v[108:109], v152 offset:12288\n"
      "ds_read_b64_tr_b16 v[110:111], v152 offset:13312\n"
      "v_mfma_f32_32x32x16_bf16 v[112:127], v[48:51], a[16:19], v[112:127]\n"
      "ds_read_b64_tr_b16 a[64:65], v147 offset:0\n"
      "ds_read_b64_tr_b16 a[66:67], v147 offset:1024\n"
      "ds_read_b64_tr_b16 a[80:81], v150 offset:0\n"
      "ds_read_b64_tr_b16 a[82:83], v150 offset:1024\n"
      "v_mfma_f32_32x32x16_bf16 v[128:143], v[64:67], a[16:19], v[128:143]\n"
      "ds_read_b64_tr_b16 a[68:69], v147 offset:4096\n"
      "ds_read_b64_tr_b16 a[70:71], v147 offset:5120\n"
      "ds_read_b64_tr_b16 a[84:85], v150 offset:4096\n"
      "ds_read_b64_tr_b16 a[86:87], v150 offset:5120\n"
      "v_mfma_f32_32x32x16_bf16 v[112:127], v[52:55], a[20:23], v[112:127]\n"
      "ds_read_b64_tr_b16 a[72:73], v147 offset:8192\n"
      "ds_read_b64_tr_b16 a[74:75], v147 offset:9216\n"
      "ds_read_b64_tr_b16 a[88:89], v150 offset:8192\n"
      "ds_read_b64_tr_b16 a[90:91], v150 offset:9216\n"
      "v_mfma_f32_32x32x16_bf16 v[128:143], v[68:71], a[20:23], v[128:143]\n"
      "ds_read_b64_tr_b16 a[76:77], v147 offset:12288\n"
      "ds_read_b64_tr_b16 a[78:79], v147 offset:13312\n"
      "ds_read_b64_tr_b16 a[92:93], v150 offset:12288\n"
      "ds_read_b64_tr_b16 a[94:95], v150 offset:13312\n"
      "v_mov_b32 v12, 6\n"
      "ds_write_b32 v153, v12\n"
      "v_mfma_f32_32x32x16_bf16 v[112:127], v[56:59], a[24:27], v[112:127]\n"
      "ds_read_b64 v[10:11], v144 offset:16\n"
      "v_mfma_f32_32x32x16_bf16 v[128:143], v[72:75], a[24:27], v[128:143]\n"
      "v_mfma_f32_32x32x16_bf16 v[112:127], v[60:63], a[28:31], v[112:127]\n"
      "v_mfma_f32_32x32x16_bf16 v[128:143], v[76:79], a[28:31], v[128:143]\n"
      "s_waitcnt lgkmcnt(0)\n"
      "v_min_u32 v12, v10, v11\n"
      "v_cmp_gt_u32 vcc, 6, v12\n"
      "s_nop 0\n"
      "s_cbranch_vccz .Lsc6_ok\n"
      "s_nop 6\n"
      "s_mov_b32 s90, 0x100000\n"
      ".Lsc6:\n"
      "s_sleep 1\n"
      "ds_read_b64 v[10:11], v144 offset:16\n"
      "s_waitcnt lgkmcnt(0)\n"
      "v_min_u32 v12, v10, v11\n"
      "v_cmp_gt_u32 vcc, 6, v12\n"
      "s_sub_u32 s90, s90, 1\n"
      "s_nop 3\n"
      "s_cbranch_vccz .Lsc6_ok\n"
      "s_cbranch_scc0 .Lsc6\n"
      ".Lsc6_ok:\n"
      "v_mfma_f32_32x32x16_bf16 v[112:127], v[80:83], a[32:35], v[112:127]\n"
      "ds_read_b128 a[0:3], v2\n"
      "ds_read_b128 a[4:7], v3\n"
      "ds_read_b128 a[8:11], v4\n"
      "ds_read_b128 a[12:15], v5\n"
      "v_mfma_f32_32x32x16_bf16 v[128:143], v[96:99], a[32:35], v[128:143]\n"
      "ds_read_b128 a[16:19], v6\n"
      "ds_read_b128 a[20:23], v7\n"
      "ds_read_b128 a[24:27], v8\n"
      "ds_read_b128 a[28:31], v9\n"
      "v_mfma_f32_32x32x16_bf16 v[112:127], v[84:87], a[36:39], v[112:127]\n"
      "v_add_u32 v2, 65536, v146\n"
      "v_xor_b32 v3, 32, v2\n"
      "v_xor_b32 v4, 64, v2\n"
      "v_xor_b32 v5, 96, v2\n"
      "v_mfma_f32_32x32x16_bf16 v[128:143], v[100:103], a[36:39], v[128:143]\n"
      "v_xor_b32 v6, 128, v2\n"
      "v_xor_b32 v7, 160, v2\n"
      "v_xor_b32 v8, 192, v2\n"
      "v_xor_b32 v9, 224, v2\n"
      "v_mfma_f32_32x32x16_bf16 v[112:127], v[88:91], a[40:43], v[112:127]\n"
      "ds_read_b64_tr_b16 v[16:17], v147 offset:32768\n"
      "ds_read_b64_tr_b16 v[18:19], v147 offset:33792\n"
      "ds_read_b64_tr_b16 v[32:33], v150 offset:32768\n"
      "ds_read_b64_tr_b16 v[34:35], v150 offset:33792\n"
      "v_mfma_f32_32x32x16_bf16 v[128:143], v[104:107], a[40:43], v[128:143]\n"
      "ds_read_b64_tr_b16 v[20:21], v147 offset:36864\n"
      "ds_read_b64_tr_b16 v[22:23], v147 offset:37888\n"
      "ds_read_b64_tr_b16 v[36:37], v150 offset:36864\n"
      "ds_read_b64_tr_b16 v[38:39], v150 offset:37888\n"
      "v_mfma_f32_32x32x16_bf16 v[112:127], v[92:95], a[44:47], v[112:127]\n"
      "ds_read_b64_tr_b16 v[24:25], v147 offset:40960\n"
      "ds_read_b64_tr_b16 v[26:27], v147 offset:41984\n"
      "ds_read_b64_tr_b16 v[40:41], v150 offset:40960\n"
      "ds_read_b64_tr_b16 v[42:43], v150 offset:41984\n"
      "v_mfma_f32_32x32x16_bf16 v[128:143], v[108:111], a[44:47], v[128:143]\n"
      "ds_read_b64_tr_b16 v[28:29], v147 offset:45056\n"
      "ds_read_b64_tr_b16 v[30:31], v147 offset:46080\n"
      "ds_read_b64_tr_b16 v[44:45], v150 offset:45056\n"
      "ds_read_b64_tr_b16 v[46:47], v150 offset:46080\n"
      "v_mfma_f32_32x32x16_bf16 v[112:127], a[64:67], a[48:51], v[112:127]\n"
      "ds_read_b64_tr_b16 v[48:49], v147 offset:49152\n"
      "ds_read_b64_tr_b16 v[50:51], v147 offset:50176\n"
      "ds_read_b64_tr_b16 v[64:65], v150 offset:49152\n"
      "ds_read_b64_tr_b16 v[66:67], v150 offset:50176\n"
      "v_mfma_f32_32x32x16_bf16 v[128:143], a[80:83], a[48:51], v[128:143]\n"
      "ds_read_b64_tr_b16 v[52:53], v147 offset:53248\n"
      "ds_read_b64_tr_b16 v[54:55], v147 offset:54272\n"
      "ds_read_b64_tr_b16 v[68:69], v150 offset:53248\n"
      "ds_read_b64_tr_b16 v[70:71], v150 offset:54272\n"
      "v_mfma_f32_32x32x16_bf16 v[112:127], a[68:71], a[52:55], v[112:127]\n"
      "ds_read_b64_tr_b16 v[56:57], v147 offset:57344\n"
      "ds_read_b64_tr_b16 v[58:59], v147 offset:58368\n"
      "ds_read_b64_tr_b16 v[72:73], v150 offset:57344\n"
      "ds_read_b64_tr_b16 v[74:75], v150 offset:58368\n"
      "v_mfma_f32_32x32x16_bf16 v[128:143], a[84:87], a[52:55], v[128:143]\n"
      "ds_read_b64_tr_b16 v[60:61], v147 offset:61440\n"
      "ds_read_b64_tr_b16 v[62:63], v147 offset:62464\n"
      "ds_read_b64_tr_b16 v[76:77], v150 offset:61440\n"
      "ds_read_b64_tr_b16 v[78:79], v150 offset:62464\n"
      "v_mov_b32 v12, 7\n"
      "ds_write_b32 v153, v12\n"
      "v_mfma_f32_32x32x16_bf16 v[112:127], a[72:75], a[56:59], v[112:127]\n"
      "ds_read_b64 v[10:11], v144 offset:16\n"
      "v_mfma_f32_32x32x16_bf16 v[128:143], a[88:91], a[56:59], v[128:143]\n"
      "v_mfma_f32_32x32x16_bf16 v[112:127], a[76:79], a[60:63], v[112:127]\n"
      "v_mfma_f32_32x32x16_bf16 v[128:143], a[92:95], a[60:63], v[128:143]\n"
      "s_waitcnt lgkmcnt(0)\n"
      "v_min_u32 v12, v10, v11\n"
      "v_cmp_gt_u32 vcc, 7, v12\n"
      "s_nop 0\n"
      "s_cbranch_vccz .Lsc7_ok\n"
      "s_nop 6\n"
      "s_mov_b32 s90, 0x100000\n"
      ".Lsc7:\n"
      "s_sleep 1\n"
      "ds_read_b64 v[10:11], v144 offset:16\n"
      "s_waitcnt lgkmcnt(0)\n"
      "v_min_u32 v12, v10, v11\n"
      "v_cmp_gt_u32 vcc, 7, v12\n"
      "s_sub_u32 s90, s90, 1\n"
      "s_nop 3\n"
      "s_cbranch_vccz .Lsc7_ok\n"
      "s_cbranch_scc0 .Lsc7\n"
      ".Lsc7_ok:\n"
      "v_mfma_f32_32x32x16_bf16 v[112:127], v[16:19], a[0:3], v[112:127]\n"
      "ds_read_b128 a[32:35], v2\n"
      "ds_read_b128 a[36:39], v3\n"
      "ds_read_b128 a[40:43], v4\n"
      "ds_read_b128 a[44:47], v5\n"
      "v_mfma_f32_32x32x16_bf16 v[128:143], v[32:35], a[0:3], v[128:143]\n"
      "ds_read_b128 a[48:51], v6\n"
      "ds_read_b128 a[52:55], v7\n"
      "ds_read_b128 a[56:59], v8\n"
      "ds_read_b128 a[60:63], v9\n"
      "v_mfma_f32_32x32x16_bf16 v[112:127], v[20:23], a[4:7], v[112:127]\n"
      "ds_read_b64_tr_b16 v[80:81], v148 offset:16384\n"
      "ds_read_b64_tr_b16 v[82:83], v148 offset:17408\n"
      "ds_read_b64_tr_b16 v[96:97], v151 offset:16384\n"
      "ds_read_b64_tr_b16 v[98:99], v151 offset:17408\n"
      "v_mfma_f32_32x32x16_bf16 v[128:143], v[36:39], a[4:7], v[128:143]\n"
      "ds_read_b64_tr_b16 v[84:85], v148 offset:20480\n"
      "ds_read_b64_tr_b16 v[86:87], v148 offset:21504\n"
      "ds_read_b64_tr_b16 v[100:101], v151 offset:20480\n"
      "ds_read_b64_tr_b16 v[102:103], v151 offset:21504\n"
      "v_mfma_f32_32x32x16_bf16 v[112:127], v[24:27], a[8:11], v[112:127]\n"
      "ds_read_b64_tr_b16 v[88:89], v148 offset:24576\n"
      "ds_read_b64_tr_b16 v[90:91], v148 offset:25600\n"
      "ds_read_b64_tr_b16 v[104:105], v151 offset:24576\n"
      "ds_read_b64_tr_b16 v[106:107], v151 offset:25600\n"
      "v_mfma_f32_32x32x16_bf16 v[128:143], v[40:43], a[8:11], v[128:143]\n"
      "ds_read_b64_tr_b16 v[92:93], v148 offset:28672\n"
      "ds_read_b64_tr_b16 v[94:95], v148 offset:29696\n"
      "ds_read_b64_tr_b16 v[108:109], v151 offset:28672\n"
      "ds_read_b64_tr_b16 v[110:111], v151 offset:29696\n"
      "v_mfma_f32_32x32x16_bf16 v[112:127], v[28:31], a[12:15], v[112:127]\n"
      "ds_read_b64_tr_b16 a[64:65], v148 offset:32768\n"
      "ds_read_b64_tr_b16 a[66:67], v148 offset:33792\n"
      "ds_read_b64_tr_b16 a[80:81], v151 offset:32768\n"
      "ds_read_b64_tr_b16 a[82:83], v151 offset:33792\n"
      "v_mfma_f32_32x32x16_bf16 v[128:143], v[44:47], a[12:15], v[128:143]\n"
      "ds_read_b64_tr_b16 a[68:69], v148 offset:36864\n"
      "ds_read_b64_tr_b16 a[70:71], v148 offset:37888\n"
      "ds_read_b64_tr_b16 a[84:85], v151 offset:36864\n"
      "ds_read_b64_tr_b16 a[86:87], v151 offset:37888\n"
      "v_mfma_f32_32x32x16_bf16 v[112:127], v[48:51], a[16:19], v[112:127]\n"
      "ds_read_b64_tr_b16 a[72:73], v148 offset:40960\n"
      "ds_read_b64_tr_b16 a[74:75], v148 offset:41984\n"
      "ds_read_b64_tr_b16 a[88:89], v151 offset:40960\n"
      "ds_read_b64_tr_b16 a[90:91], v151 offset:41984\n"
      "v_mfma_f32_32x32x16_bf16 v[128:143], v[64:67], a[16:19], v[128:143]\n"
      "ds_read_b64_tr_b16 a[76:77], v148 offset:45056\n"
      "ds_read_b64_tr_b16 a[78:79], v148 offset:46080\n"
      "ds_read_b64_tr_b16 a[92:93], v151 offset:45056\n"
      "ds_read_b64_tr_b16 a[94:95], v151 offset:46080\n"
      "v_mov_b32 v12, 8\n"
      "ds_write_b32 v153, v12\n"
      "v_mfma_f32_32x32x16_bf16 v[112:127], v[52:55], a[20:23], v[112:127]\n"
      "v_mfma_f32_32x32x16_bf16 v[128:143], v[68:71], a[20:23], v[128:143]\n"
      "v_mfma_f32_32x32x16_bf16 v[112:127], v[56:59], a[24:27], v[112:127]\n"
      "v_mfma_f32_32x32x16_bf16 v[128:143], v[72:75], a[24:27], v[128:143]\n"
      "v_mfma_f32_32x32x16_bf16 v[112:127], v[60:63], a[28:31], v[112:127]\n"
      "v_mfma_f32_32x32x16_bf16 v[128:143], v[76:79], a[28:31], v[128:143]\n"
      "s_waitcnt lgkmcnt(0)\n"
      "ds_read_b64 v[10:11], v144 offset:24\n"
      "s_waitcnt lgkmcnt(0)\n"
      "v_min_u32 v12, v10, v11\n"
      "v_cmp_gt_u32 vcc, 1, v12\n"
      "s_nop 0\n"
      "s_cbranch_vccz .Lst_ok\n"
      "s_nop 4\n"
      "s_mov_b32 s90, 0x100000\n"
      ".Lst:\n"
      "s_sleep 2\n"
      "ds_read_b64 v[10:11], v144 offset:24\n"
      "s_waitcnt lgkmcnt(0)\n"
      "v_min_u32 v12, v10, v11\n"
      "v_cmp_gt_u32 vcc, 1, v12\n"
      "s_sub_u32 s90, s90, 1\n"
      "s_nop 3\n"
      "s_cbranch_vccz .Lst_ok\n"
      "s_cbranch_scc0 .Lst\n"
      ".Lst_ok:\n"
      "v_mfma_f32_32x32x16_bf16 v[112:127], v[80:83], a[32:35], v[112:127]\n"
      "ds_read_b64 v[156:157], v154\n"
      "ds_read_b128 v[16:19], v155 offset:0\n"
      "ds_read_b128 v[20:23], v155 offset:32\n"
      "ds_read_b128 v[24:27], v155 offset:64\n"
      "v_mfma_f32_32x32x16_bf16 v[128:143], v[96:99], a[32:35], v[128:143]\n"
      "ds_read_b128 v[28:31], v155 offset:96\n"
      "ds_read_b128 v[32:35], v155 offset:128\n"
      "ds_read_b128 v[36:39], v155 offset:160\n"
      "ds_read_b128 v[40:43], v155 offset:192\n"
      "v_mfma_f32_32x32x16_bf16 v[112:127], v[84:87], a[36:39], v[112:127]\n"
      "ds_read_b128 v[44:47], v155 offset:224\n"
      "v_and_b32 v2, 7, v1\n"
      "v_cmp_eq_u32 vcc, 0, v2\n"
      "v_mov_b32 v3, 0x3f803f80\n"
      "v_mfma_f32_32x32x16_bf16 v[128:143], v[100:103], a[36:39], v[128:143]\n"
      "v_mov_b32 v6, 0\n"
      "v_cndmask_b32 v4, 0, v3, vcc\n"
      "v_cmp_eq_u32 vcc, 4, v2\n"
      "v_mov_b32 v7, 0\n"
      "v_mfma_f32_32x32x16_bf16 v[112:127], v[88:91], a[40:43], v[112:127]\n"
      "v_mov_b32 v8, 0\n"
      "v_cndmask_b32 v5, 0, v3, vcc\n"
      "v_mov_b32 v9, 0\n"
      "v_lshrrev_b32 v12, 2, v1\n"
      "v_mfma_f32_32x32x16_bf16 v[128:143], v[104:107], a[40:43], v[128:143]\n"
      "v_lshlrev_b32 v12, 11, v12\n"
      "v_and_b32 v13, 3, v1\n"
      "v_lshl_add_u32 v153, v13, 4, v12\n"
      "v_mov_b32 v144, 0xdddd\n"
      "v_mfma_f32_32x32x16_bf16 v[112:127], v[92:95], a[44:47], v[112:127]\n"
      "s_waitcnt lgkmcnt(0)\n"
      "v_mul_f32 v2, 0x3e8293ee, v157\n"
      "v_mul_f32_e64 v156, -v156, v2\n"
      "v_mov_b32 v3, v2\n"
      "v_mfma_f32_32x32x16_bf16 v[128:143], v[108:111], a[44:47], v[128:143]\n"
      "v_mul_f32 v16, v16, v156\n"
      "v_mul_f32 v17, v17, v156\n"
      "v_mul_f32 v18, v18, v156\n"
      "v_mul_f32 v19, v19, v156\n"
      "v_mfma_f32_32x32x16_bf16 v[112:127], a[64:67], a[48:51], v[112:127]\n"
      "v_mul_f32 v20, v20, v156\n"
      "v_mul_f32 v21, v21, v156\n"
      "v_mul_f32 v22, v22, v156\n"
      "v_mul_f32 v23, v23, v156\n"
      "v_mfma_f32_32x32x16_bf16 v[128:143], a[80:83], a[48:51], v[128:143]\n"
      "v_mul_f32 v24, v24, v156\n"
      "v_mul_f32 v25, v25, v156\n"
      "v_mul_f32 v26, v26, v156\n"
      "v_mul_f32 v27, v27, v156\n"
      "v_mfma_f32_32x32x16_bf16 v[112:127], a[68:71], a[52:55], v[112:127]\n"
      "v_mul_f32 v28, v28, v156\n"
      "v_mul_f32 v29, v29, v156\n"
      "v_mul_f32 v30, v30, v156\n"
      "v_mul_f32 v31, v31, v156\n"
      "v_mfma_f32_32x32x16_bf16 v[128:143], a[84:87], a[52:55], v[128:143]\n"
      "v_mul_f32 v32, v32, v156\n"
      "v_mul_f32 v33, v33, v156\n"
      "v_mul_f32 v34, v34, v156\n"
      "v_mul_f32 v35, v35, v156\n"
      "v_mfma_f32_32x32x16_bf16 v[112:127], a[72:75], a[56:59], v[112:127]\n"
      "v_mul_f32 v36, v36, v156\n"
      "v_mul_f32 v37, v37, v156\n"
      "v_mul_f32 v38, v38, v156\n"
      "v_mul_f32 v39, v39, v156\n"
      "v_mfma_f32_32x32x16_bf16 v[128:143], a[88:91], a[56:59], v[128:143]\n"
      "v_mul_f32 v40, v40, v156\n"
      "v_mul_f32 v41, v41, v156\n"
      "v_mul_f32 v42, v42, v156\n"
      "v_mul_f32 v43, v43, v156\n"
      "v_mfma_f32_32x32x16_bf16 v[112:127], a[76:79], a[60:63], v[112:127]\n"
      "v_mul_f32 v44, v44, v156\n"
      "v_mul_f32 v45, v45, v156\n"
      "v_mul_f32 v46, v46, v156\n"
      "v_mul_f32 v47, v47, v156\n"
      "v_mfma_f32_32x32x16_bf16 v[128:143], a[92:95], a[60:63], v[128:143]\n"
      "s_nop 6\n"
      "v_pk_fma_f32 v[112:113], v[112:113], v[2:3], v[16:17]\n"
      "v_pk_fma_f32 v[114:115], v[114:115], v[2:3], v[18:19]\n"
      "v_pk_fma_f32 v[116:117], v[116:117], v[2:3], v[20:21]\n"
      "v_pk_fma_f32 v[118:119], v[118:119], v[2:3], v[22:23]\n"
      "v_pk_fma_f32 v[120:121], v[120:121], v[2:3], v[24:25]\n"
      "v_pk_fma_f32 v[122:123], v[122:123], v[2:3], v[26:27]\n"
      "v_pk_fma_f32 v[124:125], v[124:125], v[2:3], v[28:29]\n"
      "v_pk_fma_f32 v[126:127], v[126:127], v[2:3], v[30:31]\n"
      "v_cvt_pk_bf16_f32 v48, v112, v113\n"
      "v_cvt_pk_bf16_f32 v49, v114, v115\n"
      "v_cvt_pk_bf16_f32 v50, v116, v117\n"
      "v_cvt_pk_bf16_f32 v51, v118, v119\n"
      "v_cvt_pk_bf16_f32 v52, v120, v121\n"
      "v_cvt_pk_bf16_f32 v53, v122, v123\n"
      "v_cvt_pk_bf16_f32 v54, v124, v125\n"
      "v_cvt_pk_bf16_f32 v55, v126, v127\n"
      "v_pk_fma_f32 v[128:129], v[128:129], v[2:3], v[32:33]\n"
      "v_pk_fma_f32 v[130:131], v[130:131], v[2:3], v[34:35]\n"
      "v_pk_fma_f32 v[132:133], v[132:133], v[2:3], v[36:37]\n"
      "v_pk_fma_f32 v[134:135], v[134:135], v[2:3], v[38:39]\n"
      "v_pk_fma_f32 v[136:137], v[136:137], v[2:3], v[40:41]\n"
      "v_pk_fma_f32 v[138:139], v[138:139], v[2:3], v[42:43]\n"
      "v_pk_fma_f32 v[140:141], v[140:141], v[2:3], v[44:45]\n"
      "v_pk_fma_f32 v[142:143], v[142:143], v[2:3], v[46:47]\n"
      "v_cvt_pk_bf16_f32 v56, v128, v129\n"
      "v_cvt_pk_bf16_f32 v57, v130, v131\n"
      "v_cvt_pk_bf16_f32 v58, v132, v133\n"
      "v_cvt_pk_bf16_f32 v59, v134, v135\n"
      "v_cvt_pk_bf16_f32 v60, v136, v137\n"
      "v_cvt_pk_bf16_f32 v61, v138, v139\n"
      "v_cvt_pk_bf16_f32 v62, v140, v141\n"
      "v_cvt_pk_bf16_f32 v63, v142, v143\n"
      "v_accvgpr_write_b32 a8, v48\n"
      "v_accvgpr_write_b32 a9, v49\n"
      "v_accvgpr_write_b32 a10, v50\n"
      "v_accvgpr_write_b32 a11, v51\n"
      "v_accvgpr_write_b32 a12, v52\n"
      "v_accvgpr_write_b32 a13, v53\n"
      "v_accvgpr_write_b32 a14, v54\n"
      "v_accvgpr_write_b32 a15, v55\n"
      "v_accvgpr_write_b32 a16, v56\n"
      "v_accvgpr_write_b32 a17, v57\n"
      "v_accvgpr_write_b32 a18, v58\n"
      "v_accvgpr_write_b32 a19, v59\n"
      "v_accvgpr_write_b32 a20, v60\n"
      "v_accvgpr_write_b32 a21, v61\n"
      "v_accvgpr_write_b32 a22, v62\n"
      "v_accvgpr_write_b32 a23, v63\n"
      "v_accvgpr_write_b32 a40, v4\n"
      "v_accvgpr_write_b32 a41, v4\n"
      "v_accvgpr_write_b32 a42, v5\n"
      "v_accvgpr_write_b32 a43, v5\n"
      "v_mfma_f32_32x32x16_bf16 v[112:127], v[6:9], v[6:9], 0\n"
      "v_mfma_f32_32x32x16_bf16 v[128:143], v[6:9], v[6:9], 0\n"
      "v_mfma_f32_16x16x32_bf16 a[0:3], v[6:9], v[6:9], 0\n"
      "v_mfma_f32_16x16x32_bf16 a[4:7], v[6:9], v[6:9], 0\n"
      "s_barrier\n"
      "ds_read_b128 a[24:27], v145\n"
      "ds_read_b128 a[28:31], v145 offset:1024\n"
      "ds_read_b128 a[32:35], v145 offset:2048\n"
      "s_waitcnt lgkmcnt(0)\n"
      "v_mfma_f32_32x32x16_bf16 v[16:31], a[24:27], a[8:11], 0\n"
      "v_mfma_f32_32x32x16_bf16 v[48:63], a[24:27], a[16:19], 0\n"
      "v_mfma_f32_32x32x16_bf16 v[16:31], a[28:31], a[12:15], v[16:31]\n"
      "v_mfma_f32_32x32x16_bf16 v[48:63], a[28:31], a[20:23], v[48:63]\n"
      "ds_read_b128 a[24:27], v145 offset:4096\n"
      "ds_read_b128 a[28:31], v145 offset:5120\n"
      "s_nop 8\n"
      "v_max3_f32 v2, v16, v17, v18\n"
      "v_max3_f32 v4, v48, v49, v50\n"
      "v_max3_f32 v2, v2, v19, v20\n"
      "v_max3_f32 v4, v4, v51, v52\n"
      "v_max3_f32 v2, v2, v21, v22\n"
      "v_max3_f32 v4, v4, v53, v54\n"
      "v_max3_f32 v2, v2, v23, v24\n"
      "v_max3_f32 v4, v4, v55, v56\n"
      "v_max3_f32 v2, v2, v25, v26\n"
      "v_max3_f32 v4, v4, v57, v58\n"
      "v_max3_f32 v2, v2, v27, v28\n"
      "v_max3_f32 v4, v4, v59, v60\n"
      "v_max3_f32 v2, v2, v29, v30\n"
      "v_max3_f32 v4, v4, v61, v62\n"
      "v_max_f32 v2, v2, v31\n"
      "v_max_f32 v4, v4, v63\n"
      "v_mov_b32 v3, v2\n"
      "v_mov_b32 v5, v4\n"
      "s_nop 0\n"
      "v_permlane32_swap_b32 v2, v3\n"
      "v_permlane32_swap_b32 v4, v5\n"
      "v_max_f32 v2, v2, v3\n"
      "v_max_f32 v4, v4, v5\n"
      "v_sub_f32 v80, 0, v2\n"
      "v_sub_f32 v81, 0, v2\n"
      "v_sub_f32 v96, 0, v4\n"
      "v_sub_f32 v97, 0, v4\n"
      "v_pk_add_f32 v[16:17], v[16:17], v[80:81]\n"
      "v_pk_add_f32 v[18:19], v[18:19], v[80:81]\n"
      "v_pk_add_f32 v[20:21], v[20:21], v[80:81]\n"
      "v_pk_add_f32 v[22:23], v[22:23], v[80:81]\n"
      "v_pk_add_f32 v[24:25], v[24:25], v[80:81]\n"
      "v_pk_add_f32 v[26:27], v[26:27], v[80:81]\n"
      "v_pk_add_f32 v[28:29], v[28:29], v[80:81]\n"
      "v_pk_add_f32 v[30:31], v[30:31], v[80:81]\n"
      "v_pk_add_f32 v[48:49], v[48:49], v[96:97]\n"
      "v_pk_add_f32 v[50:51], v[50:51], v[96:97]\n"
      "v_pk_add_f32 v[52:53], v[52:53], v[96:97]\n"
      "v_pk_add_f32 v[54:55], v[54:55], v[96:97]\n"
      "v_pk_add_f32 v[56:57], v[56:57], v[96:97]\n"
      "v_pk_add_f32 v[58:59], v[58:59], v[96:97]\n"
      "v_pk_add_f32 v[60:61], v[60:61], v[96:97]\n"
      "v_pk_add_f32 v[62:63], v[62:63], v[96:97]\n"
      "v_mov_b64 v[82:83], v[80:81]\n"
      "v_mov_b64 v[84:85], v[80:81]\n"
      "v_mov_b64 v[86:87], v[80:81]\n"
      "v_mov_b64 v[88:89], v[80:81]\n"
      "v_mov_b64 v[90:91], v[80:81]\n"
      "v_mov_b64 v[92:93], v[80:81]\n"
      "v_mov_b64 v[94:95], v[80:81]\n"
      "v_mov_b64 v[98:99], v[96:97]\n"
      "v_mov_b64 v[100:101], v[96:97]\n"
      "v_mov_b64 v[102:103], v[96:97]\n"
      "v_mov_b64 v[104:105], v[96:97]\n"
      "v_mov_b64 v[106:107], v[96:97]\n"
      "v_mov_b64 v[108:109], v[96:97]\n"
      "v_mov_b64 v[110:111], v[96:97]\n"
      "s_waitcnt lgkmcnt(0)\n"
      "v_exp_f32 v16, v16\n"
      "v_exp_f32 v17, v17\n"
      "v_exp_f32 v18, v18\n"
      "v_exp_f32 v19, v19\n"
      "v_exp_f32 v20, v20\n"
      "ds_read_b128 a[36:39], v145 offset:3072\n"
      "v_exp_f32 v21, v21\n"
      "v_exp_f32 v22, v22\n"
      "v_exp_f32 v23, v23\n"
      "v_exp_f32 v48, v48\n"
      "v_exp_f32 v49, v49\n"
      "v_exp_f32 v50, v50\n"
      "v_mfma_f32_32x32x16_bf16 v[32:47], a[24:27], a[8:11], v[80:95]\n"
      "v_exp_f32 v51, v51\n"
      "v_exp_f32 v52, v52\n"
      "v_exp_f32 v53, v53\n"
      "v_mfma_f32_32x32x16_bf16 v[64:79], a[24:27], a[16:19], v[96:111]\n"
      "ds_read_b128 a[24:27], v145 offset:8192\n"
      "v_exp_f32 v54, v54\n"
      "v_exp_f32 v55, v55\n"
      "v_exp_f32 v24, v24\n"
      "v_mfma_f32_32x32x16_bf16 v[32:47], a[28:31], a[12:15], v[32:47]\n"
      "v_exp_f32 v25, v25\n"
      "v_exp_f32 v26, v26\n"
      "v_exp_f32 v27, v27\n"
      "v_mfma_f32_32x32x16_bf16 v[64:79], a[28:31], a[20:23], v[64:79]\n"
      "ds_read_b128 a[28:31], v145 offset:9216\n"
      "v_exp_f32 v28, v28\n"
      "v_exp_f32 v29, v29\n"
      "v_exp_f32 v30, v30\n"
      "v_smfmac_f32_32x32x32_bf16 v[112:127], a[32:35], v[16:23], v144\n"
      "v_exp_f32 v31, v31\n"
      "v_exp_f32 v56, v56\n"
      "v_exp_f32 v57, v57\n"
      "v_smfmac_f32_32x32x32_bf16 v[128:143], a[32:35], v[48:55], v144\n"
      "ds_read_b128 a[32:35], v145 offset:6144\n"
      "v_exp_f32 v58, v58\n"
      "v_exp_f32 v59, v59\n"
      "v_exp_f32 v60, v60\n"
      "v_smfmac_f32_16x16x64_bf16 a[0:3], a[40:43], v[16:23], v144\n"
      "v_exp_f32 v61, v61\n"
      "v_exp_f32 v62, v62\n"
      "v_smfmac_f32_16x16x64_bf16 a[4:7], a[40:43], v[48:55], v144\n"
      "v_exp_f32 v63, v63\n"
      "s_waitcnt lgkmcnt(0)\n"
      "v_exp_f32 v32, v32\n"
      "v_exp_f32 v33, v33\n"
      "v_smfmac_f32_32x32x32_bf16 v[112:127], a[36:39], v[24:31], v144\n"
      "v_exp_f32 v34, v34\n"
      "v_exp_f32 v35, v35\n"
      "v_exp_f32 v36, v36\n"
      "v_smfmac_f32_32x32x32_bf16 v[128:143], a[36:39], v[56:63], v144\n"
      "ds_read_b128 a[36:39], v145 offset:7168\n"
      "v_exp_f32 v37, v37\n"
      "v_exp_f32 v38, v38\n"
      "v_exp_f32 v39, v39\n"
      "v_smfmac_f32_16x16x64_bf16 a[0:3], a[40:43], v[24:31], v144\n"
      "v_exp_f32 v64, v64\n"
      "v_smfmac_f32_16x16x64_bf16 a[4:7], a[40:43], v[56:63], v144\n"
      "v_exp_f32 v65, v65\n"
      "v_exp_f32 v66, v66\n"
      "v_mfma_f32_32x32x16_bf16 v[16:31], a[24:27], a[8:11], v[80:95]\n"
      "v_exp_f32 v67, v67\n"
      "v_exp_f32 v68, v68\n"
      "v_exp_f32 v69, v69\n"
      "v_mfma_f32_32x32x16_bf16 v[48:63], a[24:27], a[16:19], v[96:111]\n"
      "ds_read_b128 a[24:27], v145 offset:12288\n"
      "v_exp_f32 v70, v70\n"
      "v_exp_f32 v71, v71\n"
      "v_exp_f32 v40, v40\n"
      "v_mfma_f32_32x32x16_bf16 v[16:31], a[28:31], a[12:15], v[16:31]\n"
      "v_exp_f32 v41, v41\n"
      "v_exp_f32 v42, v42\n"
      "v_exp_f32 v43, v43\n"
      "v_mfma_f32_32x32x16_bf16 v[48:63], a[28:31], a[20:23], v[48:63]\n"
      "ds_read_b128 a[28:31], v145 offset:13312\n"
      "v_exp_f32 v44, v44\n"
      "v_exp_f32 v45, v45\n"
      "v_exp_f32 v46, v46\n"
      "v_smfmac_f32_32x32x32_bf16 v[112:127], a[32:35], v[32:39], v144\n"
      "v_exp_f32 v47, v47\n"
      "v_exp_f32 v72, v72\n"
      "v_exp_f32 v73, v73\n"
      "v_smfmac_f32_32x32x32_bf16 v[128:143], a[32:35], v[64:71], v144\n"
      "ds_read_b128 a[32:35], v145 offset:10240\n"
      "v_exp_f32 v74, v74\n"
      "v_exp_f32 v75, v75\n"
      "v_exp_f32 v76, v76\n"
      "v_smfmac_f32_16x16x64_bf16 a[0:3], a[40:43], v[32:39], v144\n"
      "v_exp_f32 v77, v77\n"
      "v_exp_f32 v78, v78\n"
      "v_smfmac_f32_16x16x64_bf16 a[4:7], a[40:43], v[64:71], v144\n"
      "v_exp_f32 v79, v79\n"
      "s_waitcnt lgkmcnt(0)\n"
      "s_barrier\n"
      "v_exp_f32 v16, v16\n"
      "v_exp_f32 v17, v17\n"
      "v_smfmac_f32_32x32x32_bf16 v[112:127], a[36:39], v[40:47], v144\n"
      "v_exp_f32 v18, v18\n"
      "v_exp_f32 v19, v19\n"
      "v_exp_f32 v20, v20\n"
      "v_smfmac_f32_32x32x32_bf16 v[128:143], a[36:39], v[72:79], v144\n"
      "ds_read_b128 a[36:39], v145 offset:11264\n"
      "v_exp_f32 v21, v21\n"
      "v_exp_f32 v22, v22\n"
      "v_exp_f32 v23, v23\n"
      "v_smfmac_f32_16x16x64_bf16 a[0:3], a[40:43], v[40:47], v144\n"
      "v_exp_f32 v48, v48\n"
      "v_smfmac_f32_16x16x64_bf16 a[4:7], a[40:43], v[72:79], v144\n"
      "v_exp_f32 v49, v49\n"
      "v_exp_f32 v50, v50\n"
      "v_mfma_f32_32x32x16_bf16 v[32:47], a[24:27], a[8:11], v[80:95]\n"
      "v_exp_f32 v51, v51\n"
      "v_exp_f32 v52, v52\n"
      "v_exp_f32 v53, v53\n"
      "v_mfma_f32_32x32x16_bf16 v[64:79], a[24:27], a[16:19], v[96:111]\n"
      "ds_read_b128 a[24:27], v145 offset:16384\n"
      "v_exp_f32 v54, v54\n"
      "v_exp_f32 v55, v55\n"
      "v_exp_f32 v24, v24\n"
      "v_mfma_f32_32x32x16_bf16 v[32:47], a[28:31], a[12:15], v[32:47]\n"
      "v_exp_f32 v25, v25\n"
      "v_exp_f32 v26, v26\n"
      "v_exp_f32 v27, v27\n"
      "v_mfma_f32_32x32x16_bf16 v[64:79], a[28:31], a[20:23], v[64:79]\n"
      "ds_read_b128 a[28:31], v145 offset:17408\n"
      "v_exp_f32 v28, v28\n"
      "v_exp_f32 v29, v29\n"
      "v_exp_f32 v30, v30\n"
      "v_smfmac_f32_32x32x32_bf16 v[112:127], a[32:35], v[16:23], v144\n"
      "v_exp_f32 v31, v31\n"
      "v_exp_f32 v56, v56\n"
      "v_exp_f32 v57, v57\n"
      "v_smfmac_f32_32x32x32_bf16 v[128:143], a[32:35], v[48:55], v144\n"
      "ds_read_b128 a[32:35], v145 offset:14336\n"
      "v_exp_f32 v58, v58\n"
      "v_exp_f32 v59, v59\n"
      "v_exp_f32 v60, v60\n"
      "v_smfmac_f32_16x16x64_bf16 a[0:3], a[40:43], v[16:23], v144\n"
      "v_exp_f32 v61, v61\n"
      "v_exp_f32 v62, v62\n"
      "v_smfmac_f32_16x16x64_bf16 a[4:7], a[40:43], v[48:55], v144\n"
      "v_exp_f32 v63, v63\n"
      "s_waitcnt lgkmcnt(0)\n"
      "v_exp_f32 v32, v32\n"
      "v_exp_f32 v33, v33\n"
      "v_smfmac_f32_32x32x32_bf16 v[112:127], a[36:39], v[24:31], v144\n"
      "v_exp_f32 v34, v34\n"
      "v_exp_f32 v35, v35\n"
      "v_exp_f32 v36, v36\n"
      "v_smfmac_f32_32x32x32_bf16 v[128:143], a[36:39], v[56:63], v144\n"
      "ds_read_b128 a[36:39], v145 offset:15360\n"
      "v_exp_f32 v37, v37\n"
      "v_exp_f32 v38, v38\n"
      "v_exp_f32 v39, v39\n"
      "v_smfmac_f32_16x16x64_bf16 a[0:3], a[40:43], v[24:31], v144\n"
      "v_exp_f32 v64, v64\n"
      "v_smfmac_f32_16x16x64_bf16 a[4:7], a[40:43], v[56:63], v144\n"
      "v_exp_f32 v65, v65\n"
      "v_exp_f32 v66, v66\n"
      "v_mfma_f32_32x32x16_bf16 v[16:31], a[24:27], a[8:11], v[80:95]\n"
      "v_exp_f32 v67, v67\n"
      "v_exp_f32 v68, v68\n"
      "v_exp_f32 v69, v69\n"
      "v_mfma_f32_32x32x16_bf16 v[48:63], a[24:27], a[16:19], v[96:111]\n"
      "ds_read_b128 a[24:27], v145 offset:20480\n"
      "v_exp_f32 v70, v70\n"
      "v_exp_f32 v71, v71\n"
      "v_exp_f32 v40, v40\n"
      "v_mfma_f32_32x32x16_bf16 v[16:31], a[28:31], a[12:15], v[16:31]\n"
      "v_exp_f32 v41, v41\n"
      "v_exp_f32 v42, v42\n"
      "v_exp_f32 v43, v43\n"
      "v_mfma_f32_32x32x16_bf16 v[48:63], a[28:31], a[20:23], v[48:63]\n"
      "ds_read_b128 a[28:31], v145 offset:21504\n"
      "v_exp_f32 v44, v44\n"
      "v_exp_f32 v45, v45\n"
      "v_exp_f32 v46, v46\n"
      "v_smfmac_f32_32x32x32_bf16 v[112:127], a[32:35], v[32:39], v144\n"
      "v_exp_f32 v47, v47\n"
      "v_exp_f32 v72, v72\n"
      "v_exp_f32 v73, v73\n"
      "v_smfmac_f32_32x32x32_bf16 v[128:143], a[32:35], v[64:71], v144\n"
      "ds_read_b128 a[32:35], v145 offset:18432\n"
      "v_exp_f32 v74, v74\n"
      "v_exp_f32 v75, v75\n"
      "v_exp_f32 v76, v76\n"
      "v_smfmac_f32_16x16x64_bf16 a[0:3], a[40:43], v[32:39], v144\n"
      "v_exp_f32 v77, v77\n"
      "v_exp_f32 v78, v78\n"
      "v_smfmac_f32_16x16x64_bf16 a[4:7], a[40:43], v[64:71], v144\n"
      "v_exp_f32 v79, v79\n"
      "s_waitcnt lgkmcnt(0)\n"
      "v_exp_f32 v16, v16\n"
      "v_exp_f32 v17, v17\n"
      "v_smfmac_f32_32x32x32_bf16 v[112:127], a[36:39], v[40:47], v144\n"
      "v_exp_f32 v18, v18\n"
      "v_exp_f32 v19, v19\n"
      "v_exp_f32 v20, v20\n"
      "v_smfmac_f32_32x32x32_bf16 v[128:143], a[36:39], v[72:79], v144\n"
      "ds_read_b128 a[36:39], v145 offset:19456\n"
      "v_exp_f32 v21, v21\n"
      "v_exp_f32 v22, v22\n"
      "v_exp_f32 v23, v23\n"
      "v_smfmac_f32_16x16x64_bf16 a[0:3], a[40:43], v[40:47], v144\n"
      "v_exp_f32 v48, v48\n"
      "v_smfmac_f32_16x16x64_bf16 a[4:7], a[40:43], v[72:79], v144\n"
      "v_exp_f32 v49, v49\n"
      "v_exp_f32 v50, v50\n"
      "v_mfma_f32_32x32x16_bf16 v[32:47], a[24:27], a[8:11], v[80:95]\n"
      "v_exp_f32 v51, v51\n"
      "v_exp_f32 v52, v52\n"
      "v_exp_f32 v53, v53\n"
      "v_mfma_f32_32x32x16_bf16 v[64:79], a[24:27], a[16:19], v[96:111]\n"
      "ds_read_b128 a[24:27], v145 offset:24576\n"
      "v_exp_f32 v54, v54\n"
      "v_exp_f32 v55, v55\n"
      "v_exp_f32 v24, v24\n"
      "v_mfma_f32_32x32x16_bf16 v[32:47], a[28:31], a[12:15], v[32:47]\n"
      "v_exp_f32 v25, v25\n"
      "v_exp_f32 v26, v26\n"
      "v_exp_f32 v27, v27\n"
      "v_mfma_f32_32x32x16_bf16 v[64:79], a[28:31], a[20:23], v[64:79]\n"
      "ds_read_b128 a[28:31], v145 offset:25600\n"
      "v_exp_f32 v28, v28\n"
      "v_exp_f32 v29, v29\n"
      "v_exp_f32 v30, v30\n"
      "v_smfmac_f32_32x32x32_bf16 v[112:127], a[32:35], v[16:23], v144\n"
      "v_exp_f32 v31, v31\n"
      "v_exp_f32 v56, v56\n"
      "v_exp_f32 v57, v57\n"
      "v_smfmac_f32_32x32x32_bf16 v[128:143], a[32:35], v[48:55], v144\n"
      "ds_read_b128 a[32:35], v145 offset:22528\n"
      "v_exp_f32 v58, v58\n"
      "v_exp_f32 v59, v59\n"
      "v_exp_f32 v60, v60\n"
      "v_smfmac_f32_16x16x64_bf16 a[0:3], a[40:43], v[16:23], v144\n"
      "v_exp_f32 v61, v61\n"
      "v_exp_f32 v62, v62\n"
      "v_smfmac_f32_16x16x64_bf16 a[4:7], a[40:43], v[48:55], v144\n"
      "v_exp_f32 v63, v63\n"
      "s_waitcnt lgkmcnt(0)\n"
      "v_exp_f32 v32, v32\n"
      "v_exp_f32 v33, v33\n"
      "v_smfmac_f32_32x32x32_bf16 v[112:127], a[36:39], v[24:31], v144\n"
      "v_exp_f32 v34, v34\n"
      "v_exp_f32 v35, v35\n"
      "v_exp_f32 v36, v36\n"
      "v_smfmac_f32_32x32x32_bf16 v[128:143], a[36:39], v[56:63], v144\n"
      "ds_read_b128 a[36:39], v145 offset:23552\n"
      "v_exp_f32 v37, v37\n"
      "v_exp_f32 v38, v38\n"
      "v_exp_f32 v39, v39\n"
      "v_smfmac_f32_16x16x64_bf16 a[0:3], a[40:43], v[24:31], v144\n"
      "v_exp_f32 v64, v64\n"
      "v_smfmac_f32_16x16x64_bf16 a[4:7], a[40:43], v[56:63], v144\n"
      "v_exp_f32 v65, v65\n"
      "v_exp_f32 v66, v66\n"
      "v_mfma_f32_32x32x16_bf16 v[16:31], a[24:27], a[8:11], v[80:95]\n"
      "v_exp_f32 v67, v67\n"
      "v_exp_f32 v68, v68\n"
      "v_exp_f32 v69, v69\n"
      "v_mfma_f32_32x32x16_bf16 v[48:63], a[24:27], a[16:19], v[96:111]\n"
      "ds_read_b128 a[24:27], v145 offset:28672\n"
      "v_exp_f32 v70, v70\n"
      "v_exp_f32 v71, v71\n"
      "v_exp_f32 v40, v40\n"
      "v_mfma_f32_32x32x16_bf16 v[16:31], a[28:31], a[12:15], v[16:31]\n"
      "v_exp_f32 v41, v41\n"
      "v_exp_f32 v42, v42\n"
      "v_exp_f32 v43, v43\n"
      "v_mfma_f32_32x32x16_bf16 v[48:63], a[28:31], a[20:23], v[48:63]\n"
      "ds_read_b128 a[28:31], v145 offset:29696\n"
      "v_exp_f32 v44, v44\n"
      "v_exp_f32 v45, v45\n"
      "v_exp_f32 v46, v46\n"
      "v_smfmac_f32_32x32x32_bf16 v[112:127], a[32:35], v[32:39], v144\n"
      "v_exp_f32 v47, v47\n"
      "v_exp_f32 v72, v72\n"
      "v_exp_f32 v73, v73\n"
      "v_smfmac_f32_32x32x32_bf16 v[128:143], a[32:35], v[64:71], v144\n"
      "ds_read_b128 a[32:35], v145 offset:26624\n"
      "v_exp_f32 v74, v74\n"
      "v_exp_f32 v75, v75\n"
      "v_exp_f32 v76, v76\n"
      "v_smfmac_f32_16x16x64_bf16 a[0:3], a[40:43], v[32:39], v144\n"
      "v_exp_f32 v77, v77\n"
      "v_exp_f32 v78, v78\n"
      "v_smfmac_f32_16x16x64_bf16 a[4:7], a[40:43], v[64:71], v144\n"
      "v_exp_f32 v79, v79\n"
      "s_waitcnt lgkmcnt(0)\n"
      "s_barrier\n"
      "v_exp_f32 v16, v16\n"
      "v_exp_f32 v17, v17\n"
      "v_smfmac_f32_32x32x32_bf16 v[112:127], a[36:39], v[40:47], v144\n"
      "v_exp_f32 v18, v18\n"
      "v_exp_f32 v19, v19\n"
      "v_exp_f32 v20, v20\n"
      "v_smfmac_f32_32x32x32_bf16 v[128:143], a[36:39], v[72:79], v144\n"
      "ds_read_b128 a[36:39], v145 offset:27648\n"
      "v_exp_f32 v21, v21\n"
      "v_exp_f32 v22, v22\n"
      "v_exp_f32 v23, v23\n"
      "v_smfmac_f32_16x16x64_bf16 a[0:3], a[40:43], v[40:47], v144\n"
      "v_exp_f32 v48, v48\n"
      "v_smfmac_f32_16x16x64_bf16 a[4:7], a[40:43], v[72:79], v144\n"
      "v_exp_f32 v49, v49\n"
      "v_exp_f32 v50, v50\n"
      "v_mfma_f32_32x32x16_bf16 v[32:47], a[24:27], a[8:11], v[80:95]\n"
      "v_exp_f32 v51, v51\n"
      "v_exp_f32 v52, v52\n"
      "v_exp_f32 v53, v53\n"
      "v_mfma_f32_32x32x16_bf16 v[64:79], a[24:27], a[16:19], v[96:111]\n"
      "ds_read_b128 a[24:27], v145 offset:32768\n"
      "v_exp_f32 v54, v54\n"
      "v_exp_f32 v55, v55\n"
      "v_exp_f32 v24, v24\n"
      "v_mfma_f32_32x32x16_bf16 v[32:47], a[28:31], a[12:15], v[32:47]\n"
      "v_exp_f32 v25, v25\n"
      "v_exp_f32 v26, v26\n"
      "v_exp_f32 v27, v27\n"
      "v_mfma_f32_32x32x16_bf16 v[64:79], a[28:31], a[20:23], v[64:79]\n"
      "ds_read_b128 a[28:31], v145 offset:33792\n"
      "v_exp_f32 v28, v28\n"
      "v_exp_f32 v29, v29\n"
      "v_exp_f32 v30, v30\n"
      "v_smfmac_f32_32x32x32_bf16 v[112:127], a[32:35], v[16:23], v144\n"
      "v_exp_f32 v31, v31\n"
      "v_exp_f32 v56, v56\n"
      "v_exp_f32 v57, v57\n"
      "v_smfmac_f32_32x32x32_bf16 v[128:143], a[32:35], v[48:55], v144\n"
      "ds_read_b128 a[32:35], v145 offset:30720\n"
      "v_exp_f32 v58, v58\n"
      "v_exp_f32 v59, v59\n"
      "v_exp_f32 v60, v60\n"
      "v_smfmac_f32_16x16x64_bf16 a[0:3], a[40:43], v[16:23], v144\n"
      "v_exp_f32 v61, v61\n"
      "v_exp_f32 v62, v62\n"
      "v_smfmac_f32_16x16x64_bf16 a[4:7], a[40:43], v[48:55], v144\n"
      "v_exp_f32 v63, v63\n"
      "s_waitcnt lgkmcnt(0)\n"
      "v_exp_f32 v32, v32\n"
      "v_exp_f32 v33, v33\n"
      "v_smfmac_f32_32x32x32_bf16 v[112:127], a[36:39], v[24:31], v144\n"
      "v_exp_f32 v34, v34\n"
      "v_exp_f32 v35, v35\n"
      "v_exp_f32 v36, v36\n"
      "v_smfmac_f32_32x32x32_bf16 v[128:143], a[36:39], v[56:63], v144\n"
      "ds_read_b128 a[36:39], v145 offset:31744\n"
      "v_exp_f32 v37, v37\n"
      "v_exp_f32 v38, v38\n"
      "v_exp_f32 v39, v39\n"
      "v_smfmac_f32_16x16x64_bf16 a[0:3], a[40:43], v[24:31], v144\n"
      "v_exp_f32 v64, v64\n"
      "v_smfmac_f32_16x16x64_bf16 a[4:7], a[40:43], v[56:63], v144\n"
      "v_exp_f32 v65, v65\n"
      "v_exp_f32 v66, v66\n"
      "v_mfma_f32_32x32x16_bf16 v[16:31], a[24:27], a[8:11], v[80:95]\n"
      "v_exp_f32 v67, v67\n"
      "v_exp_f32 v68, v68\n"
      "v_exp_f32 v69, v69\n"
      "v_mfma_f32_32x32x16_bf16 v[48:63], a[24:27], a[16:19], v[96:111]\n"
      "ds_read_b128 a[24:27], v145 offset:36864\n"
      "v_exp_f32 v70, v70\n"
      "v_exp_f32 v71, v71\n"
      "v_exp_f32 v40, v40\n"
      "v_mfma_f32_32x32x16_bf16 v[16:31], a[28:31], a[12:15], v[16:31]\n"
      "v_exp_f32 v41, v41\n"
      "v_exp_f32 v42, v42\n"
      "v_exp_f32 v43, v43\n"
      "v_mfma_f32_32x32x16_bf16 v[48:63], a[28:31], a[20:23], v[48:63]\n"
      "ds_read_b128 a[28:31], v145 offset:37888\n"
      "v_exp_f32 v44, v44\n"
      "v_exp_f32 v45, v45\n"
      "v_exp_f32 v46, v46\n"
      "v_smfmac_f32_32x32x32_bf16 v[112:127], a[32:35], v[32:39], v144\n"
      "v_exp_f32 v47, v47\n"
      "v_exp_f32 v72, v72\n"
      "v_exp_f32 v73, v73\n"
      "v_smfmac_f32_32x32x32_bf16 v[128:143], a[32:35], v[64:71], v144\n"
      "ds_read_b128 a[32:35], v145 offset:34816\n"
      "v_exp_f32 v74, v74\n"
      "v_exp_f32 v75, v75\n"
      "v_exp_f32 v76, v76\n"
      "v_smfmac_f32_16x16x64_bf16 a[0:3], a[40:43], v[32:39], v144\n"
      "v_exp_f32 v77, v77\n"
      "v_exp_f32 v78, v78\n"
      "v_smfmac_f32_16x16x64_bf16 a[4:7], a[40:43], v[64:71], v144\n"
      "v_exp_f32 v79, v79\n"
      "s_waitcnt lgkmcnt(0)\n"
      "v_exp_f32 v16, v16\n"
      "v_exp_f32 v17, v17\n"
      "v_smfmac_f32_32x32x32_bf16 v[112:127], a[36:39], v[40:47], v144\n"
      "v_exp_f32 v18, v18\n"
      "v_exp_f32 v19, v19\n"
      "v_exp_f32 v20, v20\n"
      "v_smfmac_f32_32x32x32_bf16 v[128:143], a[36:39], v[72:79], v144\n"
      "ds_read_b128 a[36:39], v145 offset:35840\n"
      "v_exp_f32 v21, v21\n"
      "v_exp_f32 v22, v22\n"
      "v_exp_f32 v23, v23\n"
      "v_smfmac_f32_16x16x64_bf16 a[0:3], a[40:43], v[40:47], v144\n"
      "v_exp_f32 v48, v48\n"
      "v_smfmac_f32_16x16x64_bf16 a[4:7], a[40:43], v[72:79], v144\n"
      "v_exp_f32 v49, v49\n"
      "v_exp_f32 v50, v50\n"
      "v_mfma_f32_32x32x16_bf16 v[32:47], a[24:27], a[8:11], v[80:95]\n"
      "v_exp_f32 v51, v51\n"
      "v_exp_f32 v52, v52\n"
      "v_exp_f32 v53, v53\n"
      "v_mfma_f32_32x32x16_bf16 v[64:79], a[24:27], a[16:19], v[96:111]\n"
      "ds_read_b128 a[24:27], v145 offset:40960\n"
      "v_exp_f32 v54, v54\n"
      "v_exp_f32 v55, v55\n"
      "v_exp_f32 v24, v24\n"
      "v_mfma_f32_32x32x16_bf16 v[32:47], a[28:31], a[12:15], v[32:47]\n"
      "v_exp_f32 v25, v25\n"
      "v_exp_f32 v26, v26\n"
      "v_exp_f32 v27, v27\n"
      "v_mfma_f32_32x32x16_bf16 v[64:79], a[28:31], a[20:23], v[64:79]\n"
      "ds_read_b128 a[28:31], v145 offset:41984\n"
      "v_exp_f32 v28, v28\n"
      "v_exp_f32 v29, v29\n"
      "v_exp_f32 v30, v30\n"
      "v_smfmac_f32_32x32x32_bf16 v[112:127], a[32:35], v[16:23], v144\n"
      "v_exp_f32 v31, v31\n"
      "v_exp_f32 v56, v56\n"
      "v_exp_f32 v57, v57\n"
      "v_smfmac_f32_32x32x32_bf16 v[128:143], a[32:35], v[48:55], v144\n"
      "ds_read_b128 a[32:35], v145 offset:38912\n"
      "v_exp_f32 v58, v58\n"
      "v_exp_f32 v59, v59\n"
      "v_exp_f32 v60, v60\n"
      "v_smfmac_f32_16x16x64_bf16 a[0:3], a[40:43], v[16:23], v144\n"
      "v_exp_f32 v61, v61\n"
      "v_exp_f32 v62, v62\n"
      "v_smfmac_f32_16x16x64_bf16 a[4:7], a[40:43], v[48:55], v144\n"
      "v_exp_f32 v63, v63\n"
      "s_waitcnt lgkmcnt(0)\n"
      "v_exp_f32 v32, v32\n"
      "v_exp_f32 v33, v33\n"
      "v_smfmac_f32_32x32x32_bf16 v[112:127], a[36:39], v[24:31], v144\n"
      "v_exp_f32 v34, v34\n"
      "v_exp_f32 v35, v35\n"
      "v_exp_f32 v36, v36\n"
      "v_smfmac_f32_32x32x32_bf16 v[128:143], a[36:39], v[56:63], v144\n"
      "ds_read_b128 a[36:39], v145 offset:39936\n"
      "v_exp_f32 v37, v37\n"
      "v_exp_f32 v38, v38\n"
      "v_exp_f32 v39, v39\n"
      "v_smfmac_f32_16x16x64_bf16 a[0:3], a[40:43], v[24:31], v144\n"
      "v_exp_f32 v64, v64\n"
      "v_smfmac_f32_16x16x64_bf16 a[4:7], a[40:43], v[56:63], v144\n"
      "v_exp_f32 v65, v65\n"
      "v_exp_f32 v66, v66\n"
      "v_mfma_f32_32x32x16_bf16 v[16:31], a[24:27], a[8:11], v[80:95]\n"
      "v_exp_f32 v67, v67\n"
      "v_exp_f32 v68, v68\n"
      "v_exp_f32 v69, v69\n"
      "v_mfma_f32_32x32x16_bf16 v[48:63], a[24:27], a[16:19], v[96:111]\n"
      "ds_read_b128 a[24:27], v145 offset:45056\n"
      "v_exp_f32 v70, v70\n"
      "v_exp_f32 v71, v71\n"
      "v_exp_f32 v40, v40\n"
      "v_mfma_f32_32x32x16_bf16 v[16:31], a[28:31], a[12:15], v[16:31]\n"
      "v_exp_f32 v41, v41\n"
      "v_exp_f32 v42, v42\n"
      "v_exp_f32 v43, v43\n"
      "v_mfma_f32_32x32x16_bf16 v[48:63], a[28:31], a[20:23], v[48:63]\n"
      "ds_read_b128 a[28:31], v145 offset:46080\n"
      "v_exp_f32 v44, v44\n"
      "v_exp_f32 v45, v45\n"
      "v_exp_f32 v46, v46\n"
      "v_smfmac_f32_32x32x32_bf16 v[112:127], a[32:35], v[32:39], v144\n"
      "v_exp_f32 v47, v47\n"
      "v_exp_f32 v72, v72\n"
      "v_exp_f32 v73, v73\n"
      "v_smfmac_f32_32x32x32_bf16 v[128:143], a[32:35], v[64:71], v144\n"
      "ds_read_b128 a[32:35], v145 offset:43008\n"
      "v_exp_f32 v74, v74\n"
      "v_exp_f32 v75, v75\n"
      "v_exp_f32 v76, v76\n"
      "v_smfmac_f32_16x16x64_bf16 a[0:3], a[40:43], v[32:39], v144\n"
      "v_exp_f32 v77, v77\n"
      "v_exp_f32 v78, v78\n"
      "v_smfmac_f32_16x16x64_bf16 a[4:7], a[40:43], v[64:71], v144\n"
      "v_exp_f32 v79, v79\n"
      "s_waitcnt lgkmcnt(0)\n"
      "s_barrier\n"
      "v_exp_f32 v16, v16\n"
      "v_exp_f32 v17, v17\n"
      "v_smfmac_f32_32x32x32_bf16 v[112:127], a[36:39], v[40:47], v144\n"
      "v_exp_f32 v18, v18\n"
      "v_exp_f32 v19, v19\n"
      "v_exp_f32 v20, v20\n"
      "v_smfmac_f32_32x32x32_bf16 v[128:143], a[36:39], v[72:79], v144\n"
      "ds_read_b128 a[36:39], v145 offset:44032\n"
      "v_exp_f32 v21, v21\n"
      "v_exp_f32 v22, v22\n"
      "v_exp_f32 v23, v23\n"
      "v_smfmac_f32_16x16x64_bf16 a[0:3], a[40:43], v[40:47], v144\n"
      "v_exp_f32 v48, v48\n"
      "v_smfmac_f32_16x16x64_bf16 a[4:7], a[40:43], v[72:79], v144\n"
      "v_exp_f32 v49, v49\n"
      "v_exp_f32 v50, v50\n"
      "v_mfma_f32_32x32x16_bf16 v[32:47], a[24:27], a[8:11], v[80:95]\n"
      "v_exp_f32 v51, v51\n"
      "v_exp_f32 v52, v52\n"
      "v_exp_f32 v53, v53\n"
      "v_mfma_f32_32x32x16_bf16 v[64:79], a[24:27], a[16:19], v[96:111]\n"
      "ds_read_b128 a[24:27], v145 offset:49152\n"
      "v_exp_f32 v54, v54\n"
      "v_exp_f32 v55, v55\n"
      "v_exp_f32 v24, v24\n"
      "v_mfma_f32_32x32x16_bf16 v[32:47], a[28:31], a[12:15], v[32:47]\n"
      "v_exp_f32 v25, v25\n"
      "v_exp_f32 v26, v26\n"
      "v_exp_f32 v27, v27\n"
      "v_mfma_f32_32x32x16_bf16 v[64:79], a[28:31], a[20:23], v[64:79]\n"
      "ds_read_b128 a[28:31], v145 offset:50176\n"
      "v_exp_f32 v28, v28\n"
      "v_exp_f32 v29, v29\n"
      "v_exp_f32 v30, v30\n"
      "v_smfmac_f32_32x32x32_bf16 v[112:127], a[32:35], v[16:23], v144\n"
      "v_exp_f32 v31, v31\n"
      "v_exp_f32 v56, v56\n"
      "v_exp_f32 v57, v57\n"
      "v_smfmac_f32_32x32x32_bf16 v[128:143], a[32:35], v[48:55], v144\n"
      "ds_read_b128 a[32:35], v145 offset:47104\n"
      "v_exp_f32 v58, v58\n"
      "v_exp_f32 v59, v59\n"
      "v_exp_f32 v60, v60\n"
      "v_smfmac_f32_16x16x64_bf16 a[0:3], a[40:43], v[16:23], v144\n"
      "v_exp_f32 v61, v61\n"
      "v_exp_f32 v62, v62\n"
      "v_smfmac_f32_16x16x64_bf16 a[4:7], a[40:43], v[48:55], v144\n"
      "v_exp_f32 v63, v63\n"
      "s_waitcnt lgkmcnt(0)\n"
      "v_exp_f32 v32, v32\n"
      "v_exp_f32 v33, v33\n"
      "v_smfmac_f32_32x32x32_bf16 v[112:127], a[36:39], v[24:31], v144\n"
      "v_exp_f32 v34, v34\n"
      "v_exp_f32 v35, v35\n"
      "v_exp_f32 v36, v36\n"
      "v_smfmac_f32_32x32x32_bf16 v[128:143], a[36:39], v[56:63], v144\n"
      "ds_read_b128 a[36:39], v145 offset:48128\n"
      "v_exp_f32 v37, v37\n"
      "v_exp_f32 v38, v38\n"
      "v_exp_f32 v39, v39\n"
      "v_smfmac_f32_16x16x64_bf16 a[0:3], a[40:43], v[24:31], v144\n"
      "v_exp_f32 v64, v64\n"
      "v_smfmac_f32_16x16x64_bf16 a[4:7], a[40:43], v[56:63], v144\n"
      "v_exp_f32 v65, v65\n"
      "v_exp_f32 v66, v66\n"
      "v_mfma_f32_32x32x16_bf16 v[16:31], a[24:27], a[8:11], v[80:95]\n"
      "v_exp_f32 v67, v67\n"
      "v_exp_f32 v68, v68\n"
      "v_exp_f32 v69, v69\n"
      "v_mfma_f32_32x32x16_bf16 v[48:63], a[24:27], a[16:19], v[96:111]\n"
      "ds_read_b128 a[24:27], v145 offset:53248\n"
      "v_exp_f32 v70, v70\n"
      "v_exp_f32 v71, v71\n"
      "v_exp_f32 v40, v40\n"
      "v_mfma_f32_32x32x16_bf16 v[16:31], a[28:31], a[12:15], v[16:31]\n"
      "v_exp_f32 v41, v41\n"
      "v_exp_f32 v42, v42\n"
      "v_exp_f32 v43, v43\n"
      "v_mfma_f32_32x32x16_bf16 v[48:63], a[28:31], a[20:23], v[48:63]\n"
      "ds_read_b128 a[28:31], v145 offset:54272\n"
      "v_exp_f32 v44, v44\n"
      "v_exp_f32 v45, v45\n"
      "v_exp_f32 v46, v46\n"
      "v_smfmac_f32_32x32x32_bf16 v[112:127], a[32:35], v[32:39], v144\n"
      "v_exp_f32 v47, v47\n"
      "v_exp_f32 v72, v72\n"
      "v_exp_f32 v73, v73\n"
      "v_smfmac_f32_32x32x32_bf16 v[128:143], a[32:35], v[64:71], v144\n"
      "ds_read_b128 a[32:35], v145 offset:51200\n"
      "v_exp_f32 v74, v74\n"
      "v_exp_f32 v75, v75\n"
      "v_exp_f32 v76, v76\n"
      "v_smfmac_f32_16x16x64_bf16 a[0:3], a[40:43], v[32:39], v144\n"
      "v_exp_f32 v77, v77\n"
      "v_exp_f32 v78, v78\n"
      "v_smfmac_f32_16x16x64_bf16 a[4:7], a[40:43], v[64:71], v144\n"
      "v_exp_f32 v79, v79\n"
      "s_waitcnt lgkmcnt(0)\n"
      "v_exp_f32 v16, v16\n"
      "v_exp_f32 v17, v17\n"
      "v_smfmac_f32_32x32x32_bf16 v[112:127], a[36:39], v[40:47], v144\n"
      "v_exp_f32 v18, v18\n"
      "v_exp_f32 v19, v19\n"
      "v_exp_f32 v20, v20\n"
      "v_smfmac_f32_32x32x32_bf16 v[128:143], a[36:39], v[72:79], v144\n"
      "ds_read_b128 a[36:39], v145 offset:52224\n"
      "v_exp_f32 v21, v21\n"
      "v_exp_f32 v22, v22\n"
      "v_exp_f32 v23, v23\n"
      "v_smfmac_f32_16x16x64_bf16 a[0:3], a[40:43], v[40:47], v144\n"
      "v_exp_f32 v48, v48\n"
      "v_smfmac_f32_16x16x64_bf16 a[4:7], a[40:43], v[72:79], v144\n"
      "v_exp_f32 v49, v49\n"
      "v_exp_f32 v50, v50\n"
      "v_mfma_f32_32x32x16_bf16 v[32:47], a[24:27], a[8:11], v[80:95]\n"
      "v_exp_f32 v51, v51\n"
      "v_exp_f32 v52, v52\n"
      "v_exp_f32 v53, v53\n"
      "v_mfma_f32_32x32x16_bf16 v[64:79], a[24:27], a[16:19], v[96:111]\n"
      "ds_read_b128 a[24:27], v145 offset:57344\n"
      "v_exp_f32 v54, v54\n"
      "v_exp_f32 v55, v55\n"
      "v_exp_f32 v24, v24\n"
      "v_mfma_f32_32x32x16_bf16 v[32:47], a[28:31], a[12:15], v[32:47]\n"
      "v_exp_f32 v25, v25\n"
      "v_exp_f32 v26, v26\n"
      "v_exp_f32 v27, v27\n"
      "v_mfma_f32_32x32x16_bf16 v[64:79], a[28:31], a[20:23], v[64:79]\n"
      "ds_read_b128 a[28:31], v145 offset:58368\n"
      "v_exp_f32 v28, v28\n"
      "v_exp_f32 v29, v29\n"
      "v_exp_f32 v30, v30\n"
      "v_smfmac_f32_32x32x32_bf16 v[112:127], a[32:35], v[16:23], v144\n"
      "v_exp_f32 v31, v31\n"
      "v_exp_f32 v56, v56\n"
      "v_exp_f32 v57, v57\n"
      "v_smfmac_f32_32x32x32_bf16 v[128:143], a[32:35], v[48:55], v144\n"
      "ds_read_b128 a[32:35], v145 offset:55296\n"
      "v_exp_f32 v58, v58\n"
      "v_exp_f32 v59, v59\n"
      "v_exp_f32 v60, v60\n"
      "v_smfmac_f32_16x16x64_bf16 a[0:3], a[40:43], v[16:23], v144\n"
      "v_exp_f32 v61, v61\n"
      "v_exp_f32 v62, v62\n"
      "v_smfmac_f32_16x16x64_bf16 a[4:7], a[40:43], v[48:55], v144\n"
      "v_exp_f32 v63, v63\n"
      "s_waitcnt lgkmcnt(0)\n"
      "v_exp_f32 v32, v32\n"
      "v_exp_f32 v33, v33\n"
      "v_smfmac_f32_32x32x32_bf16 v[112:127], a[36:39], v[24:31], v144\n"
      "v_exp_f32 v34, v34\n"
      "v_exp_f32 v35, v35\n"
      "v_exp_f32 v36, v36\n"
      "v_smfmac_f32_32x32x32_bf16 v[128:143], a[36:39], v[56:63], v144\n"
      "ds_read_b128 a[36:39], v145 offset:56320\n"
      "v_exp_f32 v37, v37\n"
      "v_exp_f32 v38, v38\n"
      "v_exp_f32 v39, v39\n"
      "v_smfmac_f32_16x16x64_bf16 a[0:3], a[40:43], v[24:31], v144\n"
      "v_exp_f32 v64, v64\n"
      "v_smfmac_f32_16x16x64_bf16 a[4:7], a[40:43], v[56:63], v144\n"
      "v_exp_f32 v65, v65\n"
      "v_exp_f32 v66, v66\n"
      "v_mfma_f32_32x32x16_bf16 v[16:31], a[24:27], a[8:11], v[80:95]\n"
      "v_exp_f32 v67, v67\n"
      "v_exp_f32 v68, v68\n"
      "v_exp_f32 v69, v69\n"
      "v_mfma_f32_32x32x16_bf16 v[48:63], a[24:27], a[16:19], v[96:111]\n"
      "ds_read_b128 a[24:27], v145 offset:61440\n"
      "v_exp_f32 v70, v70\n"
      "v_exp_f32 v71, v71\n"
      "v_exp_f32 v40, v40\n"
      "v_mfma_f32_32x32x16_bf16 v[16:31], a[28:31], a[12:15], v[16:31]\n"
      "v_exp_f32 v41, v41\n"
      "v_exp_f32 v42, v42\n"
      "v_exp_f32 v43, v43\n"
      "v_mfma_f32_32x32x16_bf16 v[48:63], a[28:31], a[20:23], v[48:63]\n"
      "ds_read_b128 a[28:31], v145 offset:62464\n"
      "v_exp_f32 v44, v44\n"
      "v_exp_f32 v45, v45\n"
      "v_exp_f32 v46, v46\n"
      "v_smfmac_f32_32x32x32_bf16 v[112:127], a[32:35], v[32:39], v144\n"
      "v_exp_f32 v47, v47\n"
      "v_exp_f32 v72, v72\n"
      "v_exp_f32 v73, v73\n"
      "v_smfmac_f32_32x32x32_bf16 v[128:143], a[32:35], v[64:71], v144\n"
      "ds_read_b128 a[32:35], v145 offset:59392\n"
      "v_exp_f32 v74, v74\n"
      "v_exp_f32 v75, v75\n"
      "v_exp_f32 v76, v76\n"
      "v_smfmac_f32_16x16x64_bf16 a[0:3], a[40:43], v[32:39], v144\n"
      "v_exp_f32 v77, v77\n"
      "v_exp_f32 v78, v78\n"
      "v_smfmac_f32_16x16x64_bf16 a[4:7], a[40:43], v[64:71], v144\n"
      "v_exp_f32 v79, v79\n"
      "s_waitcnt lgkmcnt(0)\n"
      "s_barrier\n"
      "v_exp_f32 v16, v16\n"
      "v_exp_f32 v17, v17\n"
      "v_smfmac_f32_32x32x32_bf16 v[112:127], a[36:39], v[40:47], v144\n"
      "v_exp_f32 v18, v18\n"
      "v_exp_f32 v19, v19\n"
      "v_exp_f32 v20, v20\n"
      "v_smfmac_f32_32x32x32_bf16 v[128:143], a[36:39], v[72:79], v144\n"
      "ds_read_b128 a[36:39], v145 offset:60416\n"
      "v_exp_f32 v21, v21\n"
      "v_exp_f32 v22, v22\n"
      "v_exp_f32 v23, v23\n"
      "v_smfmac_f32_16x16x64_bf16 a[0:3], a[40:43], v[40:47], v144\n"
      "v_exp_f32 v48, v48\n"
      "v_smfmac_f32_16x16x64_bf16 a[4:7], a[40:43], v[72:79], v144\n"
      "v_exp_f32 v49, v49\n"
      "v_exp_f32 v50, v50\n"
      "v_mfma_f32_32x32x16_bf16 v[32:47], a[24:27], a[8:11], v[80:95]\n"
      "v_exp_f32 v51, v51\n"
      "v_exp_f32 v52, v52\n"
      "v_exp_f32 v53, v53\n"
      "v_mfma_f32_32x32x16_bf16 v[64:79], a[24:27], a[16:19], v[96:111]\n"
      "ds_read_b128 a[24:27], v145 offset:0\n"
      "v_exp_f32 v54, v54\n"
      "v_exp_f32 v55, v55\n"
      "v_exp_f32 v24, v24\n"
      "v_mfma_f32_32x32x16_bf16 v[32:47], a[28:31], a[12:15], v[32:47]\n"
      "v_exp_f32 v25, v25\n"
      "v_exp_f32 v26, v26\n"
      "v_exp_f32 v27, v27\n"
      "v_mfma_f32_32x32x16_bf16 v[64:79], a[28:31], a[20:23], v[64:79]\n"
      "ds_read_b128 a[28:31], v145 offset:1024\n"
      "v_exp_f32 v28, v28\n"
      "v_exp_f32 v29, v29\n"
      "v_exp_f32 v30, v30\n"
      "v_smfmac_f32_32x32x32_bf16 v[112:127], a[32:35], v[16:23], v144\n"
      "v_exp_f32 v31, v31\n"
      "v_exp_f32 v56, v56\n"
      "v_exp_f32 v57, v57\n"
      "v_smfmac_f32_32x32x32_bf16 v[128:143], a[32:35], v[48:55], v144\n"
      "ds_read_b128 a[32:35], v145 offset:63488\n"
      "v_exp_f32 v58, v58\n"
      "v_exp_f32 v59, v59\n"
      "v_exp_f32 v60, v60\n"
      "v_smfmac_f32_16x16x64_bf16 a[0:3], a[40:43], v[16:23], v144\n"
      "v_exp_f32 v61, v61\n"
      "v_exp_f32 v62, v62\n"
      "v_smfmac_f32_16x16x64_bf16 a[4:7], a[40:43], v[48:55], v144\n"
      "v_exp_f32 v63, v63\n"
      "s_waitcnt lgkmcnt(0)\n"
      "v_exp_f32 v32, v32\n"
      "v_exp_f32 v33, v33\n"
      "v_smfmac_f32_32x32x32_bf16 v[112:127], a[36:39], v[24:31], v144\n"
      "v_exp_f32 v34, v34\n"
      "v_exp_f32 v35, v35\n"
      "v_exp_f32 v36, v36\n"
      "v_smfmac_f32_32x32x32_bf16 v[128:143], a[36:39], v[56:63], v144\n"
      "ds_read_b128 a[36:39], v145 offset:64512\n"
      "v_exp_f32 v37, v37\n"
      "v_exp_f32 v38, v38\n"
      "v_exp_f32 v39, v39\n"
      "v_smfmac_f32_16x16x64_bf16 a[0:3], a[40:43], v[24:31], v144\n"
      "v_exp_f32 v64, v64\n"
      "v_smfmac_f32_16x16x64_bf16 a[4:7], a[40:43], v[56:63], v144\n"
      "v_exp_f32 v65, v65\n"
      "v_exp_f32 v66, v66\n"
      "v_mfma_f32_32x32x16_bf16 v[16:31], a[24:27], a[8:11], v[80:95]\n"
      "v_exp_f32 v67, v67\n"
      "v_exp_f32 v68, v68\n"
      "v_exp_f32 v69, v69\n"
      "v_mfma_f32_32x32x16_bf16 v[48:63], a[24:27], a[16:19], v[96:111]\n"
      "ds_read_b128 a[24:27], v145 offset:4096\n"
      "v_exp_f32 v70, v70\n"
      "v_exp_f32 v71, v71\n"
      "v_exp_f32 v40, v40\n"
      "v_mfma_f32_32x32x16_bf16 v[16:31], a[28:31], a[12:15], v[16:31]\n"
      "v_exp_f32 v41, v41\n"
      "v_exp_f32 v42, v42\n"
      "v_exp_f32 v43, v43\n"
      "v_mfma_f32_32x32x16_bf16 v[48:63], a[28:31], a[20:23], v[48:63]\n"
      "ds_read_b128 a[28:31], v145 offset:5120\n"
      "v_exp_f32 v44, v44\n"
      "v_exp_f32 v45, v45\n"
      "v_exp_f32 v46, v46\n"
      "v_smfmac_f32_32x32x32_bf16 v[112:127], a[32:35], v[32:39], v144\n"
      "v_exp_f32 v47, v47\n"
      "v_exp_f32 v72, v72\n"
      "v_exp_f32 v73, v73\n"
      "v_smfmac_f32_32x32x32_bf16 v[128:143], a[32:35], v[64:71], v144\n"
      "ds_read_b128 a[32:35], v145 offset:2048\n"
      "v_exp_f32 v74, v74\n"
      "v_exp_f32 v75, v75\n"
      "v_exp_f32 v76, v76\n"
      "v_smfmac_f32_16x16x64_bf16 a[0:3], a[40:43], v[32:39], v144\n"
      "v_exp_f32 v77, v77\n"
      "v_exp_f32 v78, v78\n"
      "v_smfmac_f32_16x16x64_bf16 a[4:7], a[40:43], v[64:71], v144\n"
      "v_exp_f32 v79, v79\n"
      "s_waitcnt lgkmcnt(0)\n"
      "v_exp_f32 v16, v16\n"
      "v_exp_f32 v17, v17\n"
      "v_smfmac_f32_32x32x32_bf16 v[112:127], a[36:39], v[40:47], v144\n"
      "v_exp_f32 v18, v18\n"
      "v_exp_f32 v19, v19\n"
      "v_exp_f32 v20, v20\n"
      "v_smfmac_f32_32x32x32_bf16 v[128:143], a[36:39], v[72:79], v144\n"
      "ds_read_b128 a[36:39], v145 offset:3072\n"
      "v_exp_f32 v21, v21\n"
      "v_exp_f32 v22, v22\n"
      "v_exp_f32 v23, v23\n"
      "v_smfmac_f32_16x16x64_bf16 a[0:3], a[40:43], v[40:47], v144\n"
      "v_exp_f32 v48, v48\n"
      "v_smfmac_f32_16x16x64_bf16 a[4:7], a[40:43], v[72:79], v144\n"
      "v_exp_f32 v49, v49\n"
      "v_exp_f32 v50, v50\n"
      "v_mfma_f32_32x32x16_bf16 v[32:47], a[24:27], a[8:11], v[80:95]\n"
      "v_exp_f32 v51, v51\n"
      "v_exp_f32 v52, v52\n"
      "v_exp_f32 v53, v53\n"
      "v_mfma_f32_32x32x16_bf16 v[64:79], a[24:27], a[16:19], v[96:111]\n"
      "ds_read_b128 a[24:27], v145 offset:8192\n"
      "v_exp_f32 v54, v54\n"
      "v_exp_f32 v55, v55\n"
      "v_exp_f32 v24, v24\n"
      "v_mfma_f32_32x32x16_bf16 v[32:47], a[28:31], a[12:15], v[32:47]\n"
      "v_exp_f32 v25, v25\n"
      "v_exp_f32 v26, v26\n"
      "v_exp_f32 v27, v27\n"
      "v_mfma_f32_32x32x16_bf16 v[64:79], a[28:31], a[20:23], v[64:79]\n"
      "ds_read_b128 a[28:31], v145 offset:9216\n"
      "v_exp_f32 v28, v28\n"
      "v_exp_f32 v29, v29\n"
      "v_exp_f32 v30, v30\n"
      "v_smfmac_f32_32x32x32_bf16 v[112:127], a[32:35], v[16:23], v144\n"
      "v_exp_f32 v31, v31\n"
      "v_exp_f32 v56, v56\n"
      "v_exp_f32 v57, v57\n"
      "v_smfmac_f32_32x32x32_bf16 v[128:143], a[32:35], v[48:55], v144\n"
      "ds_read_b128 a[32:35], v145 offset:6144\n"
      "v_exp_f32 v58, v58\n"
      "v_exp_f32 v59, v59\n"
      "v_exp_f32 v60, v60\n"
      "v_smfmac_f32_16x16x64_bf16 a[0:3], a[40:43], v[16:23], v144\n"
      "v_exp_f32 v61, v61\n"
      "v_exp_f32 v62, v62\n"
      "v_smfmac_f32_16x16x64_bf16 a[4:7], a[40:43], v[48:55], v144\n"
      "v_exp_f32 v63, v63\n"
      "s_waitcnt lgkmcnt(0)\n"
      "v_exp_f32 v32, v32\n"
      "v_exp_f32 v33, v33\n"
      "v_smfmac_f32_32x32x32_bf16 v[112:127], a[36:39], v[24:31], v144\n"
      "v_exp_f32 v34, v34\n"
      "v_exp_f32 v35, v35\n"
      "v_exp_f32 v36, v36\n"
      "v_smfmac_f32_32x32x32_bf16 v[128:143], a[36:39], v[56:63], v144\n"
      "ds_read_b128 a[36:39], v145 offset:7168\n"
      "v_exp_f32 v37, v37\n"
      "v_exp_f32 v38, v38\n"
      "v_exp_f32 v39, v39\n"
      "v_smfmac_f32_16x16x64_bf16 a[0:3], a[40:43], v[24:31], v144\n"
      "v_exp_f32 v64, v64\n"
      "v_smfmac_f32_16x16x64_bf16 a[4:7], a[40:43], v[56:63], v144\n"
      "v_exp_f32 v65, v65\n"
      "v_exp_f32 v66, v66\n"
      "v_mfma_f32_32x32x16_bf16 v[16:31], a[24:27], a[8:11], v[80:95]\n"
      "v_exp_f32 v67, v67\n"
      "v_exp_f32 v68, v68\n"
      "v_exp_f32 v69, v69\n"
      "v_mfma_f32_32x32x16_bf16 v[48:63], a[24:27], a[16:19], v[96:111]\n"
      "ds_read_b128 a[24:27], v145 offset:12288\n"
      "v_exp_f32 v70, v70\n"
      "v_exp_f32 v71, v71\n"
      "v_exp_f32 v40, v40\n"
      "v_mfma_f32_32x32x16_bf16 v[16:31], a[28:31], a[12:15], v[16:31]\n"
      "v_exp_f32 v41, v41\n"
      "v_exp_f32 v42, v42\n"
      "v_exp_f32 v43, v43\n"
      "v_mfma_f32_32x32x16_bf16 v[48:63], a[28:31], a[20:23], v[48:63]\n"
      "ds_read_b128 a[28:31], v145 offset:13312\n"
      "v_exp_f32 v44, v44\n"
      "v_exp_f32 v45, v45\n"
      "v_exp_f32 v46, v46\n"
      "v_smfmac_f32_32x32x32_bf16 v[112:127], a[32:35], v[32:39], v144\n"
      "v_exp_f32 v47, v47\n"
      "v_exp_f32 v72, v72\n"
      "v_exp_f32 v73, v73\n"
      "v_smfmac_f32_32x32x32_bf16 v[128:143], a[32:35], v[64:71], v144\n"
      "ds_read_b128 a[32:35], v145 offset:10240\n"
      "v_exp_f32 v74, v74\n"
      "v_exp_f32 v75, v75\n"
      "v_exp_f32 v76, v76\n"
      "v_smfmac_f32_16x16x64_bf16 a[0:3], a[40:43], v[32:39], v144\n"
      "v_exp_f32 v77, v77\n"
      "v_exp_f32 v78, v78\n"
      "v_smfmac_f32_16x16x64_bf16 a[4:7], a[40:43], v[64:71], v144\n"
      "v_exp_f32 v79, v79\n"
      "s_waitcnt lgkmcnt(0)\n"
      "s_barrier\n"
      "v_exp_f32 v16, v16\n"
      "v_exp_f32 v17, v17\n"
      "v_smfmac_f32_32x32x32_bf16 v[112:127], a[36:39], v[40:47], v144\n"
      "v_exp_f32 v18, v18\n"
      "v_exp_f32 v19, v19\n"
      "v_exp_f32 v20, v20\n"
      "v_smfmac_f32_32x32x32_bf16 v[128:143], a[36:39], v[72:79], v144\n"
      "ds_read_b128 a[36:39], v145 offset:11264\n"
      "v_exp_f32 v21, v21\n"
      "v_exp_f32 v22, v22\n"
      "v_exp_f32 v23, v23\n"
      "v_smfmac_f32_16x16x64_bf16 a[0:3], a[40:43], v[40:47], v144\n"
      "v_exp_f32 v48, v48\n"
      "v_smfmac_f32_16x16x64_bf16 a[4:7], a[40:43], v[72:79], v144\n"
      "v_exp_f32 v49, v49\n"
      "v_exp_f32 v50, v50\n"
      "v_mfma_f32_32x32x16_bf16 v[32:47], a[24:27], a[8:11], v[80:95]\n"
      "v_exp_f32 v51, v51\n"
      "v_exp_f32 v52, v52\n"
      "v_exp_f32 v53, v53\n"
      "v_mfma_f32_32x32x16_bf16 v[64:79], a[24:27], a[16:19], v[96:111]\n"
      "ds_read_b128 a[24:27], v145 offset:16384\n"
      "v_exp_f32 v54, v54\n"
      "v_exp_f32 v55, v55\n"
      "v_exp_f32 v24, v24\n"
      "v_mfma_f32_32x32x16_bf16 v[32:47], a[28:31], a[12:15], v[32:47]\n"
      "v_exp_f32 v25, v25\n"
      "v_exp_f32 v26, v26\n"
      "v_exp_f32 v27, v27\n"
      "v_mfma_f32_32x32x16_bf16 v[64:79], a[28:31], a[20:23], v[64:79]\n"
      "ds_read_b128 a[28:31], v145 offset:17408\n"
      "v_exp_f32 v28, v28\n"
      "v_exp_f32 v29, v29\n"
      "v_exp_f32 v30, v30\n"
      "v_smfmac_f32_32x32x32_bf16 v[112:127], a[32:35], v[16:23], v144\n"
      "v_exp_f32 v31, v31\n"
      "v_exp_f32 v56, v56\n"
      "v_exp_f32 v57, v57\n"
      "v_smfmac_f32_32x32x32_bf16 v[128:143], a[32:35], v[48:55], v144\n"
      "ds_read_b128 a[32:35], v145 offset:14336\n"
      "v_exp_f32 v58, v58\n"
      "v_exp_f32 v59, v59\n"
      "v_exp_f32 v60, v60\n"
      "v_smfmac_f32_16x16x64_bf16 a[0:3], a[40:43], v[16:23], v144\n"
      "v_exp_f32 v61, v61\n"
      "v_exp_f32 v62, v62\n"
      "v_smfmac_f32_16x16x64_bf16 a[4:7], a[40:43], v[48:55], v144\n"
      "v_exp_f32 v63, v63\n"
      "s_waitcnt lgkmcnt(0)\n"
      "v_exp_f32 v32, v32\n"
      "v_exp_f32 v33, v33\n"
      "v_smfmac_f32_32x32x32_bf16 v[112:127], a[36:39], v[24:31], v144\n"
      "v_exp_f32 v34, v34\n"
      "v_exp_f32 v35, v35\n"
      "v_exp_f32 v36, v36\n"
      "v_smfmac_f32_32x32x32_bf16 v[128:143], a[36:39], v[56:63], v144\n"
      "ds_read_b128 a[36:39], v145 offset:15360\n"
      "v_exp_f32 v37, v37\n"
      "v_exp_f32 v38, v38\n"
      "v_exp_f32 v39, v39\n"
      "v_smfmac_f32_16x16x64_bf16 a[0:3], a[40:43], v[24:31], v144\n"
      "v_exp_f32 v64, v64\n"
      "v_smfmac_f32_16x16x64_bf16 a[4:7], a[40:43], v[56:63], v144\n"
      "v_exp_f32 v65, v65\n"
      "v_exp_f32 v66, v66\n"
      "v_mfma_f32_32x32x16_bf16 v[16:31], a[24:27], a[8:11], v[80:95]\n"
      "v_exp_f32 v67, v67\n"
      "v_exp_f32 v68, v68\n"
      "v_exp_f32 v69, v69\n"
      "v_mfma_f32_32x32x16_bf16 v[48:63], a[24:27], a[16:19], v[96:111]\n"
      "ds_read_b128 a[24:27], v145 offset:20480\n"
      "v_exp_f32 v70, v70\n"
      "v_exp_f32 v71, v71\n"
      "v_exp_f32 v40, v40\n"
      "v_mfma_f32_32x32x16_bf16 v[16:31], a[28:31], a[12:15], v[16:31]\n"
      "v_exp_f32 v41, v41\n"
      "v_exp_f32 v42, v42\n"
      "v_exp_f32 v43, v43\n"
      "v_mfma_f32_32x32x16_bf16 v[48:63], a[28:31], a[20:23], v[48:63]\n"
      "ds_read_b128 a[28:31], v145 offset:21504\n"
      "v_exp_f32 v44, v44\n"
      "v_exp_f32 v45, v45\n"
      "v_exp_f32 v46, v46\n"
      "v_smfmac_f32_32x32x32_bf16 v[112:127], a[32:35], v[32:39], v144\n"
      "v_exp_f32 v47, v47\n"
      "v_exp_f32 v72, v72\n"
      "v_exp_f32 v73, v73\n"
      "v_smfmac_f32_32x32x32_bf16 v[128:143], a[32:35], v[64:71], v144\n"
      "ds_read_b128 a[32:35], v145 offset:18432\n"
      "v_exp_f32 v74, v74\n"
      "v_exp_f32 v75, v75\n"
      "v_exp_f32 v76, v76\n"
      "v_smfmac_f32_16x16x64_bf16 a[0:3], a[40:43], v[32:39], v144\n"
      "v_exp_f32 v77, v77\n"
      "v_exp_f32 v78, v78\n"
      "v_smfmac_f32_16x16x64_bf16 a[4:7], a[40:43], v[64:71], v144\n"
      "v_exp_f32 v79, v79\n"
      "s_waitcnt lgkmcnt(0)\n"
      "v_exp_f32 v16, v16\n"
      "v_exp_f32 v17, v17\n"
      "v_smfmac_f32_32x32x32_bf16 v[112:127], a[36:39], v[40:47], v144\n"
      "v_exp_f32 v18, v18\n"
      "v_exp_f32 v19, v19\n"
      "v_exp_f32 v20, v20\n"
      "v_smfmac_f32_32x32x32_bf16 v[128:143], a[36:39], v[72:79], v144\n"
      "ds_read_b128 a[36:39], v145 offset:19456\n"
      "v_exp_f32 v21, v21\n"
      "v_exp_f32 v22, v22\n"
      "v_exp_f32 v23, v23\n"
      "v_smfmac_f32_16x16x64_bf16 a[0:3], a[40:43], v[40:47], v144\n"
      "v_exp_f32 v48, v48\n"
      "v_smfmac_f32_16x16x64_bf16 a[4:7], a[40:43], v[72:79], v144\n"
      "v_exp_f32 v49, v49\n"
      "v_exp_f32 v50, v50\n"
      "v_mfma_f32_32x32x16_bf16 v[32:47], a[24:27], a[8:11], v[80:95]\n"
      "v_exp_f32 v51, v51\n"
      "v_exp_f32 v52, v52\n"
      "v_exp_f32 v53, v53\n"
      "v_mfma_f32_32x32x16_bf16 v[64:79], a[24:27], a[16:19], v[96:111]\n"
      "ds_read_b128 a[24:27], v145 offset:24576\n"
      "v_exp_f32 v54, v54\n"
      "v_exp_f32 v55, v55\n"
      "v_exp_f32 v24, v24\n"
      "v_mfma_f32_32x32x16_bf16 v[32:47], a[28:31], a[12:15], v[32:47]\n"
      "v_exp_f32 v25, v25\n"
      "v_exp_f32 v26, v26\n"
      "v_exp_f32 v27, v27\n"
      "v_mfma_f32_32x32x16_bf16 v[64:79], a[28:31], a[20:23], v[64:79]\n"
      "ds_read_b128 a[28:31], v145 offset:25600\n"
      "v_exp_f32 v28, v28\n"
      "v_exp_f32 v29, v29\n"
      "v_exp_f32 v30, v30\n"
      "v_smfmac_f32_32x32x32_bf16 v[112:127], a[32:35], v[16:23], v144\n"
      "v_exp_f32 v31, v31\n"
      "v_exp_f32 v56, v56\n"
      "v_exp_f32 v57, v57\n"
      "v_smfmac_f32_32x32x32_bf16 v[128:143], a[32:35], v[48:55], v144\n"
      "ds_read_b128 a[32:35], v145 offset:22528\n"
      "v_exp_f32 v58, v58\n"
      "v_exp_f32 v59, v59\n"
      "v_exp_f32 v60, v60\n"
      "v_smfmac_f32_16x16x64_bf16 a[0:3], a[40:43], v[16:23], v144\n"
      "v_exp_f32 v61, v61\n"
      "v_exp_f32 v62, v62\n"
      "v_smfmac_f32_16x16x64_bf16 a[4:7], a[40:43], v[48:55], v144\n"
      "v_exp_f32 v63, v63\n"
      "s_waitcnt lgkmcnt(0)\n"
      "v_exp_f32 v32, v32\n"
      "v_exp_f32 v33, v33\n"
      "v_smfmac_f32_32x32x32_bf16 v[112:127], a[36:39], v[24:31], v144\n"
      "v_exp_f32 v34, v34\n"
      "v_exp_f32 v35, v35\n"
      "v_exp_f32 v36, v36\n"
      "v_smfmac_f32_32x32x32_bf16 v[128:143], a[36:39], v[56:63], v144\n"
      "ds_read_b128 a[36:39], v145 offset:23552\n"
      "v_exp_f32 v37, v37\n"
      "v_exp_f32 v38, v38\n"
      "v_exp_f32 v39, v39\n"
      "v_smfmac_f32_16x16x64_bf16 a[0:3], a[40:43], v[24:31], v144\n"
      "v_exp_f32 v64, v64\n"
      "v_smfmac_f32_16x16x64_bf16 a[4:7], a[40:43], v[56:63], v144\n"
      "v_exp_f32 v65, v65\n"
      "v_exp_f32 v66, v66\n"
      "v_mfma_f32_32x32x16_bf16 v[16:31], a[24:27], a[8:11], v[80:95]\n"
      "v_exp_f32 v67, v67\n"
      "v_exp_f32 v68, v68\n"
      "v_exp_f32 v69, v69\n"
      "v_mfma_f32_32x32x16_bf16 v[48:63], a[24:27], a[16:19], v[96:111]\n"
      "ds_read_b128 a[24:27], v145 offset:28672\n"
      "v_exp_f32 v70, v70\n"
      "v_exp_f32 v71, v71\n"
      "v_exp_f32 v40, v40\n"
      "v_mfma_f32_32x32x16_bf16 v[16:31], a[28:31], a[12:15], v[16:31]\n"
      "v_exp_f32 v41, v41\n"
      "v_exp_f32 v42, v42\n"
      "v_exp_f32 v43, v43\n"
      "v_mfma_f32_32x32x16_bf16 v[48:63], a[28:31], a[20:23], v[48:63]\n"
      "ds_read_b128 a[28:31], v145 offset:29696\n"
      "v_exp_f32 v44, v44\n"
      "v_exp_f32 v45, v45\n"
      "v_exp_f32 v46, v46\n"
      "v_smfmac_f32_32x32x32_bf16 v[112:127], a[32:35], v[32:39], v144\n"
      "v_exp_f32 v47, v47\n"
      "v_exp_f32 v72, v72\n"
      "v_exp_f32 v73, v73\n"
      "v_smfmac_f32_32x32x32_bf16 v[128:143], a[32:35], v[64:71], v144\n"
      "ds_read_b128 a[32:35], v145 offset:26624\n"
      "v_exp_f32 v74, v74\n"
      "v_exp_f32 v75, v75\n"
      "v_exp_f32 v76, v76\n"
      "v_smfmac_f32_16x16x64_bf16 a[0:3], a[40:43], v[32:39], v144\n"
      "v_exp_f32 v77, v77\n"
      "v_exp_f32 v78, v78\n"
      "v_smfmac_f32_16x16x64_bf16 a[4:7], a[40:43], v[64:71], v144\n"
      "v_exp_f32 v79, v79\n"
      "s_waitcnt lgkmcnt(0)\n"
      "s_barrier\n"
      "v_exp_f32 v16, v16\n"
      "v_exp_f32 v17, v17\n"
      "v_smfmac_f32_32x32x32_bf16 v[112:127], a[36:39], v[40:47], v144\n"
      "v_exp_f32 v18, v18\n"
      "v_exp_f32 v19, v19\n"
      "v_exp_f32 v20, v20\n"
      "v_smfmac_f32_32x32x32_bf16 v[128:143], a[36:39], v[72:79], v144\n"
      "ds_read_b128 a[36:39], v145 offset:27648\n"
      "v_exp_f32 v21, v21\n"
      "v_exp_f32 v22, v22\n"
      "v_exp_f32 v23, v23\n"
      "v_smfmac_f32_16x16x64_bf16 a[0:3], a[40:43], v[40:47], v144\n"
      "v_exp_f32 v48, v48\n"
      "v_smfmac_f32_16x16x64_bf16 a[4:7], a[40:43], v[72:79], v144\n"
      "v_exp_f32 v49, v49\n"
      "v_exp_f32 v50, v50\n"
      "v_mfma_f32_32x32x16_bf16 v[32:47], a[24:27], a[8:11], v[80:95]\n"
      "v_exp_f32 v51, v51\n"
      "v_exp_f32 v52, v52\n"
      "v_exp_f32 v53, v53\n"
      "v_mfma_f32_32x32x16_bf16 v[64:79], a[24:27], a[16:19], v[96:111]\n"
      "ds_read_b128 a[24:27], v145 offset:32768\n"
      "v_exp_f32 v54, v54\n"
      "v_exp_f32 v55, v55\n"
      "v_exp_f32 v24, v24\n"
      "v_mfma_f32_32x32x16_bf16 v[32:47], a[28:31], a[12:15], v[32:47]\n"
      "v_exp_f32 v25, v25\n"
      "v_exp_f32 v26, v26\n"
      "v_exp_f32 v27, v27\n"
      "v_mfma_f32_32x32x16_bf16 v[64:79], a[28:31], a[20:23], v[64:79]\n"
      "ds_read_b128 a[28:31], v145 offset:33792\n"
      "v_exp_f32 v28, v28\n"
      "v_exp_f32 v29, v29\n"
      "v_exp_f32 v30, v30\n"
      "v_smfmac_f32_32x32x32_bf16 v[112:127], a[32:35], v[16:23], v144\n"
      "v_exp_f32 v31, v31\n"
      "v_exp_f32 v56, v56\n"
      "v_exp_f32 v57, v57\n"
      "v_smfmac_f32_32x32x32_bf16 v[128:143], a[32:35], v[48:55], v144\n"
      "ds_read_b128 a[32:35], v145 offset:30720\n"
      "v_exp_f32 v58, v58\n"
      "v_exp_f32 v59, v59\n"
      "v_exp_f32 v60, v60\n"
      "v_smfmac_f32_16x16x64_bf16 a[0:3], a[40:43], v[16:23], v144\n"
      "v_exp_f32 v61, v61\n"
      "v_exp_f32 v62, v62\n"
      "v_smfmac_f32_16x16x64_bf16 a[4:7], a[40:43], v[48:55], v144\n"
      "v_exp_f32 v63, v63\n"
      "s_waitcnt lgkmcnt(0)\n"
      "v_exp_f32 v32, v32\n"
      "v_exp_f32 v33, v33\n"
      "v_smfmac_f32_32x32x32_bf16 v[112:127], a[36:39], v[24:31], v144\n"
      "v_exp_f32 v34, v34\n"
      "v_exp_f32 v35, v35\n"
      "v_exp_f32 v36, v36\n"
      "v_smfmac_f32_32x32x32_bf16 v[128:143], a[36:39], v[56:63], v144\n"
      "ds_read_b128 a[36:39], v145 offset:31744\n"
      "v_exp_f32 v37, v37\n"
      "v_exp_f32 v38, v38\n"
      "v_exp_f32 v39, v39\n"
      "v_smfmac_f32_16x16x64_bf16 a[0:3], a[40:43], v[24:31], v144\n"
      "v_exp_f32 v64, v64\n"
      "v_smfmac_f32_16x16x64_bf16 a[4:7], a[40:43], v[56:63], v144\n"
      "v_exp_f32 v65, v65\n"
      "v_exp_f32 v66, v66\n"
      "v_mfma_f32_32x32x16_bf16 v[16:31], a[24:27], a[8:11], v[80:95]\n"
      "v_exp_f32 v67, v67\n"
      "v_exp_f32 v68, v68\n"
      "v_exp_f32 v69, v69\n"
      "v_mfma_f32_32x32x16_bf16 v[48:63], a[24:27], a[16:19], v[96:111]\n"
      "ds_read_b128 a[24:27], v145 offset:36864\n"
      "v_exp_f32 v70, v70\n"
      "v_exp_f32 v71, v71\n"
      "v_exp_f32 v40, v40\n"
      "v_mfma_f32_32x32x16_bf16 v[16:31], a[28:31], a[12:15], v[16:31]\n"
      "v_exp_f32 v41, v41\n"
      "v_exp_f32 v42, v42\n"
      "v_exp_f32 v43, v43\n"
      "v_mfma_f32_32x32x16_bf16 v[48:63], a[28:31], a[20:23], v[48:63]\n"
      "ds_read_b128 a[28:31], v145 offset:37888\n"
      "v_exp_f32 v44, v44\n"
      "v_exp_f32 v45, v45\n"
      "v_exp_f32 v46, v46\n"
      "v_smfmac_f32_32x32x32_bf16 v[112:127], a[32:35], v[32:39], v144\n"
      "v_exp_f32 v47, v47\n"
      "v_exp_f32 v72, v72\n"
      "v_exp_f32 v73, v73\n"
      "v_smfmac_f32_32x32x32_bf16 v[128:143], a[32:35], v[64:71], v144\n"
      "ds_read_b128 a[32:35], v145 offset:34816\n"
      "v_exp_f32 v74, v74\n"
      "v_exp_f32 v75, v75\n"
      "v_exp_f32 v76, v76\n"
      "v_smfmac_f32_16x16x64_bf16 a[0:3], a[40:43], v[32:39], v144\n"
      "v_exp_f32 v77, v77\n"
      "v_exp_f32 v78, v78\n"
      "v_smfmac_f32_16x16x64_bf16 a[4:7], a[40:43], v[64:71], v144\n"
      "v_exp_f32 v79, v79\n"
      "s_waitcnt lgkmcnt(0)\n"
      "v_exp_f32 v16, v16\n"
      "v_exp_f32 v17, v17\n"
      "v_smfmac_f32_32x32x32_bf16 v[112:127], a[36:39], v[40:47], v144\n"
      "v_exp_f32 v18, v18\n"
      "v_exp_f32 v19, v19\n"
      "v_exp_f32 v20, v20\n"
      "v_smfmac_f32_32x32x32_bf16 v[128:143], a[36:39], v[72:79], v144\n"
      "ds_read_b128 a[36:39], v145 offset:35840\n"
      "v_exp_f32 v21, v21\n"
      "v_exp_f32 v22, v22\n"
      "v_exp_f32 v23, v23\n"
      "v_smfmac_f32_16x16x64_bf16 a[0:3], a[40:43], v[40:47], v144\n"
      "v_exp_f32 v48, v48\n"
      "v_smfmac_f32_16x16x64_bf16 a[4:7], a[40:43], v[72:79], v144\n"
      "v_exp_f32 v49, v49\n"
      "v_exp_f32 v50, v50\n"
      "v_mfma_f32_32x32x16_bf16 v[32:47], a[24:27], a[8:11], v[80:95]\n"
      "v_exp_f32 v51, v51\n"
      "v_exp_f32 v52, v52\n"
      "v_exp_f32 v53, v53\n"
      "v_mfma_f32_32x32x16_bf16 v[64:79], a[24:27], a[16:19], v[96:111]\n"
      "ds_read_b128 a[24:27], v145 offset:40960\n"
      "v_exp_f32 v54, v54\n"
      "v_exp_f32 v55, v55\n"
      "v_exp_f32 v24, v24\n"
      "v_mfma_f32_32x32x16_bf16 v[32:47], a[28:31], a[12:15], v[32:47]\n"
      "v_exp_f32 v25, v25\n"
      "v_exp_f32 v26, v26\n"
      "v_exp_f32 v27, v27\n"
      "v_mfma_f32_32x32x16_bf16 v[64:79], a[28:31], a[20:23], v[64:79]\n"
      "ds_read_b128 a[28:31], v145 offset:41984\n"
      "v_exp_f32 v28, v28\n"
      "v_exp_f32 v29, v29\n"
      "v_exp_f32 v30, v30\n"
      "v_smfmac_f32_32x32x32_bf16 v[112:127], a[32:35], v[16:23], v144\n"
      "v_exp_f32 v31, v31\n"
      "v_exp_f32 v56, v56\n"
      "v_exp_f32 v57, v57\n"
      "v_smfmac_f32_32x32x32_bf16 v[128:143], a[32:35], v[48:55], v144\n"
      "ds_read_b128 a[32:35], v145 offset:38912\n"
      "v_exp_f32 v58, v58\n"
      "v_exp_f32 v59, v59\n"
      "v_exp_f32 v60, v60\n"
      "v_smfmac_f32_16x16x64_bf16 a[0:3], a[40:43], v[16:23], v144\n"
      "v_exp_f32 v61, v61\n"
      "v_exp_f32 v62, v62\n"
      "v_smfmac_f32_16x16x64_bf16 a[4:7], a[40:43], v[48:55], v144\n"
      "v_exp_f32 v63, v63\n"
      "s_waitcnt lgkmcnt(0)\n"
      "v_exp_f32 v32, v32\n"
      "v_exp_f32 v33, v33\n"
      "v_smfmac_f32_32x32x32_bf16 v[112:127], a[36:39], v[24:31], v144\n"
      "v_exp_f32 v34, v34\n"
      "v_exp_f32 v35, v35\n"
      "v_exp_f32 v36, v36\n"
      "v_smfmac_f32_32x32x32_bf16 v[128:143], a[36:39], v[56:63], v144\n"
      "ds_read_b128 a[36:39], v145 offset:39936\n"
      "v_exp_f32 v37, v37\n"
      "v_exp_f32 v38, v38\n"
      "v_exp_f32 v39, v39\n"
      "v_smfmac_f32_16x16x64_bf16 a[0:3], a[40:43], v[24:31], v144\n"
      "v_exp_f32 v64, v64\n"
      "v_smfmac_f32_16x16x64_bf16 a[4:7], a[40:43], v[56:63], v144\n"
      "v_exp_f32 v65, v65\n"
      "v_exp_f32 v66, v66\n"
      "v_mfma_f32_32x32x16_bf16 v[16:31], a[24:27], a[8:11], v[80:95]\n"
      "v_exp_f32 v67, v67\n"
      "v_exp_f32 v68, v68\n"
      "v_exp_f32 v69, v69\n"
      "v_mfma_f32_32x32x16_bf16 v[48:63], a[24:27], a[16:19], v[96:111]\n"
      "ds_read_b128 a[24:27], v145 offset:45056\n"
      "v_exp_f32 v70, v70\n"
      "v_exp_f32 v71, v71\n"
      "v_exp_f32 v40, v40\n"
      "v_mfma_f32_32x32x16_bf16 v[16:31], a[28:31], a[12:15], v[16:31]\n"
      "v_exp_f32 v41, v41\n"
      "v_exp_f32 v42, v42\n"
      "v_exp_f32 v43, v43\n"
      "v_mfma_f32_32x32x16_bf16 v[48:63], a[28:31], a[20:23], v[48:63]\n"
      "ds_read_b128 a[28:31], v145 offset:46080\n"
      "v_exp_f32 v44, v44\n"
      "v_exp_f32 v45, v45\n"
      "v_exp_f32 v46, v46\n"
      "v_smfmac_f32_32x32x32_bf16 v[112:127], a[32:35], v[32:39], v144\n"
      "v_exp_f32 v47, v47\n"
      "v_exp_f32 v72, v72\n"
      "v_exp_f32 v73, v73\n"
      "v_smfmac_f32_32x32x32_bf16 v[128:143], a[32:35], v[64:71], v144\n"
      "ds_read_b128 a[32:35], v145 offset:43008\n"
      "v_exp_f32 v74, v74\n"
      "v_exp_f32 v75, v75\n"
      "v_exp_f32 v76, v76\n"
      "v_smfmac_f32_16x16x64_bf16 a[0:3], a[40:43], v[32:39], v144\n"
      "v_exp_f32 v77, v77\n"
      "v_exp_f32 v78, v78\n"
      "v_smfmac_f32_16x16x64_bf16 a[4:7], a[40:43], v[64:71], v144\n"
      "v_exp_f32 v79, v79\n"
      "s_waitcnt lgkmcnt(0)\n"
      "s_barrier\n"
      "v_exp_f32 v16, v16\n"
      "v_exp_f32 v17, v17\n"
      "v_smfmac_f32_32x32x32_bf16 v[112:127], a[36:39], v[40:47], v144\n"
      "v_exp_f32 v18, v18\n"
      "v_exp_f32 v19, v19\n"
      "v_exp_f32 v20, v20\n"
      "v_smfmac_f32_32x32x32_bf16 v[128:143], a[36:39], v[72:79], v144\n"
      "ds_read_b128 a[36:39], v145 offset:44032\n"
      "v_exp_f32 v21, v21\n"
      "v_exp_f32 v22, v22\n"
      "v_exp_f32 v23, v23\n"
      "v_smfmac_f32_16x16x64_bf16 a[0:3], a[40:43], v[40:47], v144\n"
      "v_exp_f32 v48, v48\n"
      "v_smfmac_f32_16x16x64_bf16 a[4:7], a[40:43], v[72:79], v144\n"
      "v_exp_f32 v49, v49\n"
      "v_exp_f32 v50, v50\n"
      "v_mfma_f32_32x32x16_bf16 v[32:47], a[24:27], a[8:11], v[80:95]\n"
      "v_exp_f32 v51, v51\n"
      "v_exp_f32 v52, v52\n"
      "v_exp_f32 v53, v53\n"
      "v_mfma_f32_32x32x16_bf16 v[64:79], a[24:27], a[16:19], v[96:111]\n"
      "ds_read_b128 a[24:27], v145 offset:49152\n"
      "v_exp_f32 v54, v54\n"
      "v_exp_f32 v55, v55\n"
      "v_exp_f32 v24, v24\n"
      "v_mfma_f32_32x32x16_bf16 v[32:47], a[28:31], a[12:15], v[32:47]\n"
      "v_exp_f32 v25, v25\n"
      "v_exp_f32 v26, v26\n"
      "v_exp_f32 v27, v27\n"
      "v_mfma_f32_32x32x16_bf16 v[64:79], a[28:31], a[20:23], v[64:79]\n"
      "ds_read_b128 a[28:31], v145 offset:50176\n"
      "v_exp_f32 v28, v28\n"
      "v_exp_f32 v29, v29\n"
      "v_exp_f32 v30, v30\n"
      "v_smfmac_f32_32x32x32_bf16 v[112:127], a[32:35], v[16:23], v144\n"
      "v_exp_f32 v31, v31\n"
      "v_exp_f32 v56, v56\n"
      "v_exp_f32 v57, v57\n"
      "v_smfmac_f32_32x32x32_bf16 v[128:143], a[32:35], v[48:55], v144\n"
      "ds_read_b128 a[32:35], v145 offset:47104\n"
      "v_exp_f32 v58, v58\n"
      "v_exp_f32 v59, v59\n"
      "v_exp_f32 v60, v60\n"
      "v_smfmac_f32_16x16x64_bf16 a[0:3], a[40:43], v[16:23], v144\n"
      "v_exp_f32 v61, v61\n"
      "v_exp_f32 v62, v62\n"
      "v_smfmac_f32_16x16x64_bf16 a[4:7], a[40:43], v[48:55], v144\n"
      "v_exp_f32 v63, v63\n"
      "s_waitcnt lgkmcnt(0)\n"
      "v_exp_f32 v32, v32\n"
      "v_exp_f32 v33, v33\n"
      "v_smfmac_f32_32x32x32_bf16 v[112:127], a[36:39], v[24:31], v144\n"
      "v_exp_f32 v34, v34\n"
      "v_exp_f32 v35, v35\n"
      "v_exp_f32 v36, v36\n"
      "v_smfmac_f32_32x32x32_bf16 v[128:143], a[36:39], v[56:63], v144\n"
      "ds_read_b128 a[36:39], v145 offset:48128\n"
      "v_exp_f32 v37, v37\n"
      "v_exp_f32 v38, v38\n"
      "v_exp_f32 v39, v39\n"
      "v_smfmac_f32_16x16x64_bf16 a[0:3], a[40:43], v[24:31], v144\n"
      "v_exp_f32 v64, v64\n"
      "v_smfmac_f32_16x16x64_bf16 a[4:7], a[40:43], v[56:63], v144\n"
      "v_exp_f32 v65, v65\n"
      "v_exp_f32 v66, v66\n"
      "v_mfma_f32_32x32x16_bf16 v[16:31], a[24:27], a[8:11], v[80:95]\n"
      "v_exp_f32 v67, v67\n"
      "v_exp_f32 v68, v68\n"
      "v_exp_f32 v69, v69\n"
      "v_mfma_f32_32x32x16_bf16 v[48:63], a[24:27], a[16:19], v[96:111]\n"
      "ds_read_b128 a[24:27], v145 offset:53248\n"
      "v_exp_f32 v70, v70\n"
      "v_exp_f32 v71, v71\n"
      "v_exp_f32 v40, v40\n"
      "v_mfma_f32_32x32x16_bf16 v[16:31], a[28:31], a[12:15], v[16:31]\n"
      "v_exp_f32 v41, v41\n"
      "v_exp_f32 v42, v42\n"
      "v_exp_f32 v43, v43\n"
      "v_mfma_f32_32x32x16_bf16 v[48:63], a[28:31], a[20:23], v[48:63]\n"
      "ds_read_b128 a[28:31], v145 offset:54272\n"
      "v_exp_f32 v44, v44\n"
      "v_exp_f32 v45, v45\n"
      "v_exp_f32 v46, v46\n"
      "v_smfmac_f32_32x32x32_bf16 v[112:127], a[32:35], v[32:39], v144\n"
      "v_exp_f32 v47, v47\n"
      "v_exp_f32 v72, v72\n"
      "v_exp_f32 v73, v73\n"
      "v_smfmac_f32_32x32x32_bf16 v[128:143], a[32:35], v[64:71], v144\n"
      "ds_read_b128 a[32:35], v145 offset:51200\n"
      "v_exp_f32 v74, v74\n"
      "v_exp_f32 v75, v75\n"
      "v_exp_f32 v76, v76\n"
      "v_smfmac_f32_16x16x64_bf16 a[0:3], a[40:43], v[32:39], v144\n"
      "v_exp_f32 v77, v77\n"
      "v_exp_f32 v78, v78\n"
      "v_smfmac_f32_16x16x64_bf16 a[4:7], a[40:43], v[64:71], v144\n"
      "v_exp_f32 v79, v79\n"
      "s_waitcnt lgkmcnt(0)\n"
      "v_exp_f32 v16, v16\n"
      "v_exp_f32 v17, v17\n"
      "v_smfmac_f32_32x32x32_bf16 v[112:127], a[36:39], v[40:47], v144\n"
      "v_exp_f32 v18, v18\n"
      "v_exp_f32 v19, v19\n"
      "v_exp_f32 v20, v20\n"
      "v_smfmac_f32_32x32x32_bf16 v[128:143], a[36:39], v[72:79], v144\n"
      "ds_read_b128 a[36:39], v145 offset:52224\n"
      "v_exp_f32 v21, v21\n"
      "v_exp_f32 v22, v22\n"
      "v_exp_f32 v23, v23\n"
      "v_smfmac_f32_16x16x64_bf16 a[0:3], a[40:43], v[40:47], v144\n"
      "v_exp_f32 v48, v48\n"
      "v_smfmac_f32_16x16x64_bf16 a[4:7], a[40:43], v[72:79], v144\n"
      "v_exp_f32 v49, v49\n"
      "v_exp_f32 v50, v50\n"
      "v_mfma_f32_32x32x16_bf16 v[32:47], a[24:27], a[8:11], v[80:95]\n"
      "v_exp_f32 v51, v51\n"
      "v_exp_f32 v52, v52\n"
      "v_exp_f32 v53, v53\n"
      "v_mfma_f32_32x32x16_bf16 v[64:79], a[24:27], a[16:19], v[96:111]\n"
      "ds_read_b128 a[24:27], v145 offset:57344\n"
      "v_exp_f32 v54, v54\n"
      "v_exp_f32 v55, v55\n"
      "v_exp_f32 v24, v24\n"
      "v_mfma_f32_32x32x16_bf16 v[32:47], a[28:31], a[12:15], v[32:47]\n"
      "v_exp_f32 v25, v25\n"
      "v_exp_f32 v26, v26\n"
      "v_exp_f32 v27, v27\n"
      "v_mfma_f32_32x32x16_bf16 v[64:79], a[28:31], a[20:23], v[64:79]\n"
      "ds_read_b128 a[28:31], v145 offset:58368\n"
      "v_exp_f32 v28, v28\n"
      "v_exp_f32 v29, v29\n"
      "v_exp_f32 v30, v30\n"
      "v_smfmac_f32_32x32x32_bf16 v[112:127], a[32:35], v[16:23], v144\n"
      "v_exp_f32 v31, v31\n"
      "v_exp_f32 v56, v56\n"
      "v_exp_f32 v57, v57\n"
      "v_smfmac_f32_32x32x32_bf16 v[128:143], a[32:35], v[48:55], v144\n"
      "ds_read_b128 a[32:35], v145 offset:55296\n"
      "v_exp_f32 v58, v58\n"
      "v_exp_f32 v59, v59\n"
      "v_exp_f32 v60, v60\n"
      "v_smfmac_f32_16x16x64_bf16 a[0:3], a[40:43], v[16:23], v144\n"
      "v_exp_f32 v61, v61\n"
      "v_exp_f32 v62, v62\n"
      "v_smfmac_f32_16x16x64_bf16 a[4:7], a[40:43], v[48:55], v144\n"
      "v_exp_f32 v63, v63\n"
      "s_waitcnt lgkmcnt(0)\n"
      "v_exp_f32 v32, v32\n"
      "v_exp_f32 v33, v33\n"
      "v_smfmac_f32_32x32x32_bf16 v[112:127], a[36:39], v[24:31], v144\n"
      "v_exp_f32 v34, v34\n"
      "v_exp_f32 v35, v35\n"
      "v_exp_f32 v36, v36\n"
      "v_smfmac_f32_32x32x32_bf16 v[128:143], a[36:39], v[56:63], v144\n"
      "ds_read_b128 a[36:39], v145 offset:56320\n"
      "v_exp_f32 v37, v37\n"
      "v_exp_f32 v38, v38\n"
      "v_exp_f32 v39, v39\n"
      "v_smfmac_f32_16x16x64_bf16 a[0:3], a[40:43], v[24:31], v144\n"
      "v_exp_f32 v64, v64\n"
      "v_smfmac_f32_16x16x64_bf16 a[4:7], a[40:43], v[56:63], v144\n"
      "v_exp_f32 v65, v65\n"
      "v_exp_f32 v66, v66\n"
      "v_mfma_f32_32x32x16_bf16 v[16:31], a[24:27], a[8:11], v[80:95]\n"
      "v_exp_f32 v67, v67\n"
      "v_exp_f32 v68, v68\n"
      "v_exp_f32 v69, v69\n"
      "v_mfma_f32_32x32x16_bf16 v[48:63], a[24:27], a[16:19], v[96:111]\n"
      "ds_read_b128 a[24:27], v145 offset:61440\n"
      "v_exp_f32 v70, v70\n"
      "v_exp_f32 v71, v71\n"
      "v_exp_f32 v40, v40\n"
      "v_mfma_f32_32x32x16_bf16 v[16:31], a[28:31], a[12:15], v[16:31]\n"
      "v_exp_f32 v41, v41\n"
      "v_exp_f32 v42, v42\n"
      "v_exp_f32 v43, v43\n"
      "v_mfma_f32_32x32x16_bf16 v[48:63], a[28:31], a[20:23], v[48:63]\n"
      "ds_read_b128 a[28:31], v145 offset:62464\n"
      "v_exp_f32 v44, v44\n"
      "v_exp_f32 v45, v45\n"
      "v_exp_f32 v46, v46\n"
      "v_smfmac_f32_32x32x32_bf16 v[112:127], a[32:35], v[32:39], v144\n"
      "v_exp_f32 v47, v47\n"
      "v_exp_f32 v72, v72\n"
      "v_exp_f32 v73, v73\n"
      "v_smfmac_f32_32x32x32_bf16 v[128:143], a[32:35], v[64:71], v144\n"
      "ds_read_b128 a[32:35], v145 offset:59392\n"
      "v_exp_f32 v74, v74\n"
      "v_exp_f32 v75, v75\n"
      "v_exp_f32 v76, v76\n"
      "v_smfmac_f32_16x16x64_bf16 a[0:3], a[40:43], v[32:39], v144\n"
      "v_exp_f32 v77, v77\n"
      "v_exp_f32 v78, v78\n"
      "v_smfmac_f32_16x16x64_bf16 a[4:7], a[40:43], v[64:71], v144\n"
      "v_exp_f32 v79, v79\n"
      "s_waitcnt lgkmcnt(0)\n"
      "s_barrier\n"
      "v_exp_f32 v16, v16\n"
      "v_exp_f32 v17, v17\n"
      "v_smfmac_f32_32x32x32_bf16 v[112:127], a[36:39], v[40:47], v144\n"
      "v_exp_f32 v18, v18\n"
      "v_exp_f32 v19, v19\n"
      "v_exp_f32 v20, v20\n"
      "v_smfmac_f32_32x32x32_bf16 v[128:143], a[36:39], v[72:79], v144\n"
      "ds_read_b128 a[36:39], v145 offset:60416\n"
      "v_exp_f32 v21, v21\n"
      "v_exp_f32 v22, v22\n"
      "v_exp_f32 v23, v23\n"
      "v_smfmac_f32_16x16x64_bf16 a[0:3], a[40:43], v[40:47], v144\n"
      "v_exp_f32 v48, v48\n"
      "v_smfmac_f32_16x16x64_bf16 a[4:7], a[40:43], v[72:79], v144\n"
      "v_exp_f32 v49, v49\n"
      "v_exp_f32 v50, v50\n"
      "v_mfma_f32_32x32x16_bf16 v[32:47], a[24:27], a[8:11], v[80:95]\n"
      "v_exp_f32 v51, v51\n"
      "v_exp_f32 v52, v52\n"
      "v_exp_f32 v53, v53\n"
      "v_mfma_f32_32x32x16_bf16 v[64:79], a[24:27], a[16:19], v[96:111]\n"
      "ds_read_b128 a[24:27], v145 offset:0\n"
      "v_exp_f32 v54, v54\n"
      "v_exp_f32 v55, v55\n"
      "v_exp_f32 v24, v24\n"
      "v_mfma_f32_32x32x16_bf16 v[32:47], a[28:31], a[12:15], v[32:47]\n"
      "v_exp_f32 v25, v25\n"
      "v_exp_f32 v26, v26\n"
      "v_exp_f32 v27, v27\n"
      "v_mfma_f32_32x32x16_bf16 v[64:79], a[28:31], a[20:23], v[64:79]\n"
      "ds_read_b128 a[28:31], v145 offset:1024\n"
      "v_exp_f32 v28, v28\n"
      "v_exp_f32 v29, v29\n"
      "v_exp_f32 v30, v30\n"
      "v_smfmac_f32_32x32x32_bf16 v[112:127], a[32:35], v[16:23], v144\n"
      "v_exp_f32 v31, v31\n"
      "v_exp_f32 v56, v56\n"
      "v_exp_f32 v57, v57\n"
      "v_smfmac_f32_32x32x32_bf16 v[128:143], a[32:35], v[48:55], v144\n"
      "ds_read_b128 a[32:35], v145 offset:63488\n"
      "v_exp_f32 v58, v58\n"
      "v_exp_f32 v59, v59\n"
      "v_exp_f32 v60, v60\n"
      "v_smfmac_f32_16x16x64_bf16 a[0:3], a[40:43], v[16:23], v144\n"
      "v_exp_f32 v61, v61\n"
      "v_exp_f32 v62, v62\n"
      "v_smfmac_f32_16x16x64_bf16 a[4:7], a[40:43], v[48:55], v144\n"
      "v_exp_f32 v63, v63\n"
      "s_waitcnt lgkmcnt(0)\n"
      "v_exp_f32 v32, v32\n"
      "v_exp_f32 v33, v33\n"
      "v_smfmac_f32_32x32x32_bf16 v[112:127], a[36:39], v[24:31], v144\n"
      "v_exp_f32 v34, v34\n"
      "v_exp_f32 v35, v35\n"
      "v_exp_f32 v36, v36\n"
      "v_smfmac_f32_32x32x32_bf16 v[128:143], a[36:39], v[56:63], v144\n"
      "ds_read_b128 a[36:39], v145 offset:64512\n"
      "v_exp_f32 v37, v37\n"
      "v_exp_f32 v38, v38\n"
      "v_exp_f32 v39, v39\n"
      "v_smfmac_f32_16x16x64_bf16 a[0:3], a[40:43], v[24:31], v144\n"
      "v_exp_f32 v64, v64\n"
      "v_smfmac_f32_16x16x64_bf16 a[4:7], a[40:43], v[56:63], v144\n"
      "v_exp_f32 v65, v65\n"
      "v_exp_f32 v66, v66\n"
      "v_mfma_f32_32x32x16_bf16 v[16:31], a[24:27], a[8:11], v[80:95]\n"
      "v_exp_f32 v67, v67\n"
      "v_exp_f32 v68, v68\n"
      "v_exp_f32 v69, v69\n"
      "v_mfma_f32_32x32x16_bf16 v[48:63], a[24:27], a[16:19], v[96:111]\n"
      "ds_read_b128 a[24:27], v145 offset:4096\n"
      "v_exp_f32 v70, v70\n"
      "v_exp_f32 v71, v71\n"
      "v_exp_f32 v40, v40\n"
      "v_mfma_f32_32x32x16_bf16 v[16:31], a[28:31], a[12:15], v[16:31]\n"
      "v_exp_f32 v41, v41\n"
      "v_exp_f32 v42, v42\n"
      "v_exp_f32 v43, v43\n"
      "v_mfma_f32_32x32x16_bf16 v[48:63], a[28:31], a[20:23], v[48:63]\n"
      "ds_read_b128 a[28:31], v145 offset:5120\n"
      "v_exp_f32 v44, v44\n"
      "v_exp_f32 v45, v45\n"
      "v_exp_f32 v46, v46\n"
      "v_smfmac_f32_32x32x32_bf16 v[112:127], a[32:35], v[32:39], v144\n"
      "v_exp_f32 v47, v47\n"
      "v_exp_f32 v72, v72\n"
      "v_exp_f32 v73, v73\n"
      "v_smfmac_f32_32x32x32_bf16 v[128:143], a[32:35], v[64:71], v144\n"
      "ds_read_b128 a[32:35], v145 offset:2048\n"
      "v_exp_f32 v74, v74\n"
      "v_exp_f32 v75, v75\n"
      "v_exp_f32 v76, v76\n"
      "v_smfmac_f32_16x16x64_bf16 a[0:3], a[40:43], v[32:39], v144\n"
      "v_exp_f32 v77, v77\n"
      "v_exp_f32 v78, v78\n"
      "v_smfmac_f32_16x16x64_bf16 a[4:7], a[40:43], v[64:71], v144\n"
      "v_exp_f32 v79, v79\n"
      "s_waitcnt lgkmcnt(0)\n"
      "v_exp_f32 v16, v16\n"
      "v_exp_f32 v17, v17\n"
      "v_smfmac_f32_32x32x32_bf16 v[112:127], a[36:39], v[40:47], v144\n"
      "v_exp_f32 v18, v18\n"
      "v_exp_f32 v19, v19\n"
      "v_exp_f32 v20, v20\n"
      "v_smfmac_f32_32x32x32_bf16 v[128:143], a[36:39], v[72:79], v144\n"
      "ds_read_b128 a[36:39], v145 offset:3072\n"
      "v_exp_f32 v21, v21\n"
      "v_exp_f32 v22, v22\n"
      "v_exp_f32 v23, v23\n"
      "v_smfmac_f32_16x16x64_bf16 a[0:3], a[40:43], v[40:47], v144\n"
      "v_exp_f32 v48, v48\n"
      "v_smfmac_f32_16x16x64_bf16 a[4:7], a[40:43], v[72:79], v144\n"
      "v_exp_f32 v49, v49\n"
      "v_exp_f32 v50, v50\n"
      "v_mfma_f32_32x32x16_bf16 v[32:47], a[24:27], a[8:11], v[80:95]\n"
      "v_exp_f32 v51, v51\n"
      "v_exp_f32 v52, v52\n"
      "v_exp_f32 v53, v53\n"
      "v_mfma_f32_32x32x16_bf16 v[64:79], a[24:27], a[16:19], v[96:111]\n"
      "ds_read_b128 a[24:27], v145 offset:8192\n"
      "v_exp_f32 v54, v54\n"
      "v_exp_f32 v55, v55\n"
      "v_exp_f32 v24, v24\n"
      "v_mfma_f32_32x32x16_bf16 v[32:47], a[28:31], a[12:15], v[32:47]\n"
      "v_exp_f32 v25, v25\n"
      "v_exp_f32 v26, v26\n"
      "v_exp_f32 v27, v27\n"
      "v_mfma_f32_32x32x16_bf16 v[64:79], a[28:31], a[20:23], v[64:79]\n"
      "ds_read_b128 a[28:31], v145 offset:9216\n"
      "v_exp_f32 v28, v28\n"
      "v_exp_f32 v29, v29\n"
      "v_exp_f32 v30, v30\n"
      "v_smfmac_f32_32x32x32_bf16 v[112:127], a[32:35], v[16:23], v144\n"
      "v_exp_f32 v31, v31\n"
      "v_exp_f32 v56, v56\n"
      "v_exp_f32 v57, v57\n"
      "v_smfmac_f32_32x32x32_bf16 v[128:143], a[32:35], v[48:55], v144\n"
      "ds_read_b128 a[32:35], v145 offset:6144\n"
      "v_exp_f32 v58, v58\n"
      "v_exp_f32 v59, v59\n"
      "v_exp_f32 v60, v60\n"
      "v_smfmac_f32_16x16x64_bf16 a[0:3], a[40:43], v[16:23], v144\n"
      "v_exp_f32 v61, v61\n"
      "v_exp_f32 v62, v62\n"
      "v_smfmac_f32_16x16x64_bf16 a[4:7], a[40:43], v[48:55], v144\n"
      "v_exp_f32 v63, v63\n"
      "s_waitcnt lgkmcnt(0)\n"
      "v_exp_f32 v32, v32\n"
      "v_exp_f32 v33, v33\n"
      "v_smfmac_f32_32x32x32_bf16 v[112:127], a[36:39], v[24:31], v144\n"
      "v_exp_f32 v34, v34\n"
      "v_exp_f32 v35, v35\n"
      "v_exp_f32 v36, v36\n"
      "v_smfmac_f32_32x32x32_bf16 v[128:143], a[36:39], v[56:63], v144\n"
      "ds_read_b128 a[36:39], v145 offset:7168\n"
      "v_exp_f32 v37, v37\n"
      "v_exp_f32 v38, v38\n"
      "v_exp_f32 v39, v39\n"
      "v_smfmac_f32_16x16x64_bf16 a[0:3], a[40:43], v[24:31], v144\n"
      "v_exp_f32 v64, v64\n"
      "v_smfmac_f32_16x16x64_bf16 a[4:7], a[40:43], v[56:63], v144\n"
      "v_exp_f32 v65, v65\n"
      "v_exp_f32 v66, v66\n"
      "v_mfma_f32_32x32x16_bf16 v[16:31], a[24:27], a[8:11], v[80:95]\n"
      "v_exp_f32 v67, v67\n"
      "v_exp_f32 v68, v68\n"
      "v_exp_f32 v69, v69\n"
      "v_mfma_f32_32x32x16_bf16 v[48:63], a[24:27], a[16:19], v[96:111]\n"
      "ds_read_b128 a[24:27], v145 offset:12288\n"
      "v_exp_f32 v70, v70\n"
      "v_exp_f32 v71, v71\n"
      "v_exp_f32 v40, v40\n"
      "v_mfma_f32_32x32x16_bf16 v[16:31], a[28:31], a[12:15], v[16:31]\n"
      "v_exp_f32 v41, v41\n"
      "v_exp_f32 v42, v42\n"
      "v_exp_f32 v43, v43\n"
      "v_mfma_f32_32x32x16_bf16 v[48:63], a[28:31], a[20:23], v[48:63]\n"
      "ds_read_b128 a[28:31], v145 offset:13312\n"
      "v_exp_f32 v44, v44\n"
      "v_exp_f32 v45, v45\n"
      "v_exp_f32 v46, v46\n"
      "v_smfmac_f32_32x32x32_bf16 v[112:127], a[32:35], v[32:39], v144\n"
      "v_exp_f32 v47, v47\n"
      "v_exp_f32 v72, v72\n"
      "v_exp_f32 v73, v73\n"
      "v_smfmac_f32_32x32x32_bf16 v[128:143], a[32:35], v[64:71], v144\n"
      "ds_read_b128 a[32:35], v145 offset:10240\n"
      "v_exp_f32 v74, v74\n"
      "v_exp_f32 v75, v75\n"
      "v_exp_f32 v76, v76\n"
      "v_smfmac_f32_16x16x64_bf16 a[0:3], a[40:43], v[32:39], v144\n"
      "v_exp_f32 v77, v77\n"
      "v_exp_f32 v78, v78\n"
      "v_smfmac_f32_16x16x64_bf16 a[4:7], a[40:43], v[64:71], v144\n"
      "v_exp_f32 v79, v79\n"
      "s_waitcnt lgkmcnt(0)\n"
      "s_barrier\n"
      "v_exp_f32 v16, v16\n"
      "v_exp_f32 v17, v17\n"
      "v_smfmac_f32_32x32x32_bf16 v[112:127], a[36:39], v[40:47], v144\n"
      "v_exp_f32 v18, v18\n"
      "v_exp_f32 v19, v19\n"
      "v_exp_f32 v20, v20\n"
      "v_smfmac_f32_32x32x32_bf16 v[128:143], a[36:39], v[72:79], v144\n"
      "ds_read_b128 a[36:39], v145 offset:11264\n"
      "v_exp_f32 v21, v21\n"
      "v_exp_f32 v22, v22\n"
      "v_exp_f32 v23, v23\n"
      "v_smfmac_f32_16x16x64_bf16 a[0:3], a[40:43], v[40:47], v144\n"
      "v_exp_f32 v48, v48\n"
      "v_smfmac_f32_16x16x64_bf16 a[4:7], a[40:43], v[72:79], v144\n"
      "v_exp_f32 v49, v49\n"
      "v_exp_f32 v50, v50\n"
      "v_mfma_f32_32x32x16_bf16 v[32:47], a[24:27], a[8:11], v[80:95]\n"
      "v_exp_f32 v51, v51\n"
      "v_exp_f32 v52, v52\n"
      "v_exp_f32 v53, v53\n"
      "v_mfma_f32_32x32x16_bf16 v[64:79], a[24:27], a[16:19], v[96:111]\n"
      "ds_read_b128 a[24:27], v145 offset:16384\n"
      "v_exp_f32 v54, v54\n"
      "v_exp_f32 v55, v55\n"
      "v_exp_f32 v24, v24\n"
      "v_mfma_f32_32x32x16_bf16 v[32:47], a[28:31], a[12:15], v[32:47]\n"
      "v_exp_f32 v25, v25\n"
      "v_exp_f32 v26, v26\n"
      "v_exp_f32 v27, v27\n"
      "v_mfma_f32_32x32x16_bf16 v[64:79], a[28:31], a[20:23], v[64:79]\n"
      "ds_read_b128 a[28:31], v145 offset:17408\n"
      "v_exp_f32 v28, v28\n"
      "v_exp_f32 v29, v29\n"
      "v_exp_f32 v30, v30\n"
      "v_smfmac_f32_32x32x32_bf16 v[112:127], a[32:35], v[16:23], v144\n"
      "v_exp_f32 v31, v31\n"
      "v_exp_f32 v56, v56\n"
      "v_exp_f32 v57, v57\n"
      "v_smfmac_f32_32x32x32_bf16 v[128:143], a[32:35], v[48:55], v144\n"
      "ds_read_b128 a[32:35], v145 offset:14336\n"
      "v_exp_f32 v58, v58\n"
      "v_exp_f32 v59, v59\n"
      "v_exp_f32 v60, v60\n"
      "v_smfmac_f32_16x16x64_bf16 a[0:3], a[40:43], v[16:23], v144\n"
      "v_exp_f32 v61, v61\n"
      "v_exp_f32 v62, v62\n"
      "v_smfmac_f32_16x16x64_bf16 a[4:7], a[40:43], v[48:55], v144\n"
      "v_exp_f32 v63, v63\n"
      "s_waitcnt lgkmcnt(0)\n"
      "v_exp_f32 v32, v32\n"
      "v_exp_f32 v33, v33\n"
      "v_smfmac_f32_32x32x32_bf16 v[112:127], a[36:39], v[24:31], v144\n"
      "v_exp_f32 v34, v34\n"
      "v_exp_f32 v35, v35\n"
      "v_exp_f32 v36, v36\n"
      "v_smfmac_f32_32x32x32_bf16 v[128:143], a[36:39], v[56:63], v144\n"
      "ds_read_b128 a[36:39], v145 offset:15360\n"
      "v_exp_f32 v37, v37\n"
      "v_exp_f32 v38, v38\n"
      "v_exp_f32 v39, v39\n"
      "v_smfmac_f32_16x16x64_bf16 a[0:3], a[40:43], v[24:31], v144\n"
      "v_exp_f32 v64, v64\n"
      "v_smfmac_f32_16x16x64_bf16 a[4:7], a[40:43], v[56:63], v144\n"
      "v_exp_f32 v65, v65\n"
      "v_exp_f32 v66, v66\n"
      "v_mfma_f32_32x32x16_bf16 v[16:31], a[24:27], a[8:11], v[80:95]\n"
      "v_exp_f32 v67, v67\n"
      "v_exp_f32 v68, v68\n"
      "v_exp_f32 v69, v69\n"
      "v_mfma_f32_32x32x16_bf16 v[48:63], a[24:27], a[16:19], v[96:111]\n"
      "ds_read_b128 a[24:27], v145 offset:20480\n"
      "v_exp_f32 v70, v70\n"
      "v_exp_f32 v71, v71\n"
      "v_exp_f32 v40, v40\n"
      "v_mfma_f32_32x32x16_bf16 v[16:31], a[28:31], a[12:15], v[16:31]\n"
      "v_exp_f32 v41, v41\n"
      "v_exp_f32 v42, v42\n"
      "v_exp_f32 v43, v43\n"
      "v_mfma_f32_32x32x16_bf16 v[48:63], a[28:31], a[20:23], v[48:63]\n"
      "ds_read_b128 a[28:31], v145 offset:21504\n"
      "v_exp_f32 v44, v44\n"
      "v_exp_f32 v45, v45\n"
      "v_exp_f32 v46, v46\n"
      "v_smfmac_f32_32x32x32_bf16 v[112:127], a[32:35], v[32:39], v144\n"
      "v_exp_f32 v47, v47\n"
      "v_exp_f32 v72, v72\n"
      "v_exp_f32 v73, v73\n"
      "v_smfmac_f32_32x32x32_bf16 v[128:143], a[32:35], v[64:71], v144\n"
      "ds_read_b128 a[32:35], v145 offset:18432\n"
      "v_exp_f32 v74, v74\n"
      "v_exp_f32 v75, v75\n"
      "v_exp_f32 v76, v76\n"
      "v_smfmac_f32_16x16x64_bf16 a[0:3], a[40:43], v[32:39], v144\n"
      "v_exp_f32 v77, v77\n"
      "v_exp_f32 v78, v78\n"
      "v_smfmac_f32_16x16x64_bf16 a[4:7], a[40:43], v[64:71], v144\n"
      "v_exp_f32 v79, v79\n"
      "s_waitcnt lgkmcnt(0)\n"
      "v_exp_f32 v16, v16\n"
      "v_exp_f32 v17, v17\n"
      "v_smfmac_f32_32x32x32_bf16 v[112:127], a[36:39], v[40:47], v144\n"
      "v_exp_f32 v18, v18\n"
      "v_exp_f32 v19, v19\n"
      "v_exp_f32 v20, v20\n"
      "v_smfmac_f32_32x32x32_bf16 v[128:143], a[36:39], v[72:79], v144\n"
      "ds_read_b128 a[36:39], v145 offset:19456\n"
      "v_exp_f32 v21, v21\n"
      "v_exp_f32 v22, v22\n"
      "v_exp_f32 v23, v23\n"
      "v_smfmac_f32_16x16x64_bf16 a[0:3], a[40:43], v[40:47], v144\n"
      "v_exp_f32 v48, v48\n"
      "v_smfmac_f32_16x16x64_bf16 a[4:7], a[40:43], v[72:79], v144\n"
      "v_exp_f32 v49, v49\n"
      "v_exp_f32 v50, v50\n"
      "v_mfma_f32_32x32x16_bf16 v[32:47], a[24:27], a[8:11], v[80:95]\n"
      "v_exp_f32 v51, v51\n"
      "v_exp_f32 v52, v52\n"
      "v_exp_f32 v53, v53\n"
      "v_mfma_f32_32x32x16_bf16 v[64:79], a[24:27], a[16:19], v[96:111]\n"
      "ds_read_b128 a[24:27], v145 offset:24576\n"
      "v_exp_f32 v54, v54\n"
      "v_exp_f32 v55, v55\n"
      "v_exp_f32 v24, v24\n"
      "v_mfma_f32_32x32x16_bf16 v[32:47], a[28:31], a[12:15], v[32:47]\n"
      "v_exp_f32 v25, v25\n"
      "v_exp_f32 v26, v26\n"
      "v_exp_f32 v27, v27\n"
      "v_mfma_f32_32x32x16_bf16 v[64:79], a[28:31], a[20:23], v[64:79]\n"
      "ds_read_b128 a[28:31], v145 offset:25600\n"
      "v_exp_f32 v28, v28\n"
      "v_exp_f32 v29, v29\n"
      "v_exp_f32 v30, v30\n"
      "v_smfmac_f32_32x32x32_bf16 v[112:127], a[32:35], v[16:23], v144\n"
      "v_exp_f32 v31, v31\n"
      "v_exp_f32 v56, v56\n"
      "v_exp_f32 v57, v57\n"
      "v_smfmac_f32_32x32x32_bf16 v[128:143], a[32:35], v[48:55], v144\n"
      "ds_read_b128 a[32:35], v145 offset:22528\n"
      "v_exp_f32 v58, v58\n"
      "v_exp_f32 v59, v59\n"
      "v_exp_f32 v60, v60\n"
      "v_smfmac_f32_16x16x64_bf16 a[0:3], a[40:43], v[16:23], v144\n"
      "v_exp_f32 v61, v61\n"
      "v_exp_f32 v62, v62\n"
      "v_smfmac_f32_16x16x64_bf16 a[4:7], a[40:43], v[48:55], v144\n"
      "v_exp_f32 v63, v63\n"
      "s_waitcnt lgkmcnt(0)\n"
      "v_exp_f32 v32, v32\n"
      "v_exp_f32 v33, v33\n"
      "v_smfmac_f32_32x32x32_bf16 v[112:127], a[36:39], v[24:31], v144\n"
      "v_exp_f32 v34, v34\n"
      "v_exp_f32 v35, v35\n"
      "v_exp_f32 v36, v36\n"
      "v_smfmac_f32_32x32x32_bf16 v[128:143], a[36:39], v[56:63], v144\n"
      "ds_read_b128 a[36:39], v145 offset:23552\n"
      "v_exp_f32 v37, v37\n"
      "v_exp_f32 v38, v38\n"
      "v_exp_f32 v39, v39\n"
      "v_smfmac_f32_16x16x64_bf16 a[0:3], a[40:43], v[24:31], v144\n"
      "v_exp_f32 v64, v64\n"
      "v_smfmac_f32_16x16x64_bf16 a[4:7], a[40:43], v[56:63], v144\n"
      "v_exp_f32 v65, v65\n"
      "v_exp_f32 v66, v66\n"
      "v_mfma_f32_32x32x16_bf16 v[16:31], a[24:27], a[8:11], v[80:95]\n"
      "v_exp_f32 v67, v67\n"
      "v_exp_f32 v68, v68\n"
      "v_exp_f32 v69, v69\n"
      "v_mfma_f32_32x32x16_bf16 v[48:63], a[24:27], a[16:19], v[96:111]\n"
      "ds_read_b128 a[24:27], v145 offset:28672\n"
      "v_exp_f32 v70, v70\n"
      "v_exp_f32 v71, v71\n"
      "v_exp_f32 v40, v40\n"
      "v_mfma_f32_32x32x16_bf16 v[16:31], a[28:31], a[12:15], v[16:31]\n"
      "v_exp_f32 v41, v41\n"
      "v_exp_f32 v42, v42\n"
      "v_exp_f32 v43, v43\n"
      "v_mfma_f32_32x32x16_bf16 v[48:63], a[28:31], a[20:23], v[48:63]\n"
      "ds_read_b128 a[28:31], v145 offset:29696\n"
      "v_exp_f32 v44, v44\n"
      "v_exp_f32 v45, v45\n"
      "v_exp_f32 v46, v46\n"
      "v_smfmac_f32_32x32x32_bf16 v[112:127], a[32:35], v[32:39], v144\n"
      "v_exp_f32 v47, v47\n"
      "v_exp_f32 v72, v72\n"
      "v_exp_f32 v73, v73\n"
      "v_smfmac_f32_32x32x32_bf16 v[128:143], a[32:35], v[64:71], v144\n"
      "ds_read_b128 a[32:35], v145 offset:26624\n"
      "v_exp_f32 v74, v74\n"
      "v_exp_f32 v75, v75\n"
      "v_exp_f32 v76, v76\n"
      "v_smfmac_f32_16x16x64_bf16 a[0:3], a[40:43], v[32:39], v144\n"
      "v_exp_f32 v77, v77\n"
      "v_exp_f32 v78, v78\n"
      "v_smfmac_f32_16x16x64_bf16 a[4:7], a[40:43], v[64:71], v144\n"
      "v_exp_f32 v79, v79\n"
      "s_waitcnt lgkmcnt(0)\n"
      "s_barrier\n"
      "v_exp_f32 v16, v16\n"
      "v_exp_f32 v17, v17\n"
      "v_smfmac_f32_32x32x32_bf16 v[112:127], a[36:39], v[40:47], v144\n"
      "v_exp_f32 v18, v18\n"
      "v_exp_f32 v19, v19\n"
      "v_exp_f32 v20, v20\n"
      "v_smfmac_f32_32x32x32_bf16 v[128:143], a[36:39], v[72:79], v144\n"
      "ds_read_b128 a[36:39], v145 offset:27648\n"
      "v_exp_f32 v21, v21\n"
      "v_exp_f32 v22, v22\n"
      "v_exp_f32 v23, v23\n"
      "v_smfmac_f32_16x16x64_bf16 a[0:3], a[40:43], v[40:47], v144\n"
      "v_exp_f32 v48, v48\n"
      "v_smfmac_f32_16x16x64_bf16 a[4:7], a[40:43], v[72:79], v144\n"
      "v_exp_f32 v49, v49\n"
      "v_exp_f32 v50, v50\n"
      "v_mfma_f32_32x32x16_bf16 v[32:47], a[24:27], a[8:11], v[80:95]\n"
      "v_exp_f32 v51, v51\n"
      "v_exp_f32 v52, v52\n"
      "v_exp_f32 v53, v53\n"
      "v_mfma_f32_32x32x16_bf16 v[64:79], a[24:27], a[16:19], v[96:111]\n"
      "ds_read_b128 a[24:27], v145 offset:32768\n"
      "v_exp_f32 v54, v54\n"
      "v_exp_f32 v55, v55\n"
      "v_exp_f32 v24, v24\n"
      "v_mfma_f32_32x32x16_bf16 v[32:47], a[28:31], a[12:15], v[32:47]\n"
      "v_exp_f32 v25, v25\n"
      "v_exp_f32 v26, v26\n"
      "v_exp_f32 v27, v27\n"
      "v_mfma_f32_32x32x16_bf16 v[64:79], a[28:31], a[20:23], v[64:79]\n"
      "ds_read_b128 a[28:31], v145 offset:33792\n"
      "v_exp_f32 v28, v28\n"
      "v_exp_f32 v29, v29\n"
      "v_exp_f32 v30, v30\n"
      "v_smfmac_f32_32x32x32_bf16 v[112:127], a[32:35], v[16:23], v144\n"
      "v_exp_f32 v31, v31\n"
      "v_exp_f32 v56, v56\n"
      "v_exp_f32 v57, v57\n"
      "v_smfmac_f32_32x32x32_bf16 v[128:143], a[32:35], v[48:55], v144\n"
      "ds_read_b128 a[32:35], v145 offset:30720\n"
      "v_exp_f32 v58, v58\n"
      "v_exp_f32 v59, v59\n"
      "v_exp_f32 v60, v60\n"
      "v_smfmac_f32_16x16x64_bf16 a[0:3], a[40:43], v[16:23], v144\n"
      "v_exp_f32 v61, v61\n"
      "v_exp_f32 v62, v62\n"
      "v_smfmac_f32_16x16x64_bf16 a[4:7], a[40:43], v[48:55], v144\n"
      "v_exp_f32 v63, v63\n"
      "s_waitcnt lgkmcnt(0)\n"
      "v_exp_f32 v32, v32\n"
      "v_exp_f32 v33, v33\n"
      "v_smfmac_f32_32x32x32_bf16 v[112:127], a[36:39], v[24:31], v144\n"
      "v_exp_f32 v34, v34\n"
      "v_exp_f32 v35, v35\n"
      "v_exp_f32 v36, v36\n"
      "v_smfmac_f32_32x32x32_bf16 v[128:143], a[36:39], v[56:63], v144\n"
      "ds_read_b128 a[36:39], v145 offset:31744\n"
      "v_exp_f32 v37, v37\n"
      "v_exp_f32 v38, v38\n"
      "v_exp_f32 v39, v39\n"
      "v_smfmac_f32_16x16x64_bf16 a[0:3], a[40:43], v[24:31], v144\n"
      "v_exp_f32 v64, v64\n"
      "v_smfmac_f32_16x16x64_bf16 a[4:7], a[40:43], v[56:63], v144\n"
      "v_exp_f32 v65, v65\n"
      "v_exp_f32 v66, v66\n"
      "v_mfma_f32_32x32x16_bf16 v[16:31], a[24:27], a[8:11], v[80:95]\n"
      "v_exp_f32 v67, v67\n"
      "v_exp_f32 v68, v68\n"
      "v_exp_f32 v69, v69\n"
      "v_mfma_f32_32x32x16_bf16 v[48:63], a[24:27], a[16:19], v[96:111]\n"
      "ds_read_b128 a[24:27], v145 offset:36864\n"
      "v_exp_f32 v70, v70\n"
      "v_exp_f32 v71, v71\n"
      "v_exp_f32 v40, v40\n"
      "v_mfma_f32_32x32x16_bf16 v[16:31], a[28:31], a[12:15], v[16:31]\n"
      "v_exp_f32 v41, v41\n"
      "v_exp_f32 v42, v42\n"
      "v_exp_f32 v43, v43\n"
      "v_mfma_f32_32x32x16_bf16 v[48:63], a[28:31], a[20:23], v[48:63]\n"
      "ds_read_b128 a[28:31], v145 offset:37888\n"
      "v_exp_f32 v44, v44\n"
      "v_exp_f32 v45, v45\n"
      "v_exp_f32 v46, v46\n"
      "v_smfmac_f32_32x32x32_bf16 v[112:127], a[32:35], v[32:39], v144\n"
      "v_exp_f32 v47, v47\n"
      "v_exp_f32 v72, v72\n"
      "v_exp_f32 v73, v73\n"
      "v_smfmac_f32_32x32x32_bf16 v[128:143], a[32:35], v[64:71], v144\n"
      "ds_read_b128 a[32:35], v145 offset:34816\n"
      "v_exp_f32 v74, v74\n"
      "v_exp_f32 v75, v75\n"
      "v_exp_f32 v76, v76\n"
      "v_smfmac_f32_16x16x64_bf16 a[0:3], a[40:43], v[32:39], v144\n"
      "v_exp_f32 v77, v77\n"
      "v_exp_f32 v78, v78\n"
      "v_smfmac_f32_16x16x64_bf16 a[4:7], a[40:43], v[64:71], v144\n"
      "v_exp_f32 v79, v79\n"
      "s_waitcnt lgkmcnt(0)\n"
      "v_exp_f32 v16, v16\n"
      "v_exp_f32 v17, v17\n"
      "v_smfmac_f32_32x32x32_bf16 v[112:127], a[36:39], v[40:47], v144\n"
      "v_exp_f32 v18, v18\n"
      "v_exp_f32 v19, v19\n"
      "v_exp_f32 v20, v20\n"
      "v_smfmac_f32_32x32x32_bf16 v[128:143], a[36:39], v[72:79], v144\n"
      "ds_read_b128 a[36:39], v145 offset:35840\n"
      "v_exp_f32 v21, v21\n"
      "v_exp_f32 v22, v22\n"
      "v_exp_f32 v23, v23\n"
      "v_smfmac_f32_16x16x64_bf16 a[0:3], a[40:43], v[40:47], v144\n"
      "v_exp_f32 v48, v48\n"
      "v_smfmac_f32_16x16x64_bf16 a[4:7], a[40:43], v[72:79], v144\n"
      "v_exp_f32 v49, v49\n"
      "v_exp_f32 v50, v50\n"
      "v_mfma_f32_32x32x16_bf16 v[32:47], a[24:27], a[8:11], v[80:95]\n"
      "v_exp_f32 v51, v51\n"
      "v_exp_f32 v52, v52\n"
      "v_exp_f32 v53, v53\n"
      "v_mfma_f32_32x32x16_bf16 v[64:79], a[24:27], a[16:19], v[96:111]\n"
      "ds_read_b128 a[24:27], v145 offset:40960\n"
      "v_exp_f32 v54, v54\n"
      "v_exp_f32 v55, v55\n"
      "v_exp_f32 v24, v24\n"
      "v_mfma_f32_32x32x16_bf16 v[32:47], a[28:31], a[12:15], v[32:47]\n"
      "v_exp_f32 v25, v25\n"
      "v_exp_f32 v26, v26\n"
      "v_exp_f32 v27, v27\n"
      "v_mfma_f32_32x32x16_bf16 v[64:79], a[28:31], a[20:23], v[64:79]\n"
      "ds_read_b128 a[28:31], v145 offset:41984\n"
      "v_exp_f32 v28, v28\n"
      "v_exp_f32 v29, v29\n"
      "v_exp_f32 v30, v30\n"
      "v_smfmac_f32_32x32x32_bf16 v[112:127], a[32:35], v[16:23], v144\n"
      "v_exp_f32 v31, v31\n"
      "v_exp_f32 v56, v56\n"
      "v_exp_f32 v57, v57\n"
      "v_smfmac_f32_32x32x32_bf16 v[128:143], a[32:35], v[48:55], v144\n"
      "ds_read_b128 a[32:35], v145 offset:38912\n"
      "v_exp_f32 v58, v58\n"
      "v_exp_f32 v59, v59\n"
      "v_exp_f32 v60, v60\n"
      "v_smfmac_f32_16x16x64_bf16 a[0:3], a[40:43], v[16:23], v144\n"
      "v_exp_f32 v61, v61\n"
      "v_exp_f32 v62, v62\n"
      "v_smfmac_f32_16x16x64_bf16 a[4:7], a[40:43], v[48:55], v144\n"
      "v_exp_f32 v63, v63\n"
      "s_waitcnt lgkmcnt(0)\n"
      "v_exp_f32 v32, v32\n"
      "v_exp_f32 v33, v33\n"
      "v_smfmac_f32_32x32x32_bf16 v[112:127], a[36:39], v[24:31], v144\n"
      "v_exp_f32 v34, v34\n"
      "v_exp_f32 v35, v35\n"
      "v_exp_f32 v36, v36\n"
      "v_smfmac_f32_32x32x32_bf16 v[128:143], a[36:39], v[56:63], v144\n"
      "ds_read_b128 a[36:39], v145 offset:39936\n"
      "v_exp_f32 v37, v37\n"
      "v_exp_f32 v38, v38\n"
      "v_exp_f32 v39, v39\n"
      "v_smfmac_f32_16x16x64_bf16 a[0:3], a[40:43], v[24:31], v144\n"
      "v_exp_f32 v64, v64\n"
      "v_smfmac_f32_16x16x64_bf16 a[4:7], a[40:43], v[56:63], v144\n"
      "v_exp_f32 v65, v65\n"
      "v_exp_f32 v66, v66\n"
      "v_mfma_f32_32x32x16_bf16 v[16:31], a[24:27], a[8:11], v[80:95]\n"
      "v_exp_f32 v67, v67\n"
      "v_exp_f32 v68, v68\n"
      "v_exp_f32 v69, v69\n"
      "v_mfma_f32_32x32x16_bf16 v[48:63], a[24:27], a[16:19], v[96:111]\n"
      "ds_read_b128 a[24:27], v145 offset:45056\n"
      "v_exp_f32 v70, v70\n"
      "v_exp_f32 v71, v71\n"
      "v_exp_f32 v40, v40\n"
      "v_mfma_f32_32x32x16_bf16 v[16:31], a[28:31], a[12:15], v[16:31]\n"
      "v_exp_f32 v41, v41\n"
      "v_exp_f32 v42, v42\n"
      "v_exp_f32 v43, v43\n"
      "v_mfma_f32_32x32x16_bf16 v[48:63], a[28:31], a[20:23], v[48:63]\n"
      "ds_read_b128 a[28:31], v145 offset:46080\n"
      "v_exp_f32 v44, v44\n"
      "v_exp_f32 v45, v45\n"
      "v_exp_f32 v46, v46\n"
      "v_smfmac_f32_32x32x32_bf16 v[112:127], a[32:35], v[32:39], v144\n"
      "v_exp_f32 v47, v47\n"
      "v_exp_f32 v72, v72\n"
      "v_exp_f32 v73, v73\n"
      "v_smfmac_f32_32x32x32_bf16 v[128:143], a[32:35], v[64:71], v144\n"
      "ds_read_b128 a[32:35], v145 offset:43008\n"
      "v_exp_f32 v74, v74\n"
      "v_exp_f32 v75, v75\n"
      "v_exp_f32 v76, v76\n"
      "v_smfmac_f32_16x16x64_bf16 a[0:3], a[40:43], v[32:39], v144\n"
      "v_exp_f32 v77, v77\n"
      "v_exp_f32 v78, v78\n"
      "v_smfmac_f32_16x16x64_bf16 a[4:7], a[40:43], v[64:71], v144\n"
      "v_exp_f32 v79, v79\n"
      "s_waitcnt lgkmcnt(0)\n"
      "s_barrier\n"
      "v_exp_f32 v16, v16\n"
      "v_exp_f32 v17, v17\n"
      "v_smfmac_f32_32x32x32_bf16 v[112:127], a[36:39], v[40:47], v144\n"
      "v_exp_f32 v18, v18\n"
      "v_exp_f32 v19, v19\n"
      "v_exp_f32 v20, v20\n"
      "v_smfmac_f32_32x32x32_bf16 v[128:143], a[36:39], v[72:79], v144\n"
      "ds_read_b128 a[36:39], v145 offset:44032\n"
      "v_exp_f32 v21, v21\n"
      "v_exp_f32 v22, v22\n"
      "v_exp_f32 v23, v23\n"
      "v_smfmac_f32_16x16x64_bf16 a[0:3], a[40:43], v[40:47], v144\n"
      "v_exp_f32 v48, v48\n"
      "v_smfmac_f32_16x16x64_bf16 a[4:7], a[40:43], v[72:79], v144\n"
      "v_exp_f32 v49, v49\n"
      "v_exp_f32 v50, v50\n"
      "v_mfma_f32_32x32x16_bf16 v[32:47], a[24:27], a[8:11], v[80:95]\n"
      "v_exp_f32 v51, v51\n"
      "v_exp_f32 v52, v52\n"
      "v_exp_f32 v53, v53\n"
      "v_mfma_f32_32x32x16_bf16 v[64:79], a[24:27], a[16:19], v[96:111]\n"
      "ds_read_b128 a[24:27], v145 offset:49152\n"
      "v_exp_f32 v54, v54\n"
      "v_exp_f32 v55, v55\n"
      "v_exp_f32 v24, v24\n"
      "v_mfma_f32_32x32x16_bf16 v[32:47], a[28:31], a[12:15], v[32:47]\n"
      "v_exp_f32 v25, v25\n"
      "v_exp_f32 v26, v26\n"
      "v_exp_f32 v27, v27\n"
      "v_mfma_f32_32x32x16_bf16 v[64:79], a[28:31], a[20:23], v[64:79]\n"
      "ds_read_b128 a[28:31], v145 offset:50176\n"
      "v_exp_f32 v28, v28\n"
      "v_exp_f32 v29, v29\n"
      "v_exp_f32 v30, v30\n"
      "v_smfmac_f32_32x32x32_bf16 v[112:127], a[32:35], v[16:23], v144\n"
      "v_exp_f32 v31, v31\n"
      "v_exp_f32 v56, v56\n"
      "v_exp_f32 v57, v57\n"
      "v_smfmac_f32_32x32x32_bf16 v[128:143], a[32:35], v[48:55], v144\n"
      "ds_read_b128 a[32:35], v145 offset:47104\n"
      "v_exp_f32 v58, v58\n"
      "v_exp_f32 v59, v59\n"
      "v_exp_f32 v60, v60\n"
      "v_smfmac_f32_16x16x64_bf16 a[0:3], a[40:43], v[16:23], v144\n"
      "v_exp_f32 v61, v61\n"
      "v_exp_f32 v62, v62\n"
      "v_smfmac_f32_16x16x64_bf16 a[4:7], a[40:43], v[48:55], v144\n"
      "v_exp_f32 v63, v63\n"
      "s_waitcnt lgkmcnt(0)\n"
      "v_exp_f32 v32, v32\n"
      "v_exp_f32 v33, v33\n"
      "v_smfmac_f32_32x32x32_bf16 v[112:127], a[36:39], v[24:31], v144\n"
      "v_exp_f32 v34, v34\n"
      "v_exp_f32 v35, v35\n"
      "v_exp_f32 v36, v36\n"
      "v_smfmac_f32_32x32x32_bf16 v[128:143], a[36:39], v[56:63], v144\n"
      "ds_read_b128 a[36:39], v145 offset:48128\n"
      "v_exp_f32 v37, v37\n"
      "v_exp_f32 v38, v38\n"
      "v_exp_f32 v39, v39\n"
      "v_smfmac_f32_16x16x64_bf16 a[0:3], a[40:43], v[24:31], v144\n"
      "v_exp_f32 v64, v64\n"
      "v_smfmac_f32_16x16x64_bf16 a[4:7], a[40:43], v[56:63], v144\n"
      "v_exp_f32 v65, v65\n"
      "v_exp_f32 v66, v66\n"
      "v_mfma_f32_32x32x16_bf16 v[16:31], a[24:27], a[8:11], v[80:95]\n"
      "v_exp_f32 v67, v67\n"
      "v_exp_f32 v68, v68\n"
      "v_exp_f32 v69, v69\n"
      "v_mfma_f32_32x32x16_bf16 v[48:63], a[24:27], a[16:19], v[96:111]\n"
      "ds_read_b128 a[24:27], v145 offset:53248\n"
      "v_exp_f32 v70, v70\n"
      "v_exp_f32 v71, v71\n"
      "v_exp_f32 v40, v40\n"
      "v_mfma_f32_32x32x16_bf16 v[16:31], a[28:31], a[12:15], v[16:31]\n"
      "v_exp_f32 v41, v41\n"
      "v_exp_f32 v42, v42\n"
      "v_exp_f32 v43, v43\n"
      "v_mfma_f32_32x32x16_bf16 v[48:63], a[28:31], a[20:23], v[48:63]\n"
      "ds_read_b128 a[28:31], v145 offset:54272\n"
      "v_exp_f32 v44, v44\n"
      "v_exp_f32 v45, v45\n"
      "v_exp_f32 v46, v46\n"
      "v_smfmac_f32_32x32x32_bf16 v[112:127], a[32:35], v[32:39], v144\n"
      "v_exp_f32 v47, v47\n"
      "v_exp_f32 v72, v72\n"
      "v_exp_f32 v73, v73\n"
      "v_smfmac_f32_32x32x32_bf16 v[128:143], a[32:35], v[64:71], v144\n"
      "ds_read_b128 a[32:35], v145 offset:51200\n"
      "v_exp_f32 v74, v74\n"
      "v_exp_f32 v75, v75\n"
      "v_exp_f32 v76, v76\n"
      "v_smfmac_f32_16x16x64_bf16 a[0:3], a[40:43], v[32:39], v144\n"
      "v_exp_f32 v77, v77\n"
      "v_exp_f32 v78, v78\n"
      "v_smfmac_f32_16x16x64_bf16 a[4:7], a[40:43], v[64:71], v144\n"
      "v_exp_f32 v79, v79\n"
      "s_waitcnt lgkmcnt(0)\n"
      "v_exp_f32 v16, v16\n"
      "v_exp_f32 v17, v17\n"
      "v_smfmac_f32_32x32x32_bf16 v[112:127], a[36:39], v[40:47], v144\n"
      "v_exp_f32 v18, v18\n"
      "v_exp_f32 v19, v19\n"
      "v_exp_f32 v20, v20\n"
      "v_smfmac_f32_32x32x32_bf16 v[128:143], a[36:39], v[72:79], v144\n"
      "ds_read_b128 a[36:39], v145 offset:52224\n"
      "v_exp_f32 v21, v21\n"
      "v_exp_f32 v22, v22\n"
      "v_exp_f32 v23, v23\n"
      "v_smfmac_f32_16x16x64_bf16 a[0:3], a[40:43], v[40:47], v144\n"
      "v_exp_f32 v48, v48\n"
      "v_smfmac_f32_16x16x64_bf16 a[4:7], a[40:43], v[72:79], v144\n"
      "v_exp_f32 v49, v49\n"
      "v_exp_f32 v50, v50\n"
      "v_mfma_f32_32x32x16_bf16 v[32:47], a[24:27], a[8:11], v[80:95]\n"
      "v_exp_f32 v51, v51\n"
      "v_exp_f32 v52, v52\n"
      "v_exp_f32 v53, v53\n"
      "v_mfma_f32_32x32x16_bf16 v[64:79], a[24:27], a[16:19], v[96:111]\n"
      "ds_read_b128 a[24:27], v145 offset:57344\n"
      "v_exp_f32 v54, v54\n"
      "v_exp_f32 v55, v55\n"
      "v_exp_f32 v24, v24\n"
      "v_mfma_f32_32x32x16_bf16 v[32:47], a[28:31], a[12:15], v[32:47]\n"
      "v_exp_f32 v25, v25\n"
      "v_exp_f32 v26, v26\n"
      "v_exp_f32 v27, v27\n"
      "v_mfma_f32_32x32x16_bf16 v[64:79], a[28:31], a[20:23], v[64:79]\n"
      "ds_read_b128 a[28:31], v145 offset:58368\n"
      "v_exp_f32 v28, v28\n"
      "v_exp_f32 v29, v29\n"
      "v_exp_f32 v30, v30\n"
      "v_smfmac_f32_32x32x32_bf16 v[112:127], a[32:35], v[16:23], v144\n"
      "v_exp_f32 v31, v31\n"
      "v_exp_f32 v56, v56\n"
      "v_exp_f32 v57, v57\n"
      "v_smfmac_f32_32x32x32_bf16 v[128:143], a[32:35], v[48:55], v144\n"
      "ds_read_b128 a[32:35], v145 offset:55296\n"
      "v_exp_f32 v58, v58\n"
      "v_exp_f32 v59, v59\n"
      "v_exp_f32 v60, v60\n"
      "v_smfmac_f32_16x16x64_bf16 a[0:3], a[40:43], v[16:23], v144\n"
      "v_exp_f32 v61, v61\n"
      "v_exp_f32 v62, v62\n"
      "v_smfmac_f32_16x16x64_bf16 a[4:7], a[40:43], v[48:55], v144\n"
      "v_exp_f32 v63, v63\n"
      "s_waitcnt lgkmcnt(0)\n"
      "v_exp_f32 v32, v32\n"
      "v_exp_f32 v33, v33\n"
      "v_smfmac_f32_32x32x32_bf16 v[112:127], a[36:39], v[24:31], v144\n"
      "v_exp_f32 v34, v34\n"
      "v_exp_f32 v35, v35\n"
      "v_exp_f32 v36, v36\n"
      "v_smfmac_f32_32x32x32_bf16 v[128:143], a[36:39], v[56:63], v144\n"
      "ds_read_b128 a[36:39], v145 offset:56320\n"
      "v_exp_f32 v37, v37\n"
      "v_exp_f32 v38, v38\n"
      "v_exp_f32 v39, v39\n"
      "v_smfmac_f32_16x16x64_bf16 a[0:3], a[40:43], v[24:31], v144\n"
      "v_exp_f32 v64, v64\n"
      "v_smfmac_f32_16x16x64_bf16 a[4:7], a[40:43], v[56:63], v144\n"
      "v_exp_f32 v65, v65\n"
      "v_exp_f32 v66, v66\n"
      "v_mfma_f32_32x32x16_bf16 v[16:31], a[24:27], a[8:11], v[80:95]\n"
      "v_exp_f32 v67, v67\n"
      "v_exp_f32 v68, v68\n"
      "v_exp_f32 v69, v69\n"
      "v_mfma_f32_32x32x16_bf16 v[48:63], a[24:27], a[16:19], v[96:111]\n"
      "ds_read_b128 a[24:27], v145 offset:61440\n"
      "v_exp_f32 v70, v70\n"
      "v_exp_f32 v71, v71\n"
      "v_exp_f32 v40, v40\n"
      "v_mfma_f32_32x32x16_bf16 v[16:31], a[28:31], a[12:15], v[16:31]\n"
      "v_exp_f32 v41, v41\n"
      "v_exp_f32 v42, v42\n"
      "v_exp_f32 v43, v43\n"
      "v_mfma_f32_32x32x16_bf16 v[48:63], a[28:31], a[20:23], v[48:63]\n"
      "ds_read_b128 a[28:31], v145 offset:62464\n"
      "v_exp_f32 v44, v44\n"
      "v_exp_f32 v45, v45\n"
      "v_exp_f32 v46, v46\n"
      "v_smfmac_f32_32x32x32_bf16 v[112:127], a[32:35], v[32:39], v144\n"
      "v_exp_f32 v47, v47\n"
      "v_exp_f32 v72, v72\n"
      "v_exp_f32 v73, v73\n"
      "v_smfmac_f32_32x32x32_bf16 v[128:143], a[32:35], v[64:71], v144\n"
      "ds_read_b128 a[32:35], v145 offset:59392\n"
      "v_exp_f32 v74, v74\n"
      "v_exp_f32 v75, v75\n"
      "v_exp_f32 v76, v76\n"
      "v_smfmac_f32_16x16x64_bf16 a[0:3], a[40:43], v[32:39], v144\n"
      "v_exp_f32 v77, v77\n"
      "v_exp_f32 v78, v78\n"
      "v_smfmac_f32_16x16x64_bf16 a[4:7], a[40:43], v[64:71], v144\n"
      "v_exp_f32 v79, v79\n"
      "s_waitcnt lgkmcnt(0)\n"
      "s_barrier\n"
      "v_exp_f32 v16, v16\n"
      "v_exp_f32 v17, v17\n"
      "v_smfmac_f32_32x32x32_bf16 v[112:127], a[36:39], v[40:47], v144\n"
      "v_exp_f32 v18, v18\n"
      "v_exp_f32 v19, v19\n"
      "v_exp_f32 v20, v20\n"
      "v_smfmac_f32_32x32x32_bf16 v[128:143], a[36:39], v[72:79], v144\n"
      "ds_read_b128 a[36:39], v145 offset:60416\n"
      "v_exp_f32 v21, v21\n"
      "v_exp_f32 v22, v22\n"
      "v_exp_f32 v23, v23\n"
      "v_smfmac_f32_16x16x64_bf16 a[0:3], a[40:43], v[40:47], v144\n"
      "v_exp_f32 v48, v48\n"
      "v_smfmac_f32_16x16x64_bf16 a[4:7], a[40:43], v[72:79], v144\n"
      "v_exp_f32 v49, v49\n"
      "v_exp_f32 v50, v50\n"
      "v_mfma_f32_32x32x16_bf16 v[32:47], a[24:27], a[8:11], v[80:95]\n"
      "v_exp_f32 v51, v51\n"
      "v_exp_f32 v52, v52\n"
      "v_exp_f32 v53, v53\n"
      "v_mfma_f32_32x32x16_bf16 v[64:79], a[24:27], a[16:19], v[96:111]\n"
      "ds_read_b128 a[24:27], v145 offset:0\n"
      "v_exp_f32 v54, v54\n"
      "v_exp_f32 v55, v55\n"
      "v_exp_f32 v24, v24\n"
      "v_mfma_f32_32x32x16_bf16 v[32:47], a[28:31], a[12:15], v[32:47]\n"
      "v_exp_f32 v25, v25\n"
      "v_exp_f32 v26, v26\n"
      "v_exp_f32 v27, v27\n"
      "v_mfma_f32_32x32x16_bf16 v[64:79], a[28:31], a[20:23], v[64:79]\n"
      "ds_read_b128 a[28:31], v145 offset:1024\n"
      "v_exp_f32 v28, v28\n"
      "v_exp_f32 v29, v29\n"
      "v_exp_f32 v30, v30\n"
      "v_smfmac_f32_32x32x32_bf16 v[112:127], a[32:35], v[16:23], v144\n"
      "v_exp_f32 v31, v31\n"
      "v_exp_f32 v56, v56\n"
      "v_exp_f32 v57, v57\n"
      "v_smfmac_f32_32x32x32_bf16 v[128:143], a[32:35], v[48:55], v144\n"
      "ds_read_b128 a[32:35], v145 offset:63488\n"
      "v_exp_f32 v58, v58\n"
      "v_exp_f32 v59, v59\n"
      "v_exp_f32 v60, v60\n"
      "v_smfmac_f32_16x16x64_bf16 a[0:3], a[40:43], v[16:23], v144\n"
      "v_exp_f32 v61, v61\n"
      "v_exp_f32 v62, v62\n"
      "v_smfmac_f32_16x16x64_bf16 a[4:7], a[40:43], v[48:55], v144\n"
      "v_exp_f32 v63, v63\n"
      "s_waitcnt lgkmcnt(0)\n"
      "v_exp_f32 v32, v32\n"
      "v_exp_f32 v33, v33\n"
      "v_smfmac_f32_32x32x32_bf16 v[112:127], a[36:39], v[24:31], v144\n"
      "v_exp_f32 v34, v34\n"
      "v_exp_f32 v35, v35\n"
      "v_exp_f32 v36, v36\n"
      "v_smfmac_f32_32x32x32_bf16 v[128:143], a[36:39], v[56:63], v144\n"
      "ds_read_b128 a[36:39], v145 offset:64512\n"
      "v_exp_f32 v37, v37\n"
      "v_exp_f32 v38, v38\n"
      "v_exp_f32 v39, v39\n"
      "v_smfmac_f32_16x16x64_bf16 a[0:3], a[40:43], v[24:31], v144\n"
      "v_exp_f32 v64, v64\n"
      "v_smfmac_f32_16x16x64_bf16 a[4:7], a[40:43], v[56:63], v144\n"
      "v_exp_f32 v65, v65\n"
      "v_exp_f32 v66, v66\n"
      "v_mfma_f32_32x32x16_bf16 v[16:31], a[24:27], a[8:11], v[80:95]\n"
      "v_exp_f32 v67, v67\n"
      "v_exp_f32 v68, v68\n"
      "v_exp_f32 v69, v69\n"
      "v_mfma_f32_32x32x16_bf16 v[48:63], a[24:27], a[16:19], v[96:111]\n"
      "ds_read_b128 a[24:27], v145 offset:4096\n"
      "v_exp_f32 v70, v70\n"
      "v_exp_f32 v71, v71\n"
      "v_exp_f32 v40, v40\n"
      "v_mfma_f32_32x32x16_bf16 v[16:31], a[28:31], a[12:15], v[16:31]\n"
      "v_exp_f32 v41, v41\n"
      "v_exp_f32 v42, v42\n"
      "v_exp_f32 v43, v43\n"
      "v_mfma_f32_32x32x16_bf16 v[48:63], a[28:31], a[20:23], v[48:63]\n"
      "ds_read_b128 a[28:31], v145 offset:5120\n"
      "v_exp_f32 v44, v44\n"
      "v_exp_f32 v45, v45\n"
      "v_exp_f32 v46, v46\n"
      "v_smfmac_f32_32x32x32_bf16 v[112:127], a[32:35], v[32:39], v144\n"
      "v_exp_f32 v47, v47\n"
      "v_exp_f32 v72, v72\n"
      "v_exp_f32 v73, v73\n"
      "v_smfmac_f32_32x32x32_bf16 v[128:143], a[32:35], v[64:71], v144\n"
      "ds_read_b128 a[32:35], v145 offset:2048\n"
      "v_exp_f32 v74, v74\n"
      "v_exp_f32 v75, v75\n"
      "v_exp_f32 v76, v76\n"
      "v_smfmac_f32_16x16x64_bf16 a[0:3], a[40:43], v[32:39], v144\n"
      "v_exp_f32 v77, v77\n"
      "v_exp_f32 v78, v78\n"
      "v_smfmac_f32_16x16x64_bf16 a[4:7], a[40:43], v[64:71], v144\n"
      "v_exp_f32 v79, v79\n"
      "s_waitcnt lgkmcnt(0)\n"
      "v_exp_f32 v16, v16\n"
      "v_exp_f32 v17, v17\n"
      "v_smfmac_f32_32x32x32_bf16 v[112:127], a[36:39], v[40:47], v144\n"
      "v_exp_f32 v18, v18\n"
      "v_exp_f32 v19, v19\n"
      "v_exp_f32 v20, v20\n"
      "v_smfmac_f32_32x32x32_bf16 v[128:143], a[36:39], v[72:79], v144\n"
      "ds_read_b128 a[36:39], v145 offset:3072\n"
      "v_exp_f32 v21, v21\n"
      "v_exp_f32 v22, v22\n"
      "v_exp_f32 v23, v23\n"
      "v_smfmac_f32_16x16x64_bf16 a[0:3], a[40:43], v[40:47], v144\n"
      "v_exp_f32 v48, v48\n"
      "v_smfmac_f32_16x16x64_bf16 a[4:7], a[40:43], v[72:79], v144\n"
      "v_exp_f32 v49, v49\n"
      "v_exp_f32 v50, v50\n"
      "v_mfma_f32_32x32x16_bf16 v[32:47], a[24:27], a[8:11], v[80:95]\n"
      "v_exp_f32 v51, v51\n"
      "v_exp_f32 v52, v52\n"
      "v_exp_f32 v53, v53\n"
      "v_mfma_f32_32x32x16_bf16 v[64:79], a[24:27], a[16:19], v[96:111]\n"
      "ds_read_b128 a[24:27], v145 offset:8192\n"
      "v_exp_f32 v54, v54\n"
      "v_exp_f32 v55, v55\n"
      "v_exp_f32 v24, v24\n"
      "v_mfma_f32_32x32x16_bf16 v[32:47], a[28:31], a[12:15], v[32:47]\n"
      "v_exp_f32 v25, v25\n"
      "v_exp_f32 v26, v26\n"
      "v_exp_f32 v27, v27\n"
      "v_mfma_f32_32x32x16_bf16 v[64:79], a[28:31], a[20:23], v[64:79]\n"
      "ds_read_b128 a[28:31], v145 offset:9216\n"
      "v_exp_f32 v28, v28\n"
      "v_exp_f32 v29, v29\n"
      "v_exp_f32 v30, v30\n"
      "v_smfmac_f32_32x32x32_bf16 v[112:127], a[32:35], v[16:23], v144\n"
      "v_exp_f32 v31, v31\n"
      "v_exp_f32 v56, v56\n"
      "v_exp_f32 v57, v57\n"
      "v_smfmac_f32_32x32x32_bf16 v[128:143], a[32:35], v[48:55], v144\n"
      "ds_read_b128 a[32:35], v145 offset:6144\n"
      "v_exp_f32 v58, v58\n"
      "v_exp_f32 v59, v59\n"
      "v_exp_f32 v60, v60\n"
      "v_smfmac_f32_16x16x64_bf16 a[0:3], a[40:43], v[16:23], v144\n"
      "v_exp_f32 v61, v61\n"
      "v_exp_f32 v62, v62\n"
      "v_smfmac_f32_16x16x64_bf16 a[4:7], a[40:43], v[48:55], v144\n"
      "v_exp_f32 v63, v63\n"
      "s_waitcnt lgkmcnt(0)\n"
      "v_exp_f32 v32, v32\n"
      "v_exp_f32 v33, v33\n"
      "v_smfmac_f32_32x32x32_bf16 v[112:127], a[36:39], v[24:31], v144\n"
      "v_exp_f32 v34, v34\n"
      "v_exp_f32 v35, v35\n"
      "v_exp_f32 v36, v36\n"
      "v_smfmac_f32_32x32x32_bf16 v[128:143], a[36:39], v[56:63], v144\n"
      "ds_read_b128 a[36:39], v145 offset:7168\n"
      "v_exp_f32 v37, v37\n"
      "v_exp_f32 v38, v38\n"
      "v_exp_f32 v39, v39\n"
      "v_smfmac_f32_16x16x64_bf16 a[0:3], a[40:43], v[24:31], v144\n"
      "v_exp_f32 v64, v64\n"
      "v_smfmac_f32_16x16x64_bf16 a[4:7], a[40:43], v[56:63], v144\n"
      "v_exp_f32 v65, v65\n"
      "v_exp_f32 v66, v66\n"
      "v_mfma_f32_32x32x16_bf16 v[16:31], a[24:27], a[8:11], v[80:95]\n"
      "v_exp_f32 v67, v67\n"
      "v_exp_f32 v68, v68\n"
      "v_exp_f32 v69, v69\n"
      "v_mfma_f32_32x32x16_bf16 v[48:63], a[24:27], a[16:19], v[96:111]\n"
      "ds_read_b128 a[24:27], v145 offset:12288\n"
      "v_exp_f32 v70, v70\n"
      "v_exp_f32 v71, v71\n"
      "v_exp_f32 v40, v40\n"
      "v_mfma_f32_32x32x16_bf16 v[16:31], a[28:31], a[12:15], v[16:31]\n"
      "v_exp_f32 v41, v41\n"
      "v_exp_f32 v42, v42\n"
      "v_exp_f32 v43, v43\n"
      "v_mfma_f32_32x32x16_bf16 v[48:63], a[28:31], a[20:23], v[48:63]\n"
      "ds_read_b128 a[28:31], v145 offset:13312\n"
      "v_exp_f32 v44, v44\n"
      "v_exp_f32 v45, v45\n"
      "v_exp_f32 v46, v46\n"
      "v_smfmac_f32_32x32x32_bf16 v[112:127], a[32:35], v[32:39], v144\n"
      "v_exp_f32 v47, v47\n"
      "v_exp_f32 v72, v72\n"
      "v_exp_f32 v73, v73\n"
      "v_smfmac_f32_32x32x32_bf16 v[128:143], a[32:35], v[64:71], v144\n"
      "ds_read_b128 a[32:35], v145 offset:10240\n"
      "v_exp_f32 v74, v74\n"
      "v_exp_f32 v75, v75\n"
      "v_exp_f32 v76, v76\n"
      "v_smfmac_f32_16x16x64_bf16 a[0:3], a[40:43], v[32:39], v144\n"
      "v_exp_f32 v77, v77\n"
      "v_exp_f32 v78, v78\n"
      "v_smfmac_f32_16x16x64_bf16 a[4:7], a[40:43], v[64:71], v144\n"
      "v_exp_f32 v79, v79\n"
      "s_waitcnt lgkmcnt(0)\n"
      "s_barrier\n"
      "v_exp_f32 v16, v16\n"
      "v_exp_f32 v17, v17\n"
      "v_smfmac_f32_32x32x32_bf16 v[112:127], a[36:39], v[40:47], v144\n"
      "v_exp_f32 v18, v18\n"
      "v_exp_f32 v19, v19\n"
      "v_exp_f32 v20, v20\n"
      "v_smfmac_f32_32x32x32_bf16 v[128:143], a[36:39], v[72:79], v144\n"
      "ds_read_b128 a[36:39], v145 offset:11264\n"
      "v_exp_f32 v21, v21\n"
      "v_exp_f32 v22, v22\n"
      "v_exp_f32 v23, v23\n"
      "v_smfmac_f32_16x16x64_bf16 a[0:3], a[40:43], v[40:47], v144\n"
      "v_exp_f32 v48, v48\n"
      "v_smfmac_f32_16x16x64_bf16 a[4:7], a[40:43], v[72:79], v144\n"
      "v_exp_f32 v49, v49\n"
      "v_exp_f32 v50, v50\n"
      "v_mfma_f32_32x32x16_bf16 v[32:47], a[24:27], a[8:11], v[80:95]\n"
      "v_exp_f32 v51, v51\n"
      "v_exp_f32 v52, v52\n"
      "v_exp_f32 v53, v53\n"
      "v_mfma_f32_32x32x16_bf16 v[64:79], a[24:27], a[16:19], v[96:111]\n"
      "ds_read_b128 a[24:27], v145 offset:16384\n"
      "v_exp_f32 v54, v54\n"
      "v_exp_f32 v55, v55\n"
      "v_exp_f32 v24, v24\n"
      "v_mfma_f32_32x32x16_bf16 v[32:47], a[28:31], a[12:15], v[32:47]\n"
      "v_exp_f32 v25, v25\n"
      "v_exp_f32 v26, v26\n"
      "v_exp_f32 v27, v27\n"
      "v_mfma_f32_32x32x16_bf16 v[64:79], a[28:31], a[20:23], v[64:79]\n"
      "ds_read_b128 a[28:31], v145 offset:17408\n"
      "v_exp_f32 v28, v28\n"
      "v_exp_f32 v29, v29\n"
      "v_exp_f32 v30, v30\n"
      "v_smfmac_f32_32x32x32_bf16 v[112:127], a[32:35], v[16:23], v144\n"
      "v_exp_f32 v31, v31\n"
      "v_exp_f32 v56, v56\n"
      "v_exp_f32 v57, v57\n"
      "v_smfmac_f32_32x32x32_bf16 v[128:143], a[32:35], v[48:55], v144\n"
      "ds_read_b128 a[32:35], v145 offset:14336\n"
      "v_exp_f32 v58, v58\n"
      "v_exp_f32 v59, v59\n"
      "v_exp_f32 v60, v60\n"
      "v_smfmac_f32_16x16x64_bf16 a[0:3], a[40:43], v[16:23], v144\n"
      "v_exp_f32 v61, v61\n"
      "v_exp_f32 v62, v62\n"
      "v_smfmac_f32_16x16x64_bf16 a[4:7], a[40:43], v[48:55], v144\n"
      "v_exp_f32 v63, v63\n"
      "s_waitcnt lgkmcnt(0)\n"
      "v_exp_f32 v32, v32\n"
      "v_exp_f32 v33, v33\n"
      "v_smfmac_f32_32x32x32_bf16 v[112:127], a[36:39], v[24:31], v144\n"
      "v_exp_f32 v34, v34\n"
      "v_exp_f32 v35, v35\n"
      "v_exp_f32 v36, v36\n"
      "v_smfmac_f32_32x32x32_bf16 v[128:143], a[36:39], v[56:63], v144\n"
      "ds_read_b128 a[36:39], v145 offset:15360\n"
      "v_exp_f32 v37, v37\n"
      "v_exp_f32 v38, v38\n"
      "v_exp_f32 v39, v39\n"
      "v_smfmac_f32_16x16x64_bf16 a[0:3], a[40:43], v[24:31], v144\n"
      "v_exp_f32 v64, v64\n"
      "v_smfmac_f32_16x16x64_bf16 a[4:7], a[40:43], v[56:63], v144\n"
      "v_exp_f32 v65, v65\n"
      "v_exp_f32 v66, v66\n"
      "v_mfma_f32_32x32x16_bf16 v[16:31], a[24:27], a[8:11], v[80:95]\n"
      "v_exp_f32 v67, v67\n"
      "v_exp_f32 v68, v68\n"
      "v_exp_f32 v69, v69\n"
      "v_mfma_f32_32x32x16_bf16 v[48:63], a[24:27], a[16:19], v[96:111]\n"
      "ds_read_b128 a[24:27], v145 offset:20480\n"
      "v_exp_f32 v70, v70\n"
      "v_exp_f32 v71, v71\n"
      "v_exp_f32 v40, v40\n"
      "v_mfma_f32_32x32x16_bf16 v[16:31], a[28:31], a[12:15], v[16:31]\n"
      "v_exp_f32 v41, v41\n"
      "v_exp_f32 v42, v42\n"
      "v_exp_f32 v43, v43\n"
      "v_mfma_f32_32x32x16_bf16 v[48:63], a[28:31], a[20:23], v[48:63]\n"
      "ds_read_b128 a[28:31], v145 offset:21504\n"
      "v_exp_f32 v44, v44\n"
      "v_exp_f32 v45, v45\n"
      "v_exp_f32 v46, v46\n"
      "v_smfmac_f32_32x32x32_bf16 v[112:127], a[32:35], v[32:39], v144\n"
      "v_exp_f32 v47, v47\n"
      "v_exp_f32 v72, v72\n"
      "v_exp_f32 v73, v73\n"
      "v_smfmac_f32_32x32x32_bf16 v[128:143], a[32:35], v[64:71], v144\n"
      "ds_read_b128 a[32:35], v145 offset:18432\n"
      "v_exp_f32 v74, v74\n"
      "v_exp_f32 v75, v75\n"
      "v_exp_f32 v76, v76\n"
      "v_smfmac_f32_16x16x64_bf16 a[0:3], a[40:43], v[32:39], v144\n"
      "v_exp_f32 v77, v77\n"
      "v_exp_f32 v78, v78\n"
      "v_smfmac_f32_16x16x64_bf16 a[4:7], a[40:43], v[64:71], v144\n"
      "v_exp_f32 v79, v79\n"
      "s_waitcnt lgkmcnt(0)\n"
      "v_exp_f32 v16, v16\n"
      "v_exp_f32 v17, v17\n"
      "v_smfmac_f32_32x32x32_bf16 v[112:127], a[36:39], v[40:47], v144\n"
      "v_exp_f32 v18, v18\n"
      "v_exp_f32 v19, v19\n"
      "v_exp_f32 v20, v20\n"
      "v_smfmac_f32_32x32x32_bf16 v[128:143], a[36:39], v[72:79], v144\n"
      "ds_read_b128 a[36:39], v145 offset:19456\n"
      "v_exp_f32 v21, v21\n"
      "v_exp_f32 v22, v22\n"
      "v_exp_f32 v23, v23\n"
      "v_smfmac_f32_16x16x64_bf16 a[0:3], a[40:43], v[40:47], v144\n"
      "v_exp_f32 v48, v48\n"
      "v_smfmac_f32_16x16x64_bf16 a[4:7], a[40:43], v[72:79], v144\n"
      "v_exp_f32 v49, v49\n"
      "v_exp_f32 v50, v50\n"
      "v_mfma_f32_32x32x16_bf16 v[32:47], a[24:27], a[8:11], v[80:95]\n"
      "v_exp_f32 v51, v51\n"
      "v_exp_f32 v52, v52\n"
      "v_exp_f32 v53, v53\n"
      "v_mfma_f32_32x32x16_bf16 v[64:79], a[24:27], a[16:19], v[96:111]\n"
      "ds_read_b128 a[24:27], v145 offset:24576\n"
      "v_exp_f32 v54, v54\n"
      "v_exp_f32 v55, v55\n"
      "v_exp_f32 v24, v24\n"
      "v_mfma_f32_32x32x16_bf16 v[32:47], a[28:31], a[12:15], v[32:47]\n"
      "v_exp_f32 v25, v25\n"
      "v_exp_f32 v26, v26\n"
      "v_exp_f32 v27, v27\n"
      "v_mfma_f32_32x32x16_bf16 v[64:79], a[28:31], a[20:23], v[64:79]\n"
      "ds_read_b128 a[28:31], v145 offset:25600\n"
      "v_exp_f32 v28, v28\n"
      "v_exp_f32 v29, v29\n"
      "v_exp_f32 v30, v30\n"
      "v_smfmac_f32_32x32x32_bf16 v[112:127], a[32:35], v[16:23], v144\n"
      "v_exp_f32 v31, v31\n"
      "v_exp_f32 v56, v56\n"
      "v_exp_f32 v57, v57\n"
      "v_smfmac_f32_32x32x32_bf16 v[128:143], a[32:35], v[48:55], v144\n"
      "ds_read_b128 a[32:35], v145 offset:22528\n"
      "v_exp_f32 v58, v58\n"
      "v_exp_f32 v59, v59\n"
      "v_exp_f32 v60, v60\n"
      "v_smfmac_f32_16x16x64_bf16 a[0:3], a[40:43], v[16:23], v144\n"
      "v_exp_f32 v61, v61\n"
      "v_exp_f32 v62, v62\n"
      "v_smfmac_f32_16x16x64_bf16 a[4:7], a[40:43], v[48:55], v144\n"
      "v_exp_f32 v63, v63\n"
      "s_waitcnt lgkmcnt(0)\n"
      "v_exp_f32 v32, v32\n"
      "v_exp_f32 v33, v33\n"
      "v_smfmac_f32_32x32x32_bf16 v[112:127], a[36:39], v[24:31], v144\n"
      "v_exp_f32 v34, v34\n"
      "v_exp_f32 v35, v35\n"
      "v_exp_f32 v36, v36\n"
      "v_smfmac_f32_32x32x32_bf16 v[128:143], a[36:39], v[56:63], v144\n"
      "ds_read_b128 a[36:39], v145 offset:23552\n"
      "v_exp_f32 v37, v37\n"
      "v_exp_f32 v38, v38\n"
      "v_exp_f32 v39, v39\n"
      "v_smfmac_f32_16x16x64_bf16 a[0:3], a[40:43], v[24:31], v144\n"
      "v_exp_f32 v64, v64\n"
      "v_smfmac_f32_16x16x64_bf16 a[4:7], a[40:43], v[56:63], v144\n"
      "v_exp_f32 v65, v65\n"
      "v_exp_f32 v66, v66\n"
      "v_mfma_f32_32x32x16_bf16 v[16:31], a[24:27], a[8:11], v[80:95]\n"
      "v_exp_f32 v67, v67\n"
      "v_exp_f32 v68, v68\n"
      "v_exp_f32 v69, v69\n"
      "v_mfma_f32_32x32x16_bf16 v[48:63], a[24:27], a[16:19], v[96:111]\n"
      "ds_read_b128 a[24:27], v145 offset:28672\n"
      "v_exp_f32 v70, v70\n"
      "v_exp_f32 v71, v71\n"
      "v_exp_f32 v40, v40\n"
      "v_mfma_f32_32x32x16_bf16 v[16:31], a[28:31], a[12:15], v[16:31]\n"
      "v_exp_f32 v41, v41\n"
      "v_exp_f32 v42, v42\n"
      "v_exp_f32 v43, v43\n"
      "v_mfma_f32_32x32x16_bf16 v[48:63], a[28:31], a[20:23], v[48:63]\n"
      "ds_read_b128 a[28:31], v145 offset:29696\n"
      "v_exp_f32 v44, v44\n"
      "v_exp_f32 v45, v45\n"
      "v_exp_f32 v46, v46\n"
      "v_smfmac_f32_32x32x32_bf16 v[112:127], a[32:35], v[32:39], v144\n"
      "v_exp_f32 v47, v47\n"
      "v_exp_f32 v72, v72\n"
      "v_exp_f32 v73, v73\n"
      "v_smfmac_f32_32x32x32_bf16 v[128:143], a[32:35], v[64:71], v144\n"
      "ds_read_b128 a[32:35], v145 offset:26624\n"
      "v_exp_f32 v74, v74\n"
      "v_exp_f32 v75, v75\n"
      "v_exp_f32 v76, v76\n"
      "v_smfmac_f32_16x16x64_bf16 a[0:3], a[40:43], v[32:39], v144\n"
      "v_exp_f32 v77, v77\n"
      "v_exp_f32 v78, v78\n"
      "v_smfmac_f32_16x16x64_bf16 a[4:7], a[40:43], v[64:71], v144\n"
      "v_exp_f32 v79, v79\n"
      "s_waitcnt lgkmcnt(0)\n"
      "s_barrier\n"
      "v_exp_f32 v16, v16\n"
      "v_exp_f32 v17, v17\n"
      "v_smfmac_f32_32x32x32_bf16 v[112:127], a[36:39], v[40:47], v144\n"
      "v_exp_f32 v18, v18\n"
      "v_exp_f32 v19, v19\n"
      "v_exp_f32 v20, v20\n"
      "v_smfmac_f32_32x32x32_bf16 v[128:143], a[36:39], v[72:79], v144\n"
      "ds_read_b128 a[36:39], v145 offset:27648\n"
      "v_exp_f32 v21, v21\n"
      "v_exp_f32 v22, v22\n"
      "v_exp_f32 v23, v23\n"
      "v_smfmac_f32_16x16x64_bf16 a[0:3], a[40:43], v[40:47], v144\n"
      "v_exp_f32 v48, v48\n"
      "v_smfmac_f32_16x16x64_bf16 a[4:7], a[40:43], v[72:79], v144\n"
      "v_exp_f32 v49, v49\n"
      "v_exp_f32 v50, v50\n"
      "v_mfma_f32_32x32x16_bf16 v[32:47], a[24:27], a[8:11], v[80:95]\n"
      "v_exp_f32 v51, v51\n"
      "v_exp_f32 v52, v52\n"
      "v_exp_f32 v53, v53\n"
      "v_mfma_f32_32x32x16_bf16 v[64:79], a[24:27], a[16:19], v[96:111]\n"
      "ds_read_b128 a[24:27], v145 offset:32768\n"
      "v_exp_f32 v54, v54\n"
      "v_exp_f32 v55, v55\n"
      "v_exp_f32 v24, v24\n"
      "v_mfma_f32_32x32x16_bf16 v[32:47], a[28:31], a[12:15], v[32:47]\n"
      "v_exp_f32 v25, v25\n"
      "v_exp_f32 v26, v26\n"
      "v_exp_f32 v27, v27\n"
      "v_mfma_f32_32x32x16_bf16 v[64:79], a[28:31], a[20:23], v[64:79]\n"
      "ds_read_b128 a[28:31], v145 offset:33792\n"
      "v_exp_f32 v28, v28\n"
      "v_exp_f32 v29, v29\n"
      "v_exp_f32 v30, v30\n"
      "v_smfmac_f32_32x32x32_bf16 v[112:127], a[32:35], v[16:23], v144\n"
      "v_exp_f32 v31, v31\n"
      "v_exp_f32 v56, v56\n"
      "v_exp_f32 v57, v57\n"
      "v_smfmac_f32_32x32x32_bf16 v[128:143], a[32:35], v[48:55], v144\n"
      "ds_read_b128 a[32:35], v145 offset:30720\n"
      "v_exp_f32 v58, v58\n"
      "v_exp_f32 v59, v59\n"
      "v_exp_f32 v60, v60\n"
      "v_smfmac_f32_16x16x64_bf16 a[0:3], a[40:43], v[16:23], v144\n"
      "v_exp_f32 v61, v61\n"
      "v_exp_f32 v62, v62\n"
      "v_smfmac_f32_16x16x64_bf16 a[4:7], a[40:43], v[48:55], v144\n"
      "v_exp_f32 v63, v63\n"
      "s_waitcnt lgkmcnt(0)\n"
      "v_exp_f32 v32, v32\n"
      "v_exp_f32 v33, v33\n"
      "v_smfmac_f32_32x32x32_bf16 v[112:127], a[36:39], v[24:31], v144\n"
      "v_exp_f32 v34, v34\n"
      "v_exp_f32 v35, v35\n"
      "v_exp_f32 v36, v36\n"
      "v_smfmac_f32_32x32x32_bf16 v[128:143], a[36:39], v[56:63], v144\n"
      "ds_read_b128 a[36:39], v145 offset:31744\n"
      "v_exp_f32 v37, v37\n"
      "v_exp_f32 v38, v38\n"
      "v_exp_f32 v39, v39\n"
      "v_smfmac_f32_16x16x64_bf16 a[0:3], a[40:43], v[24:31], v144\n"
      "v_exp_f32 v64, v64\n"
      "v_smfmac_f32_16x16x64_bf16 a[4:7], a[40:43], v[56:63], v144\n"
      "v_exp_f32 v65, v65\n"
      "v_exp_f32 v66, v66\n"
      "v_mfma_f32_32x32x16_bf16 v[16:31], a[24:27], a[8:11], v[80:95]\n"
      "v_exp_f32 v67, v67\n"
      "v_exp_f32 v68, v68\n"
      "v_exp_f32 v69, v69\n"
      "v_mfma_f32_32x32x16_bf16 v[48:63], a[24:27], a[16:19], v[96:111]\n"
      "ds_read_b128 a[24:27], v145 offset:36864\n"
      "v_exp_f32 v70, v70\n"
      "v_exp_f32 v71, v71\n"
      "v_exp_f32 v40, v40\n"
      "v_mfma_f32_32x32x16_bf16 v[16:31], a[28:31], a[12:15], v[16:31]\n"
      "v_exp_f32 v41, v41\n"
      "v_exp_f32 v42, v42\n"
      "v_exp_f32 v43, v43\n"
      "v_mfma_f32_32x32x16_bf16 v[48:63], a[28:31], a[20:23], v[48:63]\n"
      "ds_read_b128 a[28:31], v145 offset:37888\n"
      "v_exp_f32 v44, v44\n"
      "v_exp_f32 v45, v45\n"
      "v_exp_f32 v46, v46\n"
      "v_smfmac_f32_32x32x32_bf16 v[112:127], a[32:35], v[32:39], v144\n"
      "v_exp_f32 v47, v47\n"
      "v_exp_f32 v72, v72\n"
      "v_exp_f32 v73, v73\n"
      "v_smfmac_f32_32x32x32_bf16 v[128:143], a[32:35], v[64:71], v144\n"
      "ds_read_b128 a[32:35], v145 offset:34816\n"
      "v_exp_f32 v74, v74\n"
      "v_exp_f32 v75, v75\n"
      "v_exp_f32 v76, v76\n"
      "v_smfmac_f32_16x16x64_bf16 a[0:3], a[40:43], v[32:39], v144\n"
      "v_exp_f32 v77, v77\n"
      "v_exp_f32 v78, v78\n"
      "v_smfmac_f32_16x16x64_bf16 a[4:7], a[40:43], v[64:71], v144\n"
      "v_exp_f32 v79, v79\n"
      "s_waitcnt lgkmcnt(0)\n"
      "v_exp_f32 v16, v16\n"
      "v_exp_f32 v17, v17\n"
      "v_smfmac_f32_32x32x32_bf16 v[112:127], a[36:39], v[40:47], v144\n"
      "v_exp_f32 v18, v18\n"
      "v_exp_f32 v19, v19\n"
      "v_exp_f32 v20, v20\n"
      "v_smfmac_f32_32x32x32_bf16 v[128:143], a[36:39], v[72:79], v144\n"
      "ds_read_b128 a[36:39], v145 offset:35840\n"
      "v_exp_f32 v21, v21\n"
      "v_exp_f32 v22, v22\n"
      "v_exp_f32 v23, v23\n"
      "v_smfmac_f32_16x16x64_bf16 a[0:3], a[40:43], v[40:47], v144\n"
      "v_exp_f32 v48, v48\n"
      "v_smfmac_f32_16x16x64_bf16 a[4:7], a[40:43], v[72:79], v144\n"
      "v_exp_f32 v49, v49\n"
      "v_exp_f32 v50, v50\n"
      "v_mfma_f32_32x32x16_bf16 v[32:47], a[24:27], a[8:11], v[80:95]\n"
      "v_exp_f32 v51, v51\n"
      "v_exp_f32 v52, v52\n"
      "v_exp_f32 v53, v53\n"
      "v_mfma_f32_32x32x16_bf16 v[64:79], a[24:27], a[16:19], v[96:111]\n"
      "ds_read_b128 a[24:27], v145 offset:40960\n"
      "v_exp_f32 v54, v54\n"
      "v_exp_f32 v55, v55\n"
      "v_exp_f32 v24, v24\n"
      "v_mfma_f32_32x32x16_bf16 v[32:47], a[28:31], a[12:15], v[32:47]\n"
      "v_exp_f32 v25, v25\n"
      "v_exp_f32 v26, v26\n"
      "v_exp_f32 v27, v27\n"
      "v_mfma_f32_32x32x16_bf16 v[64:79], a[28:31], a[20:23], v[64:79]\n"
      "ds_read_b128 a[28:31], v145 offset:41984\n"
      "v_exp_f32 v28, v28\n"
      "v_exp_f32 v29, v29\n"
      "v_exp_f32 v30, v30\n"
      "v_smfmac_f32_32x32x32_bf16 v[112:127], a[32:35], v[16:23], v144\n"
      "v_exp_f32 v31, v31\n"
      "v_exp_f32 v56, v56\n"
      "v_exp_f32 v57, v57\n"
      "v_smfmac_f32_32x32x32_bf16 v[128:143], a[32:35], v[48:55], v144\n"
      "ds_read_b128 a[32:35], v145 offset:38912\n"
      "v_exp_f32 v58, v58\n"
      "v_exp_f32 v59, v59\n"
      "v_exp_f32 v60, v60\n"
      "v_smfmac_f32_16x16x64_bf16 a[0:3], a[40:43], v[16:23], v144\n"
      "v_exp_f32 v61, v61\n"
      "v_exp_f32 v62, v62\n"
      "v_smfmac_f32_16x16x64_bf16 a[4:7], a[40:43], v[48:55], v144\n"
      "v_exp_f32 v63, v63\n"
      "s_waitcnt lgkmcnt(0)\n"
      "v_exp_f32 v32, v32\n"
      "v_exp_f32 v33, v33\n"
      "v_smfmac_f32_32x32x32_bf16 v[112:127], a[36:39], v[24:31], v144\n"
      "v_exp_f32 v34, v34\n"
      "v_exp_f32 v35, v35\n"
      "v_exp_f32 v36, v36\n"
      "v_smfmac_f32_32x32x32_bf16 v[128:143], a[36:39], v[56:63], v144\n"
      "ds_read_b128 a[36:39], v145 offset:39936\n"
      "v_exp_f32 v37, v37\n"
      "v_exp_f32 v38, v38\n"
      "v_exp_f32 v39, v39\n"
      "v_smfmac_f32_16x16x64_bf16 a[0:3], a[40:43], v[24:31], v144\n"
      "v_exp_f32 v64, v64\n"
      "v_smfmac_f32_16x16x64_bf16 a[4:7], a[40:43], v[56:63], v144\n"
      "v_exp_f32 v65, v65\n"
      "v_exp_f32 v66, v66\n"
      "v_mfma_f32_32x32x16_bf16 v[16:31], a[24:27], a[8:11], v[80:95]\n"
      "v_exp_f32 v67, v67\n"
      "v_exp_f32 v68, v68\n"
      "v_exp_f32 v69, v69\n"
      "v_mfma_f32_32x32x16_bf16 v[48:63], a[24:27], a[16:19], v[96:111]\n"
      "ds_read_b128 a[24:27], v145 offset:45056\n"
      "v_exp_f32 v70, v70\n"
      "v_exp_f32 v71, v71\n"
      "v_exp_f32 v40, v40\n"
      "v_mfma_f32_32x32x16_bf16 v[16:31], a[28:31], a[12:15], v[16:31]\n"
      "v_exp_f32 v41, v41\n"
      "v_exp_f32 v42, v42\n"
      "v_exp_f32 v43, v43\n"
      "v_mfma_f32_32x32x16_bf16 v[48:63], a[28:31], a[20:23], v[48:63]\n"
      "ds_read_b128 a[28:31], v145 offset:46080\n"
      "v_exp_f32 v44, v44\n"
      "v_exp_f32 v45, v45\n"
      "v_exp_f32 v46, v46\n"
      "v_smfmac_f32_32x32x32_bf16 v[112:127], a[32:35], v[32:39], v144\n"
      "v_exp_f32 v47, v47\n"
      "v_exp_f32 v72, v72\n"
      "v_exp_f32 v73, v73\n"
      "v_smfmac_f32_32x32x32_bf16 v[128:143], a[32:35], v[64:71], v144\n"
      "ds_read_b128 a[32:35], v145 offset:43008\n"
      "v_exp_f32 v74, v74\n"
      "v_exp_f32 v75, v75\n"
      "v_exp_f32 v76, v76\n"
      "v_smfmac_f32_16x16x64_bf16 a[0:3], a[40:43], v[32:39], v144\n"
      "v_exp_f32 v77, v77\n"
      "v_exp_f32 v78, v78\n"
      "v_smfmac_f32_16x16x64_bf16 a[4:7], a[40:43], v[64:71], v144\n"
      "v_exp_f32 v79, v79\n"
      "s_waitcnt lgkmcnt(0)\n"
      "s_barrier\n"
      "v_exp_f32 v16, v16\n"
      "v_exp_f32 v17, v17\n"
      "v_smfmac_f32_32x32x32_bf16 v[112:127], a[36:39], v[40:47], v144\n"
      "s_add_u32 s92, s94, 0\n"
      "v_exp_f32 v18, v18\n"
      "v_exp_f32 v19, v19\n"
      "v_exp_f32 v20, v20\n"
      "v_smfmac_f32_32x32x32_bf16 v[128:143], a[36:39], v[72:79], v144\n"
      "ds_read_b128 a[36:39], v145 offset:44032\n"
      "v_exp_f32 v21, v21\n"
      "v_exp_f32 v22, v22\n"
      "v_exp_f32 v23, v23\n"
      "v_smfmac_f32_16x16x64_bf16 a[0:3], a[40:43], v[40:47], v144\n"
      "v_exp_f32 v48, v48\n"
      "v_smfmac_f32_16x16x64_bf16 a[4:7], a[40:43], v[72:79], v144\n"
      "v_exp_f32 v49, v49\n"
      "v_exp_f32 v50, v50\n"
      "v_mfma_f32_32x32x16_bf16 v[32:47], a[24:27], a[8:11], v[80:95]\n"
      "buffer_load_dwordx4 v[2:5], v153, s[40:43], s92 offen\n"
      "v_exp_f32 v51, v51\n"
      "v_exp_f32 v52, v52\n"
      "v_exp_f32 v53, v53\n"
      "v_mfma_f32_32x32x16_bf16 v[64:79], a[24:27], a[16:19], v[96:111]\n"
      "ds_read_b128 a[24:27], v145 offset:49152\n"
      "v_exp_f32 v54, v54\n"
      "v_exp_f32 v55, v55\n"
      "v_exp_f32 v24, v24\n"
      "v_mfma_f32_32x32x16_bf16 v[32:47], a[28:31], a[12:15], v[32:47]\n"
      "v_exp_f32 v25, v25\n"
      "v_exp_f32 v26, v26\n"
      "v_exp_f32 v27, v27\n"
      "v_mfma_f32_32x32x16_bf16 v[64:79], a[28:31], a[20:23], v[64:79]\n"
      "ds_read_b128 a[28:31], v145 offset:50176\n"
      "v_exp_f32 v28, v28\n"
      "v_exp_f32 v29, v29\n"
      "v_exp_f32 v30, v30\n"
      "v_smfmac_f32_32x32x32_bf16 v[112:127], a[32:35], v[16:23], v144\n"
      "v_exp_f32 v31, v31\n"
      "v_exp_f32 v56, v56\n"
      "v_exp_f32 v57, v57\n"
      "v_smfmac_f32_32x32x32_bf16 v[128:143], a[32:35], v[48:55], v144\n"
      "ds_read_b128 a[32:35], v145 offset:47104\n"
      "v_exp_f32 v58, v58\n"
      "v_exp_f32 v59, v59\n"
      "v_exp_f32 v60, v60\n"
      "v_smfmac_f32_16x16x64_bf16 a[0:3], a[40:43], v[16:23], v144\n"
      "v_exp_f32 v61, v61\n"
      "v_exp_f32 v62, v62\n"
      "v_smfmac_f32_16x16x64_bf16 a[4:7], a[40:43], v[48:55], v144\n"
      "v_exp_f32 v63, v63\n"
      "s_waitcnt lgkmcnt(0)\n"
      "v_exp_f32 v32, v32\n"
      "v_exp_f32 v33, v33\n"
      "v_smfmac_f32_32x32x32_bf16 v[112:127], a[36:39], v[24:31], v144\n"
      "s_add_u32 s92, s94, 32768\n"
      "v_exp_f32 v34, v34\n"
      "v_exp_f32 v35, v35\n"
      "v_exp_f32 v36, v36\n"
      "v_smfmac_f32_32x32x32_bf16 v[128:143], a[36:39], v[56:63], v144\n"
      "ds_read_b128 a[36:39], v145 offset:48128\n"
      "v_exp_f32 v37, v37\n"
      "v_exp_f32 v38, v38\n"
      "v_exp_f32 v39, v39\n"
      "v_smfmac_f32_16x16x64_bf16 a[0:3], a[40:43], v[24:31], v144\n"
      "v_exp_f32 v64, v64\n"
      "v_smfmac_f32_16x16x64_bf16 a[4:7], a[40:43], v[56:63], v144\n"
      "v_exp_f32 v65, v65\n"
      "v_exp_f32 v66, v66\n"
      "v_mfma_f32_32x32x16_bf16 v[16:31], a[24:27], a[8:11], v[80:95]\n"
      "buffer_load_dwordx4 v[6:9], v153, s[40:43], s92 offen\n"
      "v_exp_f32 v67, v67\n"
      "v_exp_f32 v68, v68\n"
      "v_exp_f32 v69, v69\n"
      "v_mfma_f32_32x32x16_bf16 v[48:63], a[24:27], a[16:19], v[96:111]\n"
      "ds_read_b128 a[24:27], v145 offset:53248\n"
      "v_exp_f32 v70, v70\n"
      "v_exp_f32 v71, v71\n"
      "v_exp_f32 v40, v40\n"
      "v_mfma_f32_32x32x16_bf16 v[16:31], a[28:31], a[12:15], v[16:31]\n"
      "v_exp_f32 v41, v41\n"
      "v_exp_f32 v42, v42\n"
      "v_exp_f32 v43, v43\n"
      "v_mfma_f32_32x32x16_bf16 v[48:63], a[28:31], a[20:23], v[48:63]\n"
      "ds_read_b128 a[28:31], v145 offset:54272\n"
      "v_exp_f32 v44, v44\n"
      "v_exp_f32 v45, v45\n"
      "v_exp_f32 v46, v46\n"
      "v_smfmac_f32_32x32x32_bf16 v[112:127], a[32:35], v[32:39], v144\n"
      "v_exp_f32 v47, v47\n"
      "v_exp_f32 v72, v72\n"
      "v_exp_f32 v73, v73\n"
      "v_smfmac_f32_32x32x32_bf16 v[128:143], a[32:35], v[64:71], v144\n"
      "ds_read_b128 a[32:35], v145 offset:51200\n"
      "v_exp_f32 v74, v74\n"
      "v_exp_f32 v75, v75\n"
      "v_exp_f32 v76, v76\n"
      "v_smfmac_f32_16x16x64_bf16 a[0:3], a[40:43], v[32:39], v144\n"
      "v_exp_f32 v77, v77\n"
      "v_exp_f32 v78, v78\n"
      "v_smfmac_f32_16x16x64_bf16 a[4:7], a[40:43], v[64:71], v144\n"
      "v_exp_f32 v79, v79\n"
      "s_waitcnt lgkmcnt(0)\n"
      "v_exp_f32 v16, v16\n"
      "v_exp_f32 v17, v17\n"
      "v_smfmac_f32_32x32x32_bf16 v[112:127], a[36:39], v[40:47], v144\n"
      "s_add_u32 s92, s94, 65536\n"
      "v_exp_f32 v18, v18\n"
      "v_exp_f32 v19, v19\n"
      "v_exp_f32 v20, v20\n"
      "v_smfmac_f32_32x32x32_bf16 v[128:143], a[36:39], v[72:79], v144\n"
      "ds_read_b128 a[36:39], v145 offset:52224\n"
      "v_exp_f32 v21, v21\n"
      "v_exp_f32 v22, v22\n"
      "v_exp_f32 v23, v23\n"
      "v_smfmac_f32_16x16x64_bf16 a[0:3], a[40:43], v[40:47], v144\n"
      "v_exp_f32 v48, v48\n"
      "v_smfmac_f32_16x16x64_bf16 a[4:7], a[40:43], v[72:79], v144\n"
      "v_exp_f32 v49, v49\n"
      "v_exp_f32 v50, v50\n"
      "v_mfma_f32_32x32x16_bf16 v[32:47], a[24:27], a[8:11], v[80:95]\n"
      "buffer_load_dwordx4 v[10:13], v153, s[40:43], s92 offen\n"
      "v_exp_f32 v51, v51\n"
      "v_exp_f32 v52, v52\n"
      "v_exp_f32 v53, v53\n"
      "v_mfma_f32_32x32x16_bf16 v[64:79], a[24:27], a[16:19], v[96:111]\n"
      "ds_read_b128 a[24:27], v145 offset:57344\n"
      "v_exp_f32 v54, v54\n"
      "v_exp_f32 v55, v55\n"
      "v_exp_f32 v24, v24\n"
      "v_mfma_f32_32x32x16_bf16 v[32:47], a[28:31], a[12:15], v[32:47]\n"
      "v_exp_f32 v25, v25\n"
      "v_exp_f32 v26, v26\n"
      "v_exp_f32 v27, v27\n"
      "v_mfma_f32_32x32x16_bf16 v[64:79], a[28:31], a[20:23], v[64:79]\n"
      "ds_read_b128 a[28:31], v145 offset:58368\n"
      "v_exp_f32 v28, v28\n"
      "v_exp_f32 v29, v29\n"
      "v_exp_f32 v30, v30\n"
      "v_smfmac_f32_32x32x32_bf16 v[112:127], a[32:35], v[16:23], v144\n"
      "v_exp_f32 v31, v31\n"
      "v_exp_f32 v56, v56\n"
      "v_exp_f32 v57, v57\n"
      "v_smfmac_f32_32x32x32_bf16 v[128:143], a[32:35], v[48:55], v144\n"
      "ds_read_b128 a[32:35], v145 offset:55296\n"
      "v_exp_f32 v58, v58\n"
      "v_exp_f32 v59, v59\n"
      "v_exp_f32 v60, v60\n"
      "v_smfmac_f32_16x16x64_bf16 a[0:3], a[40:43], v[16:23], v144\n"
      "v_exp_f32 v61, v61\n"
      "v_exp_f32 v62, v62\n"
      "v_smfmac_f32_16x16x64_bf16 a[4:7], a[40:43], v[48:55], v144\n"
      "v_exp_f32 v63, v63\n"
      "s_waitcnt lgkmcnt(0)\n"
      "v_exp_f32 v32, v32\n"
      "v_exp_f32 v33, v33\n"
      "v_smfmac_f32_32x32x32_bf16 v[112:127], a[36:39], v[24:31], v144\n"
      "s_add_u32 s92, s94, 98304\n"
      "v_exp_f32 v34, v34\n"
      "v_exp_f32 v35, v35\n"
      "v_exp_f32 v36, v36\n"
      "v_smfmac_f32_32x32x32_bf16 v[128:143], a[36:39], v[56:63], v144\n"
      "ds_read_b128 a[36:39], v145 offset:56320\n"
      "v_exp_f32 v37, v37\n"
      "v_exp_f32 v38, v38\n"
      "v_exp_f32 v39, v39\n"
      "v_smfmac_f32_16x16x64_bf16 a[0:3], a[40:43], v[24:31], v144\n"
      "v_exp_f32 v64, v64\n"
      "v_smfmac_f32_16x16x64_bf16 a[4:7], a[40:43], v[56:63], v144\n"
      "v_exp_f32 v65, v65\n"
      "v_exp_f32 v66, v66\n"
      "v_mfma_f32_32x32x16_bf16 v[16:31], a[24:27], a[8:11], v[80:95]\n"
      "buffer_load_dwordx4 v[148:151], v153, s[40:43], s92 offen\n"
      "v_exp_f32 v67, v67\n"
      "v_exp_f32 v68, v68\n"
      "v_exp_f32 v69, v69\n"
      "v_mfma_f32_32x32x16_bf16 v[48:63], a[24:27], a[16:19], v[96:111]\n"
      "ds_read_b128 a[24:27], v145 offset:61440\n"
      "v_exp_f32 v70, v70\n"
      "v_exp_f32 v71, v71\n"
      "v_exp_f32 v40, v40\n"
      "v_mfma_f32_32x32x16_bf16 v[16:31], a[28:31], a[12:15], v[16:31]\n"
      "v_exp_f32 v41, v41\n"
      "v_exp_f32 v42, v42\n"
      "v_exp_f32 v43, v43\n"
      "v_mfma_f32_32x32x16_bf16 v[48:63], a[28:31], a[20:23], v[48:63]\n"
      "ds_read_b128 a[28:31], v145 offset:62464\n"
      "v_exp_f32 v44, v44\n"
      "v_exp_f32 v45, v45\n"
      "v_exp_f32 v46, v46\n"
      "v_smfmac_f32_32x32x32_bf16 v[112:127], a[32:35], v[32:39], v144\n"
      "v_exp_f32 v47, v47\n"
      "v_exp_f32 v72, v72\n"
      "v_exp_f32 v73, v73\n"
      "v_smfmac_f32_32x32x32_bf16 v[128:143], a[32:35], v[64:71], v144\n"
      "ds_read_b128 a[32:35], v145 offset:59392\n"
      "v_exp_f32 v74, v74\n"
      "v_exp_f32 v75, v75\n"
      "v_exp_f32 v76, v76\n"
      "v_smfmac_f32_16x16x64_bf16 a[0:3], a[40:43], v[32:39], v144\n"
      "v_exp_f32 v77, v77\n"
      "v_exp_f32 v78, v78\n"
      "v_smfmac_f32_16x16x64_bf16 a[4:7], a[40:43], v[64:71], v144\n"
      "v_exp_f32 v79, v79\n"
      "s_waitcnt lgkmcnt(0)\n"
      "v_exp_f32 v16, v16\n"
      "v_exp_f32 v17, v17\n"
      "v_smfmac_f32_32x32x32_bf16 v[112:127], a[36:39], v[40:47], v144\n"
      "v_exp_f32 v18, v18\n"
      "v_exp_f32 v19, v19\n"
      "v_exp_f32 v20, v20\n"
      "v_smfmac_f32_32x32x32_bf16 v[128:143], a[36:39], v[72:79], v144\n"
      "ds_read_b128 a[36:39], v145 offset:60416\n"
      "v_exp_f32 v21, v21\n"
      "v_exp_f32 v22, v22\n"
      "v_exp_f32 v23, v23\n"
      "v_smfmac_f32_16x16x64_bf16 a[0:3], a[40:43], v[40:47], v144\n"
      "v_exp_f32 v48, v48\n"
      "v_smfmac_f32_16x16x64_bf16 a[4:7], a[40:43], v[72:79], v144\n"
      "v_exp_f32 v49, v49\n"
      "v_exp_f32 v50, v50\n"
      "v_mfma_f32_32x32x16_bf16 v[32:47], a[24:27], a[8:11], v[80:95]\n"
      "v_exp_f32 v51, v51\n"
      "v_exp_f32 v52, v52\n"
      "v_exp_f32 v53, v53\n"
      "v_mfma_f32_32x32x16_bf16 v[64:79], a[24:27], a[16:19], v[96:111]\n"
      "v_exp_f32 v54, v54\n"
      "v_exp_f32 v55, v55\n"
      "v_exp_f32 v24, v24\n"
      "v_mfma_f32_32x32x16_bf16 v[32:47], a[28:31], a[12:15], v[32:47]\n"
      "v_exp_f32 v25, v25\n"
      "v_exp_f32 v26, v26\n"
      "v_exp_f32 v27, v27\n"
      "v_mfma_f32_32x32x16_bf16 v[64:79], a[28:31], a[20:23], v[64:79]\n"
      "v_exp_f32 v28, v28\n"
      "v_exp_f32 v29, v29\n"
      "v_exp_f32 v30, v30\n"
      "v_smfmac_f32_32x32x32_bf16 v[112:127], a[32:35], v[16:23], v144\n"
      "v_exp_f32 v31, v31\n"
      "v_exp_f32 v56, v56\n"
      "v_exp_f32 v57, v57\n"
      "v_smfmac_f32_32x32x32_bf16 v[128:143], a[32:35], v[48:55], v144\n"
      "ds_read_b128 a[32:35], v145 offset:63488\n"
      "v_exp_f32 v58, v58\n"
      "v_exp_f32 v59, v59\n"
      "v_exp_f32 v60, v60\n"
      "v_smfmac_f32_16x16x64_bf16 a[0:3], a[40:43], v[16:23], v144\n"
      "v_exp_f32 v61, v61\n"
      "v_exp_f32 v62, v62\n"
      "v_smfmac_f32_16x16x64_bf16 a[4:7], a[40:43], v[48:55], v144\n"
      "v_exp_f32 v63, v63\n"
      "s_waitcnt lgkmcnt(0)\n"
      "v_exp_f32 v32, v32\n"
      "v_exp_f32 v33, v33\n"
      "v_smfmac_f32_32x32x32_bf16 v[112:127], a[36:39], v[24:31], v144\n"
      "v_exp_f32 v34, v34\n"
      "v_exp_f32 v35, v35\n"
      "v_exp_f32 v36, v36\n"
      "v_smfmac_f32_32x32x32_bf16 v[128:143], a[36:39], v[56:63], v144\n"
      "ds_read_b128 a[36:39], v145 offset:64512\n"
      "v_exp_f32 v37, v37\n"
      "v_exp_f32 v38, v38\n"
      "v_exp_f32 v39, v39\n"
      "v_smfmac_f32_16x16x64_bf16 a[0:3], a[40:43], v[24:31], v144\n"
      "v_exp_f32 v64, v64\n"
      "v_smfmac_f32_16x16x64_bf16 a[4:7], a[40:43], v[56:63], v144\n"
      "v_exp_f32 v65, v65\n"
      "v_exp_f32 v66, v66\n"
      "v_exp_f32 v67, v67\n"
      "v_exp_f32 v68, v68\n"
      "v_exp_f32 v69, v69\n"
      "v_exp_f32 v70, v70\n"
      "v_exp_f32 v71, v71\n"
      "v_exp_f32 v40, v40\n"
      "v_exp_f32 v41, v41\n"
      "v_exp_f32 v42, v42\n"
      "v_exp_f32 v43, v43\n"
      "v_exp_f32 v44, v44\n"
      "v_exp_f32 v45, v45\n"
      "v_exp_f32 v46, v46\n"
      "v_smfmac_f32_32x32x32_bf16 v[112:127], a[32:35], v[32:39], v144\n"
      "v_exp_f32 v47, v47\n"
      "v_exp_f32 v72, v72\n"
      "v_exp_f32 v73, v73\n"
      "v_smfmac_f32_32x32x32_bf16 v[128:143], a[32:35], v[64:71], v144\n"
      "v_exp_f32 v74, v74\n"
      "v_exp_f32 v75, v75\n"
      "v_exp_f32 v76, v76\n"
      "v_smfmac_f32_16x16x64_bf16 a[0:3], a[40:43], v[32:39], v144\n"
      "v_exp_f32 v77, v77\n"
      "v_exp_f32 v78, v78\n"
      "v_smfmac_f32_16x16x64_bf16 a[4:7], a[40:43], v[64:71], v144\n"
      "v_exp_f32 v79, v79\n"
      "s_waitcnt lgkmcnt(0)\n"
      "v_smfmac_f32_32x32x32_bf16 v[112:127], a[36:39], v[40:47], v144\n"
      "v_smfmac_f32_32x32x32_bf16 v[128:143], a[36:39], v[72:79], v144\n"
      "v_smfmac_f32_16x16x64_bf16 a[0:3], a[40:43], v[40:47], v144\n"
      "v_smfmac_f32_16x16x64_bf16 a[4:7], a[40:43], v[72:79], v144\n"
      "v_lshrrev_b32 v154, 2, v1\n"
      "v_lshlrev_b32 v154, 3, v154\n"
      "v_add_u32 v154, 163328, v154\n"
      "s_nop 5\n"
      "v_pk_mul_f32 v[16:17], v[112:113], 0\n"
      "v_pk_mul_f32 v[18:19], v[128:129], 0\n"
      "v_pk_fma_f32 v[16:17], v[114:115], 0, v[16:17]\n"
      "v_pk_fma_f32 v[18:19], v[130:131], 0, v[18:19]\n"
      "v_pk_fma_f32 v[16:17], v[116:117], 0, v[16:17]\n"
      "v_pk_fma_f32 v[18:19], v[132:133], 0, v[18:19]\n"
      "v_pk_fma_f32 v[16:17], v[118:119], 0, v[16:17]\n"
      "v_pk_fma_f32 v[18:19], v[134:135], 0, v[18:19]\n"
      "v_pk_fma_f32 v[16:17], v[120:121], 0, v[16:17]\n"
      "v_pk_fma_f32 v[18:19], v[136:137], 0, v[18:19]\n"
      "v_pk_fma_f32 v[16:17], v[122:123], 0, v[16:17]\n"
      "v_pk_fma_f32 v[18:19], v[138:139], 0, v[18:19]\n"
      "v_pk_fma_f32 v[16:17], v[124:125], 0, v[16:17]\n"
      "v_pk_fma_f32 v[18:19], v[140:141], 0, v[18:19]\n"
      "v_pk_fma_f32 v[16:17], v[126:127], 0, v[16:17]\n"
      "v_pk_fma_f32 v[18:19], v[142:143], 0, v[18:19]\n"
      "v_accvgpr_read_b32 v20, a0\n"
      "v_accvgpr_read_b32 v21, a4\n"
      "v_add_f32 v16, v16, v17\n"
      "v_add_f32 v18, v18, v19\n"
      "v_add_f32 v16, v16, v20\n"
      "v_add_f32 v18, v18, v21\n"
      "v_mov_b32 v22, 0x5d800000\n"
      "v_cmp_nle_f32 vcc, v16, v22\n"
      "v_cmp_nle_f32 s[80:81], v18, v22\n"
      "s_nop 0\n"
      "s_or_b64 vcc, vcc, s[80:81]\n"
      "s_nop 2\n"
      "s_cbranch_vccnz .Lfallback\n"
      ".Lnormalize:\n"
      "v_accvgpr_read_b32 v20, a0\n"
      "v_accvgpr_read_b32 v22, a4\n"
      "v_rcp_f32 v20, v20\n"
      "v_rcp_f32 v22, v22\n"
      "v_mov_b32 v21, v20\n"
      "v_mov_b32 v23, v22\n"
      "v_pk_mul_f32 v[112:113], v[112:113], v[20:21]\n"
      "v_pk_mul_f32 v[114:115], v[114:115], v[20:21]\n"
      "v_pk_mul_f32 v[116:117], v[116:117], v[20:21]\n"
      "v_pk_mul_f32 v[118:119], v[118:119], v[20:21]\n"
      "v_pk_mul_f32 v[120:121], v[120:121], v[20:21]\n"
      "v_pk_mul_f32 v[122:123], v[122:123], v[20:21]\n"
      "v_pk_mul_f32 v[124:125], v[124:125], v[20:21]\n"
      "v_pk_mul_f32 v[126:127], v[126:127], v[20:21]\n"
      "v_cvt_pk_bf16_f32 v64, v112, v113\n"
      "v_cvt_pk_bf16_f32 v65, v114, v115\n"
      "v_cvt_pk_bf16_f32 v66, v116, v117\n"
      "v_cvt_pk_bf16_f32 v67, v118, v119\n"
      "v_cvt_pk_bf16_f32 v68, v120, v121\n"
      "v_cvt_pk_bf16_f32 v69, v122, v123\n"
      "v_cvt_pk_bf16_f32 v70, v124, v125\n"
      "v_cvt_pk_bf16_f32 v71, v126, v127\n"
      "buffer_store_dwordx4 v[64:67], v145, s[48:51], s22 offen offset:0 sc1\n"
      "buffer_store_dwordx4 v[68:71], v145, s[48:51], s22 offen offset:2048 sc1\n"
      "v_pk_mul_f32 v[128:129], v[128:129], v[22:23]\n"
      "v_pk_mul_f32 v[130:131], v[130:131], v[22:23]\n"
      "v_pk_mul_f32 v[132:133], v[132:133], v[22:23]\n"
      "v_pk_mul_f32 v[134:135], v[134:135], v[22:23]\n"
      "v_pk_mul_f32 v[136:137], v[136:137], v[22:23]\n"
      "v_pk_mul_f32 v[138:139], v[138:139], v[22:23]\n"
      "v_pk_mul_f32 v[140:141], v[140:141], v[22:23]\n"
      "v_pk_mul_f32 v[142:143], v[142:143], v[22:23]\n"
      "v_cvt_pk_bf16_f32 v72, v128, v129\n"
      "v_cvt_pk_bf16_f32 v73, v130, v131\n"
      "v_cvt_pk_bf16_f32 v74, v132, v133\n"
      "v_cvt_pk_bf16_f32 v75, v134, v135\n"
      "v_cvt_pk_bf16_f32 v76, v136, v137\n"
      "v_cvt_pk_bf16_f32 v77, v138, v139\n"
      "v_cvt_pk_bf16_f32 v78, v140, v141\n"
      "v_cvt_pk_bf16_f32 v79, v142, v143\n"
      "buffer_store_dwordx4 v[72:75], v145, s[48:51], s23 offen offset:0 sc1\n"
      "buffer_store_dwordx4 v[76:79], v145, s[48:51], s23 offen offset:2048 sc1\n"
      "v_bfe_u32 v16, v1, 2, 2\n"
      "v_lshrrev_b32 v17, 5, v1\n"
      "v_lshl_add_u32 v16, v17, 2, v16\n"
      "v_lshlrev_b32 v16, 6, v16\n"
      "v_bfe_u32 v17, v1, 4, 1\n"
      "v_lshl_add_u32 v16, v17, 5, v16\n"
      "v_and_b32 v17, 3, v1\n"
      "v_lshl_add_u32 v16, v17, 3, v16\n"
      "s_lshl_b32 s80, s30, 14\n"
      "s_add_u32 s80, s80, 65536\n"
      "v_add_u32 v17, s80, v16\n"
      "s_mul_i32 s80, s30, 7168\n"
      "s_add_u32 s80, s80, 131072\n"
      "v_add_u32 v18, s80, v16\n"
      "ds_read_b64_tr_b16 a[0:1], v17 offset:0\n"
      "ds_read_b64_tr_b16 a[2:3], v17 offset:512\n"
      "ds_read_b64_tr_b16 a[4:5], v17 offset:1024\n"
      "ds_read_b64_tr_b16 a[6:7], v17 offset:1536\n"
      "ds_read_b64_tr_b16 a[8:9], v17 offset:2048\n"
      "ds_read_b64_tr_b16 a[10:11], v17 offset:2560\n"
      "ds_read_b64_tr_b16 a[12:13], v17 offset:3072\n"
      "ds_read_b64_tr_b16 a[14:15], v17 offset:3584\n"
      "ds_read_b64_tr_b16 a[16:17], v17 offset:4096\n"
      "ds_read_b64_tr_b16 a[18:19], v17 offset:4608\n"
      "ds_read_b64_tr_b16 a[20:21], v17 offset:5120\n"
      "ds_read_b64_tr_b16 a[22:23], v17 offset:5632\n"
      "ds_read_b64_tr_b16 a[24:25], v17 offset:6144\n"
      "ds_read_b64_tr_b16 a[26:27], v17 offset:6656\n"
      "ds_read_b64_tr_b16 a[28:29], v17 offset:7168\n"
      "ds_read_b64_tr_b16 a[30:31], v17 offset:7680\n"
      "ds_read_b64_tr_b16 a[32:33], v17 offset:8192\n"
      "ds_read_b64_tr_b16 a[34:35], v17 offset:8704\n"
      "ds_read_b64_tr_b16 a[36:37], v17 offset:9216\n"
      "ds_read_b64_tr_b16 a[38:39], v17 offset:9728\n"
      "ds_read_b64_tr_b16 a[40:41], v17 offset:10240\n"
      "ds_read_b64_tr_b16 a[42:43], v17 offset:10752\n"
      "ds_read_b64_tr_b16 a[44:45], v17 offset:11264\n"
      "ds_read_b64_tr_b16 a[46:47], v17 offset:11776\n"
      "ds_read_b64_tr_b16 a[48:49], v17 offset:12288\n"
      "ds_read_b64_tr_b16 a[50:51], v17 offset:12800\n"
      "ds_read_b64_tr_b16 a[52:53], v17 offset:13312\n"
      "ds_read_b64_tr_b16 a[54:55], v17 offset:13824\n"
      "ds_read_b64_tr_b16 a[56:57], v17 offset:14336\n"
      "ds_read_b64_tr_b16 a[58:59], v17 offset:14848\n"
      "ds_read_b64_tr_b16 a[60:61], v17 offset:15360\n"
      "ds_read_b64_tr_b16 a[62:63], v17 offset:15872\n"
      "ds_read_b64_tr_b16 a[64:65], v18 offset:0\n"
      "ds_read_b64_tr_b16 a[66:67], v18 offset:512\n"
      "ds_read_b64_tr_b16 a[68:69], v18 offset:1024\n"
      "ds_read_b64_tr_b16 a[70:71], v18 offset:1536\n"
      "ds_read_b64 v[24:25], v154 offset:0\n"
      "ds_read_b64 v[26:27], v154 offset:128\n"
      "ds_read_b64 v[28:29], v154 offset:256\n"
      "ds_read_b64 v[30:31], v154 offset:384\n"
      "v_lshlrev_b32 v20, 26, v1\n"
      "v_mov_b32 v21, s36\n"
      "s_waitcnt lgkmcnt(0)\n"
      "s_barrier\n"
      "s_waitcnt vmcnt(0)\n"
      "buffer_store_dword v21, v20, s[48:51], s24 offen sc1\n"
      "s_or_b32 s81, s39, s30\n"
      "s_min_u32 s81, s81, 1\n"
      "s_lshl_b32 s81, s81, 31\n"
      "v_or_b32 v20, s81, v20\n"
      "buffer_store_dword v21, v20, s[48:51], 0 offen sc1\n"
      "v_lshlrev_b32 v80, 16, v2\n"
      "v_and_b32 v81, 0xffff0000, v2\n"
      "v_lshlrev_b32 v82, 16, v3\n"
      "v_and_b32 v83, 0xffff0000, v3\n"
      "v_lshlrev_b32 v84, 16, v4\n"
      "v_and_b32 v85, 0xffff0000, v4\n"
      "v_lshlrev_b32 v86, 16, v5\n"
      "v_and_b32 v87, 0xffff0000, v5\n"
      "v_sub_f32 v112, v80, v24\n"
      "v_sub_f32 v113, v81, v24\n"
      "v_sub_f32 v114, v82, v24\n"
      "v_sub_f32 v115, v83, v24\n"
      "v_sub_f32 v116, v84, v24\n"
      "v_sub_f32 v117, v85, v24\n"
      "v_sub_f32 v118, v86, v24\n"
      "v_sub_f32 v119, v87, v24\n"
      "v_cvt_pk_bf16_f32 v32, v112, v113\n"
      "v_cvt_pk_bf16_f32 v33, v114, v115\n"
      "v_cvt_pk_bf16_f32 v34, v116, v117\n"
      "v_cvt_pk_bf16_f32 v35, v118, v119\n"
      "v_lshlrev_b32 v112, 16, v32\n"
      "v_and_b32 v113, 0xffff0000, v32\n"
      "v_lshlrev_b32 v114, 16, v33\n"
      "v_and_b32 v115, 0xffff0000, v33\n"
      "v_lshlrev_b32 v116, 16, v34\n"
      "v_and_b32 v117, 0xffff0000, v34\n"
      "v_lshlrev_b32 v118, 16, v35\n"
      "v_and_b32 v119, 0xffff0000, v35\n"
      "v_mul_f32 v112, v112, v25\n"
      "v_mul_f32 v113, v113, v25\n"
      "v_mul_f32 v114, v114, v25\n"
      "v_mul_f32 v115, v115, v25\n"
      "v_mul_f32 v116, v116, v25\n"
      "v_mul_f32 v117, v117, v25\n"
      "v_mul_f32 v118, v118, v25\n"
      "v_mul_f32 v119, v119, v25\n"
      "v_cvt_pk_bf16_f32 v32, v112, v113\n"
      "v_cvt_pk_bf16_f32 v33, v114, v115\n"
      "v_cvt_pk_bf16_f32 v34, v116, v117\n"
      "v_cvt_pk_bf16_f32 v35, v118, v119\n"
      "v_lshlrev_b32 v112, 16, v32\n"
      "v_and_b32 v113, 0xffff0000, v32\n"
      "v_lshlrev_b32 v114, 16, v33\n"
      "v_and_b32 v115, 0xffff0000, v33\n"
      "v_lshlrev_b32 v116, 16, v34\n"
      "v_and_b32 v117, 0xffff0000, v34\n"
      "v_lshlrev_b32 v118, 16, v35\n"
      "v_and_b32 v119, 0xffff0000, v35\n"
      "v_mul_f32 v32, 0xbfb8aa3b, v112\n"
      "v_mul_f32 v33, 0xbfb8aa3b, v113\n"
      "v_mul_f32 v34, 0xbfb8aa3b, v114\n"
      "v_mul_f32 v35, 0xbfb8aa3b, v115\n"
      "v_mul_f32 v36, 0xbfb8aa3b, v116\n"
      "v_mul_f32 v37, 0xbfb8aa3b, v117\n"
      "v_mul_f32 v38, 0xbfb8aa3b, v118\n"
      "v_mul_f32 v39, 0xbfb8aa3b, v119\n"
      "v_exp_f32 v32, v32\n"
      "v_exp_f32 v33, v33\n"
      "v_exp_f32 v34, v34\n"
      "v_exp_f32 v35, v35\n"
      "v_exp_f32 v36, v36\n"
      "v_exp_f32 v37, v37\n"
      "v_exp_f32 v38, v38\n"
      "v_exp_f32 v39, v39\n"
      "v_add_f32 v32, 1.0, v32\n"
      "v_add_f32 v33, 1.0, v33\n"
      "v_add_f32 v34, 1.0, v34\n"
      "v_add_f32 v35, 1.0, v35\n"
      "v_add_f32 v36, 1.0, v36\n"
      "v_add_f32 v37, 1.0, v37\n"
      "v_add_f32 v38, 1.0, v38\n"
      "v_add_f32 v39, 1.0, v39\n"
      "v_rcp_f32 v32, v32\n"
      "v_rcp_f32 v33, v33\n"
      "v_rcp_f32 v34, v34\n"
      "v_rcp_f32 v35, v35\n"
      "v_rcp_f32 v36, v36\n"
      "v_rcp_f32 v37, v37\n"
      "v_rcp_f32 v38, v38\n"
      "v_rcp_f32 v39, v39\n"
      "v_mul_f32 v112, v112, v32\n"
      "v_mul_f32 v113, v113, v33\n"
      "v_mul_f32 v114, v114, v34\n"
      "v_mul_f32 v115, v115, v35\n"
      "v_mul_f32 v116, v116, v36\n"
      "v_mul_f32 v117, v117, v37\n"
      "v_mul_f32 v118, v118, v38\n"
      "v_mul_f32 v119, v119, v39\n"
      "v_lshlrev_b32 v88, 16, v6\n"
      "v_and_b32 v89, 0xffff0000, v6\n"
      "v_lshlrev_b32 v90, 16, v7\n"
      "v_and_b32 v91, 0xffff0000, v7\n"
      "v_lshlrev_b32 v92, 16, v8\n"
      "v_and_b32 v93, 0xffff0000, v8\n"
      "v_lshlrev_b32 v94, 16, v9\n"
      "v_and_b32 v95, 0xffff0000, v9\n"
      "v_sub_f32 v120, v88, v26\n"
      "v_sub_f32 v121, v89, v26\n"
      "v_sub_f32 v122, v90, v26\n"
      "v_sub_f32 v123, v91, v26\n"
      "v_sub_f32 v124, v92, v26\n"
      "v_sub_f32 v125, v93, v26\n"
      "v_sub_f32 v126, v94, v26\n"
      "v_sub_f32 v127, v95, v26\n"
      "v_cvt_pk_bf16_f32 v32, v120, v121\n"
      "v_cvt_pk_bf16_f32 v33, v122, v123\n"
      "v_cvt_pk_bf16_f32 v34, v124, v125\n"
      "v_cvt_pk_bf16_f32 v35, v126, v127\n"
      "v_lshlrev_b32 v120, 16, v32\n"
      "v_and_b32 v121, 0xffff0000, v32\n"
      "v_lshlrev_b32 v122, 16, v33\n"
      "v_and_b32 v123, 0xffff0000, v33\n"
      "v_lshlrev_b32 v124, 16, v34\n"
      "v_and_b32 v125, 0xffff0000, v34\n"
      "v_lshlrev_b32 v126, 16, v35\n"
      "v_and_b32 v127, 0xffff0000, v35\n"
      "v_mul_f32 v120, v120, v27\n"
      "v_mul_f32 v121, v121, v27\n"
      "v_mul_f32 v122, v122, v27\n"
      "v_mul_f32 v123, v123, v27\n"
      "v_mul_f32 v124, v124, v27\n"
      "v_mul_f32 v125, v125, v27\n"
      "v_mul_f32 v126, v126, v27\n"
      "v_mul_f32 v127, v127, v27\n"
      "v_cvt_pk_bf16_f32 v32, v120, v121\n"
      "v_cvt_pk_bf16_f32 v33, v122, v123\n"
      "v_cvt_pk_bf16_f32 v34, v124, v125\n"
      "v_cvt_pk_bf16_f32 v35, v126, v127\n"
      "v_lshlrev_b32 v120, 16, v32\n"
      "v_and_b32 v121, 0xffff0000, v32\n"
      "v_lshlrev_b32 v122, 16, v33\n"
      "v_and_b32 v123, 0xffff0000, v33\n"
      "v_lshlrev_b32 v124, 16, v34\n"
      "v_and_b32 v125, 0xffff0000, v34\n"
      "v_lshlrev_b32 v126, 16, v35\n"
      "v_and_b32 v127, 0xffff0000, v35\n"
      "v_mul_f32 v32, 0xbfb8aa3b, v120\n"
      "v_mul_f32 v33, 0xbfb8aa3b, v121\n"
      "v_mul_f32 v34, 0xbfb8aa3b, v122\n"
      "v_mul_f32 v35, 0xbfb8aa3b, v123\n"
      "v_mul_f32 v36, 0xbfb8aa3b, v124\n"
      "v_mul_f32 v37, 0xbfb8aa3b, v125\n"
      "v_mul_f32 v38, 0xbfb8aa3b, v126\n"
      "v_mul_f32 v39, 0xbfb8aa3b, v127\n"
      "v_exp_f32 v32, v32\n"
      "v_exp_f32 v33, v33\n"
      "v_exp_f32 v34, v34\n"
      "v_exp_f32 v35, v35\n"
      "v_exp_f32 v36, v36\n"
      "v_exp_f32 v37, v37\n"
      "v_exp_f32 v38, v38\n"
      "v_exp_f32 v39, v39\n"
      "v_add_f32 v32, 1.0, v32\n"
      "v_add_f32 v33, 1.0, v33\n"
      "v_add_f32 v34, 1.0, v34\n"
      "v_add_f32 v35, 1.0, v35\n"
      "v_add_f32 v36, 1.0, v36\n"
      "v_add_f32 v37, 1.0, v37\n"
      "v_add_f32 v38, 1.0, v38\n"
      "v_add_f32 v39, 1.0, v39\n"
      "v_rcp_f32 v32, v32\n"
      "v_rcp_f32 v33, v33\n"
      "v_rcp_f32 v34, v34\n"
      "v_rcp_f32 v35, v35\n"
      "v_rcp_f32 v36, v36\n"
      "v_rcp_f32 v37, v37\n"
      "v_rcp_f32 v38, v38\n"
      "v_rcp_f32 v39, v39\n"
      "v_mul_f32 v120, v120, v32\n"
      "v_mul_f32 v121, v121, v33\n"
      "v_mul_f32 v122, v122, v34\n"
      "v_mul_f32 v123, v123, v35\n"
      "v_mul_f32 v124, v124, v36\n"
      "v_mul_f32 v125, v125, v37\n"
      "v_mul_f32 v126, v126, v38\n"
      "v_mul_f32 v127, v127, v39\n"
      "v_lshlrev_b32 v96, 16, v10\n"
      "v_and_b32 v97, 0xffff0000, v10\n"
      "v_lshlrev_b32 v98, 16, v11\n"
      "v_and_b32 v99, 0xffff0000, v11\n"
      "v_lshlrev_b32 v100, 16, v12\n"
      "v_and_b32 v101, 0xffff0000, v12\n"
      "v_lshlrev_b32 v102, 16, v13\n"
      "v_and_b32 v103, 0xffff0000, v13\n"
      "v_sub_f32 v128, v96, v28\n"
      "v_sub_f32 v129, v97, v28\n"
      "v_sub_f32 v130, v98, v28\n"
      "v_sub_f32 v131, v99, v28\n"
      "v_sub_f32 v132, v100, v28\n"
      "v_sub_f32 v133, v101, v28\n"
      "v_sub_f32 v134, v102, v28\n"
      "v_sub_f32 v135, v103, v28\n"
      "v_cvt_pk_bf16_f32 v32, v128, v129\n"
      "v_cvt_pk_bf16_f32 v33, v130, v131\n"
      "v_cvt_pk_bf16_f32 v34, v132, v133\n"
      "v_cvt_pk_bf16_f32 v35, v134, v135\n"
      "v_lshlrev_b32 v128, 16, v32\n"
      "v_and_b32 v129, 0xffff0000, v32\n"
      "v_lshlrev_b32 v130, 16, v33\n"
      "v_and_b32 v131, 0xffff0000, v33\n"
      "v_lshlrev_b32 v132, 16, v34\n"
      "v_and_b32 v133, 0xffff0000, v34\n"
      "v_lshlrev_b32 v134, 16, v35\n"
      "v_and_b32 v135, 0xffff0000, v35\n"
      "v_mul_f32 v128, v128, v29\n"
      "v_mul_f32 v129, v129, v29\n"
      "v_mul_f32 v130, v130, v29\n"
      "v_mul_f32 v131, v131, v29\n"
      "v_mul_f32 v132, v132, v29\n"
      "v_mul_f32 v133, v133, v29\n"
      "v_mul_f32 v134, v134, v29\n"
      "v_mul_f32 v135, v135, v29\n"
      "v_cvt_pk_bf16_f32 v32, v128, v129\n"
      "v_cvt_pk_bf16_f32 v33, v130, v131\n"
      "v_cvt_pk_bf16_f32 v34, v132, v133\n"
      "v_cvt_pk_bf16_f32 v35, v134, v135\n"
      "v_lshlrev_b32 v128, 16, v32\n"
      "v_and_b32 v129, 0xffff0000, v32\n"
      "v_lshlrev_b32 v130, 16, v33\n"
      "v_and_b32 v131, 0xffff0000, v33\n"
      "v_lshlrev_b32 v132, 16, v34\n"
      "v_and_b32 v133, 0xffff0000, v34\n"
      "v_lshlrev_b32 v134, 16, v35\n"
      "v_and_b32 v135, 0xffff0000, v35\n"
      "v_mul_f32 v32, 0xbfb8aa3b, v128\n"
      "v_mul_f32 v33, 0xbfb8aa3b, v129\n"
      "v_mul_f32 v34, 0xbfb8aa3b, v130\n"
      "v_mul_f32 v35, 0xbfb8aa3b, v131\n"
      "v_mul_f32 v36, 0xbfb8aa3b, v132\n"
      "v_mul_f32 v37, 0xbfb8aa3b, v133\n"
      "v_mul_f32 v38, 0xbfb8aa3b, v134\n"
      "v_mul_f32 v39, 0xbfb8aa3b, v135\n"
      "v_exp_f32 v32, v32\n"
      "v_exp_f32 v33, v33\n"
      "v_exp_f32 v34, v34\n"
      "v_exp_f32 v35, v35\n"
      "v_exp_f32 v36, v36\n"
      "v_exp_f32 v37, v37\n"
      "v_exp_f32 v38, v38\n"
      "v_exp_f32 v39, v39\n"
      "v_add_f32 v32, 1.0, v32\n"
      "v_add_f32 v33, 1.0, v33\n"
      "v_add_f32 v34, 1.0, v34\n"
      "v_add_f32 v35, 1.0, v35\n"
      "v_add_f32 v36, 1.0, v36\n"
      "v_add_f32 v37, 1.0, v37\n"
      "v_add_f32 v38, 1.0, v38\n"
      "v_add_f32 v39, 1.0, v39\n"
      "v_rcp_f32 v32, v32\n"
      "v_rcp_f32 v33, v33\n"
      "v_rcp_f32 v34, v34\n"
      "v_rcp_f32 v35, v35\n"
      "v_rcp_f32 v36, v36\n"
      "v_rcp_f32 v37, v37\n"
      "v_rcp_f32 v38, v38\n"
      "v_rcp_f32 v39, v39\n"
      "v_mul_f32 v128, v128, v32\n"
      "v_mul_f32 v129, v129, v33\n"
      "v_mul_f32 v130, v130, v34\n"
      "v_mul_f32 v131, v131, v35\n"
      "v_mul_f32 v132, v132, v36\n"
      "v_mul_f32 v133, v133, v37\n"
      "v_mul_f32 v134, v134, v38\n"
      "v_mul_f32 v135, v135, v39\n"
      "v_lshlrev_b32 v104, 16, v148\n"
      "v_and_b32 v105, 0xffff0000, v148\n"
      "v_lshlrev_b32 v106, 16, v149\n"
      "v_and_b32 v107, 0xffff0000, v149\n"
      "v_lshlrev_b32 v108, 16, v150\n"
      "v_and_b32 v109, 0xffff0000, v150\n"
      "v_lshlrev_b32 v110, 16, v151\n"
      "v_and_b32 v111, 0xffff0000, v151\n"
      "v_sub_f32 v136, v104, v30\n"
      "v_sub_f32 v137, v105, v30\n"
      "v_sub_f32 v138, v106, v30\n"
      "v_sub_f32 v139, v107, v30\n"
      "v_sub_f32 v140, v108, v30\n"
      "v_sub_f32 v141, v109, v30\n"
      "v_sub_f32 v142, v110, v30\n"
      "v_sub_f32 v143, v111, v30\n"
      "v_cvt_pk_bf16_f32 v32, v136, v137\n"
      "v_cvt_pk_bf16_f32 v33, v138, v139\n"
      "v_cvt_pk_bf16_f32 v34, v140, v141\n"
      "v_cvt_pk_bf16_f32 v35, v142, v143\n"
      "v_lshlrev_b32 v136, 16, v32\n"
      "v_and_b32 v137, 0xffff0000, v32\n"
      "v_lshlrev_b32 v138, 16, v33\n"
      "v_and_b32 v139, 0xffff0000, v33\n"
      "v_lshlrev_b32 v140, 16, v34\n"
      "v_and_b32 v141, 0xffff0000, v34\n"
      "v_lshlrev_b32 v142, 16, v35\n"
      "v_and_b32 v143, 0xffff0000, v35\n"
      "v_mul_f32 v136, v136, v31\n"
      "v_mul_f32 v137, v137, v31\n"
      "v_mul_f32 v138, v138, v31\n"
      "v_mul_f32 v139, v139, v31\n"
      "v_mul_f32 v140, v140, v31\n"
      "v_mul_f32 v141, v141, v31\n"
      "v_mul_f32 v142, v142, v31\n"
      "v_mul_f32 v143, v143, v31\n"
      "v_cvt_pk_bf16_f32 v32, v136, v137\n"
      "v_cvt_pk_bf16_f32 v33, v138, v139\n"
      "v_cvt_pk_bf16_f32 v34, v140, v141\n"
      "v_cvt_pk_bf16_f32 v35, v142, v143\n"
      "v_lshlrev_b32 v136, 16, v32\n"
      "v_and_b32 v137, 0xffff0000, v32\n"
      "v_lshlrev_b32 v138, 16, v33\n"
      "v_and_b32 v139, 0xffff0000, v33\n"
      "v_lshlrev_b32 v140, 16, v34\n"
      "v_and_b32 v141, 0xffff0000, v34\n"
      "v_lshlrev_b32 v142, 16, v35\n"
      "v_and_b32 v143, 0xffff0000, v35\n"
      "v_mul_f32 v32, 0xbfb8aa3b, v136\n"
      "v_mul_f32 v33, 0xbfb8aa3b, v137\n"
      "v_mul_f32 v34, 0xbfb8aa3b, v138\n"
      "v_mul_f32 v35, 0xbfb8aa3b, v139\n"
      "v_mul_f32 v36, 0xbfb8aa3b, v140\n"
      "v_mul_f32 v37, 0xbfb8aa3b, v141\n"
      "v_mul_f32 v38, 0xbfb8aa3b, v142\n"
      "v_mul_f32 v39, 0xbfb8aa3b, v143\n"
      "v_exp_f32 v32, v32\n"
      "v_exp_f32 v33, v33\n"
      "v_exp_f32 v34, v34\n"
      "v_exp_f32 v35, v35\n"
      "v_exp_f32 v36, v36\n"
      "v_exp_f32 v37, v37\n"
      "v_exp_f32 v38, v38\n"
      "v_exp_f32 v39, v39\n"
      "v_add_f32 v32, 1.0, v32\n"
      "v_add_f32 v33, 1.0, v33\n"
      "v_add_f32 v34, 1.0, v34\n"
      "v_add_f32 v35, 1.0, v35\n"
      "v_add_f32 v36, 1.0, v36\n"
      "v_add_f32 v37, 1.0, v37\n"
      "v_add_f32 v38, 1.0, v38\n"
      "v_add_f32 v39, 1.0, v39\n"
      "v_rcp_f32 v32, v32\n"
      "v_rcp_f32 v33, v33\n"
      "v_rcp_f32 v34, v34\n"
      "v_rcp_f32 v35, v35\n"
      "v_rcp_f32 v36, v36\n"
      "v_rcp_f32 v37, v37\n"
      "v_rcp_f32 v38, v38\n"
      "v_rcp_f32 v39, v39\n"
      "v_mul_f32 v136, v136, v32\n"
      "v_mul_f32 v137, v137, v33\n"
      "v_mul_f32 v138, v138, v34\n"
      "v_mul_f32 v139, v139, v35\n"
      "v_mul_f32 v140, v140, v36\n"
      "v_mul_f32 v141, v141, v37\n"
      "v_mul_f32 v142, v142, v38\n"
      "v_mul_f32 v143, v143, v39\n"
      "v_add_u32 v146, 0x10000, v145\n"
      "s_lshl_b32 s85, s30, 15\n"
      "s_add_u32 s85, s85, 0\n"
      "v_and_b32 v2, 31, v1\n"
      "v_lshrrev_b32 v3, 5, v1\n"
      "v_and_b32 v4, 7, v1\n"
      "v_xor_b32 v3, v3, v4\n"
      "v_lshlrev_b32 v3, 4, v3\n"
      "v_lshl_add_u32 v2, v2, 7, v3\n"
      "v_add_u32 v6, s85, v2\n"
      "v_xor_b32 v7, 32, v6\n"
      "v_xor_b32 v8, 64, v6\n"
      "v_xor_b32 v9, 96, v6\n"
      "v_lshrrev_b32 v2, 2, v1\n"
      "v_and_b32 v3, 3, v1\n"
      "v_lshlrev_b32 v3, 1, v3\n"
      "v_and_b32 v4, 7, v2\n"
      "v_xor_b32 v3, v3, v4\n"
      "v_lshlrev_b32 v3, 4, v3\n"
      "v_lshl_add_u32 v2, v2, 7, v3\n"
      "v_add_u32 v10, s85, v2\n"
      "v_xor_b32 v11, 16, v10\n"
      "s_barrier\n"
      "ds_read_b128 v[12:15], v145 offset:4096\n"
      "ds_read_b128 v[16:19], v145 offset:5120\n"
      "s_waitcnt lgkmcnt(1)\n"
      "v_mfma_f32_32x32x16_bf16 v[48:63], a[0:3], v[12:15], 0\n"
      "ds_read_b128 v[20:23], v145 offset:6144\n"
      "s_waitcnt lgkmcnt(1)\n"
      "v_mfma_f32_32x32x16_bf16 v[64:79], a[0:3], v[16:19], 0\n"
      "ds_read_b128 v[12:15], v145 offset:7168\n"
      "s_waitcnt lgkmcnt(1)\n"
      "v_mfma_f32_32x32x16_bf16 v[48:63], a[4:7], v[20:23], v[48:63]\n"
      "ds_read_b128 v[16:19], v145 offset:36864\n"
      "s_waitcnt lgkmcnt(1)\n"
      "v_mfma_f32_32x32x16_bf16 v[64:79], a[4:7], v[12:15], v[64:79]\n"
      "ds_read_b128 v[20:23], v145 offset:37888\n"
      "s_waitcnt lgkmcnt(1)\n"
      "v_mfma_f32_32x32x16_bf16 v[48:63], a[20:23], v[16:19], v[48:63]\n"
      "ds_read_b128 v[12:15], v145 offset:38912\n"
      "s_waitcnt lgkmcnt(1)\n"
      "v_mfma_f32_32x32x16_bf16 v[64:79], a[20:23], v[20:23], v[64:79]\n"
      "ds_read_b128 v[16:19], v145 offset:39936\n"
      "s_waitcnt lgkmcnt(1)\n"
      "v_mfma_f32_32x32x16_bf16 v[48:63], a[24:27], v[12:15], v[48:63]\n"
      "ds_read_b128 v[20:23], v146 offset:6144\n"
      "s_waitcnt lgkmcnt(1)\n"
      "v_mfma_f32_32x32x16_bf16 v[64:79], a[24:27], v[16:19], v[64:79]\n"
      "ds_read_b128 v[12:15], v146 offset:7168\n"
      "s_waitcnt lgkmcnt(1)\n"
      "v_mfma_f32_32x32x16_bf16 v[48:63], a[40:43], v[20:23], v[48:63]\n"
      "ds_read_b128 v[16:19], v146 offset:38912\n"
      "s_waitcnt lgkmcnt(1)\n"
      "v_mfma_f32_32x32x16_bf16 v[64:79], a[40:43], v[12:15], v[64:79]\n"
      "ds_read_b128 v[20:23], v146 offset:39936\n"
      "s_waitcnt lgkmcnt(1)\n"
      "v_mfma_f32_32x32x16_bf16 v[48:63], a[56:59], v[16:19], v[48:63]\n"
      "s_barrier\n"
      "ds_read_b128 v[12:15], v145 offset:14336\n"
      "s_waitcnt lgkmcnt(1)\n"
      "v_mfma_f32_32x32x16_bf16 v[64:79], a[56:59], v[20:23], v[64:79]\n"
      "ds_read_b128 v[16:19], v145 offset:15360\n"
      "s_waitcnt lgkmcnt(1)\n"
      "v_mfma_f32_32x32x16_bf16 v[48:63], a[8:11], v[12:15], v[48:63]\n"
      "ds_read_b128 v[20:23], v145 offset:47104\n"
      "s_waitcnt lgkmcnt(1)\n"
      "v_mfma_f32_32x32x16_bf16 v[64:79], a[8:11], v[16:19], v[64:79]\n"
      "ds_read_b128 v[12:15], v145 offset:48128\n"
      "s_waitcnt lgkmcnt(1)\n"
      "v_mfma_f32_32x32x16_bf16 v[48:63], a[28:31], v[20:23], v[48:63]\n"
      "ds_read_b128 v[16:19], v146 offset:14336\n"
      "s_waitcnt lgkmcnt(1)\n"
      "v_mfma_f32_32x32x16_bf16 v[64:79], a[28:31], v[12:15], v[64:79]\n"
      "ds_read_b128 v[20:23], v146 offset:15360\n"
      "s_waitcnt lgkmcnt(1)\n"
      "v_mfma_f32_32x32x16_bf16 v[48:63], a[44:47], v[16:19], v[48:63]\n"
      "ds_read_b128 v[12:15], v146 offset:47104\n"
      "s_waitcnt lgkmcnt(1)\n"
      "v_mfma_f32_32x32x16_bf16 v[64:79], a[44:47], v[20:23], v[64:79]\n"
      "ds_read_b128 v[16:19], v146 offset:48128\n"
      "s_waitcnt lgkmcnt(1)\n"
      "v_mfma_f32_32x32x16_bf16 v[48:63], a[60:63], v[12:15], v[48:63]\n"
      "s_barrier\n"
      "ds_read_b128 v[20:23], v145 offset:22528\n"
      "s_waitcnt lgkmcnt(1)\n"
      "v_mfma_f32_32x32x16_bf16 v[64:79], a[60:63], v[16:19], v[64:79]\n"
      "ds_read_b128 v[12:15], v145 offset:23552\n"
      "s_waitcnt lgkmcnt(1)\n"
      "v_mfma_f32_32x32x16_bf16 v[48:63], a[12:15], v[20:23], v[48:63]\n"
      "ds_read_b128 v[16:19], v145 offset:55296\n"
      "s_waitcnt lgkmcnt(1)\n"
      "v_mfma_f32_32x32x16_bf16 v[64:79], a[12:15], v[12:15], v[64:79]\n"
      "ds_read_b128 v[20:23], v145 offset:56320\n"
      "s_waitcnt lgkmcnt(1)\n"
      "v_mfma_f32_32x32x16_bf16 v[48:63], a[32:35], v[16:19], v[48:63]\n"
      "ds_read_b128 v[12:15], v146 offset:22528\n"
      "s_waitcnt lgkmcnt(1)\n"
      "v_mfma_f32_32x32x16_bf16 v[64:79], a[32:35], v[20:23], v[64:79]\n"
      "ds_read_b128 v[16:19], v146 offset:23552\n"
      "s_waitcnt lgkmcnt(1)\n"
      "v_mfma_f32_32x32x16_bf16 v[48:63], a[48:51], v[12:15], v[48:63]\n"
      "ds_read_b128 v[20:23], v146 offset:55296\n"
      "s_waitcnt lgkmcnt(1)\n"
      "v_mfma_f32_32x32x16_bf16 v[64:79], a[48:51], v[16:19], v[64:79]\n"
      "ds_read_b128 v[12:15], v146 offset:56320\n"
      "s_waitcnt lgkmcnt(1)\n"
      "v_mfma_f32_32x32x16_bf16 v[48:63], a[64:67], v[20:23], v[48:63]\n"
      "s_barrier\n"
      "ds_read_b128 v[16:19], v145 offset:30720\n"
      "s_waitcnt lgkmcnt(1)\n"
      "v_mfma_f32_32x32x16_bf16 v[64:79], a[64:67], v[12:15], v[64:79]\n"
      "ds_read_b128 v[20:23], v145 offset:31744\n"
      "s_waitcnt lgkmcnt(1)\n"
      "v_mfma_f32_32x32x16_bf16 v[48:63], a[16:19], v[16:19], v[48:63]\n"
      "ds_read_b128 v[12:15], v145 offset:63488\n"
      "s_waitcnt lgkmcnt(1)\n"
      "v_mfma_f32_32x32x16_bf16 v[64:79], a[16:19], v[20:23], v[64:79]\n"
      "ds_read_b128 v[16:19], v145 offset:64512\n"
      "s_waitcnt lgkmcnt(1)\n"
      "v_mfma_f32_32x32x16_bf16 v[48:63], a[36:39], v[12:15], v[48:63]\n"
      "ds_read_b128 v[20:23], v146 offset:30720\n"
      "s_waitcnt lgkmcnt(1)\n"
      "v_mfma_f32_32x32x16_bf16 v[64:79], a[36:39], v[16:19], v[64:79]\n"
      "ds_read_b128 v[12:15], v146 offset:31744\n"
      "s_waitcnt lgkmcnt(1)\n"
      "v_mfma_f32_32x32x16_bf16 v[48:63], a[52:55], v[20:23], v[48:63]\n"
      "ds_read_b128 v[16:19], v146 offset:63488\n"
      "s_waitcnt lgkmcnt(1)\n"
      "v_mfma_f32_32x32x16_bf16 v[64:79], a[52:55], v[12:15], v[64:79]\n"
      "ds_read_b128 v[20:23], v146 offset:64512\n"
      "s_waitcnt lgkmcnt(1)\n"
      "v_mfma_f32_32x32x16_bf16 v[48:63], a[68:71], v[16:19], v[48:63]\n"
      "s_waitcnt lgkmcnt(0)\n"
      "v_mfma_f32_32x32x16_bf16 v[64:79], a[68:71], v[20:23], v[64:79]\n"
      "s_nop 9\n"
      "ds_write_b128 v6, v[48:51] offset:0\n"
      "ds_write_b128 v7, v[52:55] offset:0\n"
      "ds_write_b128 v8, v[56:59] offset:0\n"
      "ds_write_b128 v9, v[60:63] offset:0\n"
      "ds_write_b128 v6, v[64:67] offset:4096\n"
      "ds_write_b128 v7, v[68:71] offset:4096\n"
      "ds_write_b128 v8, v[72:75] offset:4096\n"
      "ds_write_b128 v9, v[76:79] offset:4096\n"
      "s_lshl_b32 s80, s30, 7\n"
      "s_add_u32 s80, s80, 160896\n"
      "v_and_b32 v2, 3, v1\n"
      "v_lshl_add_u32 v2, v2, 5, s80\n"
      "v_mov_b32 v3, 163264\n"
      "ds_read_b128 v[40:43], v2\n"
      "ds_read_b128 v[44:47], v2 offset:16\n"
      "ds_read_b32 v4, v3\n"
      "s_waitcnt lgkmcnt(0)\n"
      "v_readfirstlane_b32 s37, v4\n"
      "s_nop 0\n"
      "s_lshr_b32 s84, s37, 6\n"
      "s_cmp_eq_u32 s84, s31\n"
      "s_nop 1\n"
      "s_cbranch_scc0 .Lnoy\n"
      "s_and_b32 s85, s37, 15\n"
      "v_lshrrev_b32 v2, 2, v1\n"
      "v_cmp_eq_u32 vcc, s85, v2\n"
      "s_bfe_u32 s85, s37, 0x20004\n"
      "s_nop 0\n"
      "v_cndmask_b32 v40, 0, v40, vcc\n"
      "v_cndmask_b32 v41, 0, v41, vcc\n"
      "v_cndmask_b32 v42, 0, v42, vcc\n"
      "v_cndmask_b32 v43, 0, v43, vcc\n"
      "v_cndmask_b32 v44, 0, v44, vcc\n"
      "v_cndmask_b32 v45, 0, v45, vcc\n"
      "v_cndmask_b32 v46, 0, v46, vcc\n"
      "v_cndmask_b32 v47, 0, v47, vcc\n"
      "s_cmp_eq_u32 s85, 0\n"
      "s_nop 1\n"
      "s_cbranch_scc0 .Lnofix0\n"
      "v_sub_f32 v112, v80, v24\n"
      "v_sub_f32 v113, v81, v24\n"
      "v_sub_f32 v114, v82, v24\n"
      "v_sub_f32 v115, v83, v24\n"
      "v_sub_f32 v116, v84, v24\n"
      "v_sub_f32 v117, v85, v24\n"
      "v_sub_f32 v118, v86, v24\n"
      "v_sub_f32 v119, v87, v24\n"
      "v_cvt_pk_bf16_f32 v32, v112, v113\n"
      "v_cvt_pk_bf16_f32 v33, v114, v115\n"
      "v_cvt_pk_bf16_f32 v34, v116, v117\n"
      "v_cvt_pk_bf16_f32 v35, v118, v119\n"
      "v_lshlrev_b32 v112, 16, v32\n"
      "v_and_b32 v113, 0xffff0000, v32\n"
      "v_lshlrev_b32 v114, 16, v33\n"
      "v_and_b32 v115, 0xffff0000, v33\n"
      "v_lshlrev_b32 v116, 16, v34\n"
      "v_and_b32 v117, 0xffff0000, v34\n"
      "v_lshlrev_b32 v118, 16, v35\n"
      "v_and_b32 v119, 0xffff0000, v35\n"
      "v_mul_f32 v112, v112, v25\n"
      "v_mul_f32 v113, v113, v25\n"
      "v_mul_f32 v114, v114, v25\n"
      "v_mul_f32 v115, v115, v25\n"
      "v_mul_f32 v116, v116, v25\n"
      "v_mul_f32 v117, v117, v25\n"
      "v_mul_f32 v118, v118, v25\n"
      "v_mul_f32 v119, v119, v25\n"
      "v_cvt_pk_bf16_f32 v32, v112, v113\n"
      "v_cvt_pk_bf16_f32 v33, v114, v115\n"
      "v_cvt_pk_bf16_f32 v34, v116, v117\n"
      "v_cvt_pk_bf16_f32 v35, v118, v119\n"
      "v_lshlrev_b32 v112, 16, v32\n"
      "v_and_b32 v113, 0xffff0000, v32\n"
      "v_lshlrev_b32 v114, 16, v33\n"
      "v_and_b32 v115, 0xffff0000, v33\n"
      "v_lshlrev_b32 v116, 16, v34\n"
      "v_and_b32 v117, 0xffff0000, v34\n"
      "v_lshlrev_b32 v118, 16, v35\n"
      "v_and_b32 v119, 0xffff0000, v35\n"
      "v_add_f32 v112, v112, v40\n"
      "v_add_f32 v113, v113, v41\n"
      "v_add_f32 v114, v114, v42\n"
      "v_add_f32 v115, v115, v43\n"
      "v_add_f32 v116, v116, v44\n"
      "v_add_f32 v117, v117, v45\n"
      "v_add_f32 v118, v118, v46\n"
      "v_add_f32 v119, v119, v47\n"
      "v_cvt_pk_bf16_f32 v32, v112, v113\n"
      "v_cvt_pk_bf16_f32 v33, v114, v115\n"
      "v_cvt_pk_bf16_f32 v34, v116, v117\n"
      "v_cvt_pk_bf16_f32 v35, v118, v119\n"
      "v_lshlrev_b32 v112, 16, v32\n"
      "v_and_b32 v113, 0xffff0000, v32\n"
      "v_lshlrev_b32 v114, 16, v33\n"
      "v_and_b32 v115, 0xffff0000, v33\n"
      "v_lshlrev_b32 v116, 16, v34\n"
      "v_and_b32 v117, 0xffff0000, v34\n"
      "v_lshlrev_b32 v118, 16, v35\n"
      "v_and_b32 v119, 0xffff0000, v35\n"
      "v_mul_f32 v32, 0xbfb8aa3b, v112\n"
      "v_mul_f32 v33, 0xbfb8aa3b, v113\n"
      "v_mul_f32 v34, 0xbfb8aa3b, v114\n"
      "v_mul_f32 v35, 0xbfb8aa3b, v115\n"
      "v_mul_f32 v36, 0xbfb8aa3b, v116\n"
      "v_mul_f32 v37, 0xbfb8aa3b, v117\n"
      "v_mul_f32 v38, 0xbfb8aa3b, v118\n"
      "v_mul_f32 v39, 0xbfb8aa3b, v119\n"
      "v_exp_f32 v32, v32\n"
      "v_exp_f32 v33, v33\n"
      "v_exp_f32 v34, v34\n"
      "v_exp_f32 v35, v35\n"
      "v_exp_f32 v36, v36\n"
      "v_exp_f32 v37, v37\n"
      "v_exp_f32 v38, v38\n"
      "v_exp_f32 v39, v39\n"
      "v_add_f32 v32, 1.0, v32\n"
      "v_add_f32 v33, 1.0, v33\n"
      "v_add_f32 v34, 1.0, v34\n"
      "v_add_f32 v35, 1.0, v35\n"
      "v_add_f32 v36, 1.0, v36\n"
      "v_add_f32 v37, 1.0, v37\n"
      "v_add_f32 v38, 1.0, v38\n"
      "v_add_f32 v39, 1.0, v39\n"
      "v_rcp_f32 v32, v32\n"
      "v_rcp_f32 v33, v33\n"
      "v_rcp_f32 v34, v34\n"
      "v_rcp_f32 v35, v35\n"
      "v_rcp_f32 v36, v36\n"
      "v_rcp_f32 v37, v37\n"
      "v_rcp_f32 v38, v38\n"
      "v_rcp_f32 v39, v39\n"
      "v_mul_f32 v112, v112, v32\n"
      "v_mul_f32 v113, v113, v33\n"
      "v_mul_f32 v114, v114, v34\n"
      "v_mul_f32 v115, v115, v35\n"
      "v_mul_f32 v116, v116, v36\n"
      "v_mul_f32 v117, v117, v37\n"
      "v_mul_f32 v118, v118, v38\n"
      "v_mul_f32 v119, v119, v39\n"
      "s_nop 2\n"
      ".Lnofix0:\n"
      "s_cmp_eq_u32 s85, 1\n"
      "s_cbranch_scc0 .Lnofix1\n"
      "v_sub_f32 v120, v88, v26\n"
      "v_sub_f32 v121, v89, v26\n"
      "v_sub_f32 v122, v90, v26\n"
      "v_sub_f32 v123, v91, v26\n"
      "v_sub_f32 v124, v92, v26\n"
      "v_sub_f32 v125, v93, v26\n"
      "v_sub_f32 v126, v94, v26\n"
      "v_sub_f32 v127, v95, v26\n"
      "v_cvt_pk_bf16_f32 v32, v120, v121\n"
      "v_cvt_pk_bf16_f32 v33, v122, v123\n"
      "v_cvt_pk_bf16_f32 v34, v124, v125\n"
      "v_cvt_pk_bf16_f32 v35, v126, v127\n"
      "v_lshlrev_b32 v120, 16, v32\n"
      "v_and_b32 v121, 0xffff0000, v32\n"
      "v_lshlrev_b32 v122, 16, v33\n"
      "v_and_b32 v123, 0xffff0000, v33\n"
      "v_lshlrev_b32 v124, 16, v34\n"
      "v_and_b32 v125, 0xffff0000, v34\n"
      "v_lshlrev_b32 v126, 16, v35\n"
      "v_and_b32 v127, 0xffff0000, v35\n"
      "v_mul_f32 v120, v120, v27\n"
      "v_mul_f32 v121, v121, v27\n"
      "v_mul_f32 v122, v122, v27\n"
      "v_mul_f32 v123, v123, v27\n"
      "v_mul_f32 v124, v124, v27\n"
      "v_mul_f32 v125, v125, v27\n"
      "v_mul_f32 v126, v126, v27\n"
      "v_mul_f32 v127, v127, v27\n"
      "v_cvt_pk_bf16_f32 v32, v120, v121\n"
      "v_cvt_pk_bf16_f32 v33, v122, v123\n"
      "v_cvt_pk_bf16_f32 v34, v124, v125\n"
      "v_cvt_pk_bf16_f32 v35, v126, v127\n"
      "v_lshlrev_b32 v120, 16, v32\n"
      "v_and_b32 v121, 0xffff0000, v32\n"
      "v_lshlrev_b32 v122, 16, v33\n"
      "v_and_b32 v123, 0xffff0000, v33\n"
      "v_lshlrev_b32 v124, 16, v34\n"
      "v_and_b32 v125, 0xffff0000, v34\n"
      "v_lshlrev_b32 v126, 16, v35\n"
      "v_and_b32 v127, 0xffff0000, v35\n"
      "v_add_f32 v120, v120, v40\n"
      "v_add_f32 v121, v121, v41\n"
      "v_add_f32 v122, v122, v42\n"
      "v_add_f32 v123, v123, v43\n"
      "v_add_f32 v124, v124, v44\n"
      "v_add_f32 v125, v125, v45\n"
      "v_add_f32 v126, v126, v46\n"
      "v_add_f32 v127, v127, v47\n"
      "v_cvt_pk_bf16_f32 v32, v120, v121\n"
      "v_cvt_pk_bf16_f32 v33, v122, v123\n"
      "v_cvt_pk_bf16_f32 v34, v124, v125\n"
      "v_cvt_pk_bf16_f32 v35, v126, v127\n"
      "v_lshlrev_b32 v120, 16, v32\n"
      "v_and_b32 v121, 0xffff0000, v32\n"
      "v_lshlrev_b32 v122, 16, v33\n"
      "v_and_b32 v123, 0xffff0000, v33\n"
      "v_lshlrev_b32 v124, 16, v34\n"
      "v_and_b32 v125, 0xffff0000, v34\n"
      "v_lshlrev_b32 v126, 16, v35\n"
      "v_and_b32 v127, 0xffff0000, v35\n"
      "v_mul_f32 v32, 0xbfb8aa3b, v120\n"
      "v_mul_f32 v33, 0xbfb8aa3b, v121\n"
      "v_mul_f32 v34, 0xbfb8aa3b, v122\n"
      "v_mul_f32 v35, 0xbfb8aa3b, v123\n"
      "v_mul_f32 v36, 0xbfb8aa3b, v124\n"
      "v_mul_f32 v37, 0xbfb8aa3b, v125\n"
      "v_mul_f32 v38, 0xbfb8aa3b, v126\n"
      "v_mul_f32 v39, 0xbfb8aa3b, v127\n"
      "v_exp_f32 v32, v32\n"
      "v_exp_f32 v33, v33\n"
      "v_exp_f32 v34, v34\n"
      "v_exp_f32 v35, v35\n"
      "v_exp_f32 v36, v36\n"
      "v_exp_f32 v37, v37\n"
      "v_exp_f32 v38, v38\n"
      "v_exp_f32 v39, v39\n"
      "v_add_f32 v32, 1.0, v32\n"
      "v_add_f32 v33, 1.0, v33\n"
      "v_add_f32 v34, 1.0, v34\n"
      "v_add_f32 v35, 1.0, v35\n"
      "v_add_f32 v36, 1.0, v36\n"
      "v_add_f32 v37, 1.0, v37\n"
      "v_add_f32 v38, 1.0, v38\n"
      "v_add_f32 v39, 1.0, v39\n"
      "v_rcp_f32 v32, v32\n"
      "v_rcp_f32 v33, v33\n"
      "v_rcp_f32 v34, v34\n"
      "v_rcp_f32 v35, v35\n"
      "v_rcp_f32 v36, v36\n"
      "v_rcp_f32 v37, v37\n"
      "v_rcp_f32 v38, v38\n"
      "v_rcp_f32 v39, v39\n"
      "v_mul_f32 v120, v120, v32\n"
      "v_mul_f32 v121, v121, v33\n"
      "v_mul_f32 v122, v122, v34\n"
      "v_mul_f32 v123, v123, v35\n"
      "v_mul_f32 v124, v124, v36\n"
      "v_mul_f32 v125, v125, v37\n"
      "v_mul_f32 v126, v126, v38\n"
      "v_mul_f32 v127, v127, v39\n"
      "s_nop 2\n"
      ".Lnofix1:\n"
      "s_cmp_eq_u32 s85, 2\n"
      "s_cbranch_scc0 .Lnofix2\n"
      "v_sub_f32 v128, v96, v28\n"
      "v_sub_f32 v129, v97, v28\n"
      "v_sub_f32 v130, v98, v28\n"
      "v_sub_f32 v131, v99, v28\n"
      "v_sub_f32 v132, v100, v28\n"
      "v_sub_f32 v133, v101, v28\n"
      "v_sub_f32 v134, v102, v28\n"
      "v_sub_f32 v135, v103, v28\n"
      "v_cvt_pk_bf16_f32 v32, v128, v129\n"
      "v_cvt_pk_bf16_f32 v33, v130, v131\n"
      "v_cvt_pk_bf16_f32 v34, v132, v133\n"
      "v_cvt_pk_bf16_f32 v35, v134, v135\n"
      "v_lshlrev_b32 v128, 16, v32\n"
      "v_and_b32 v129, 0xffff0000, v32\n"
      "v_lshlrev_b32 v130, 16, v33\n"
      "v_and_b32 v131, 0xffff0000, v33\n"
      "v_lshlrev_b32 v132, 16, v34\n"
      "v_and_b32 v133, 0xffff0000, v34\n"
      "v_lshlrev_b32 v134, 16, v35\n"
      "v_and_b32 v135, 0xffff0000, v35\n"
      "v_mul_f32 v128, v128, v29\n"
      "v_mul_f32 v129, v129, v29\n"
      "v_mul_f32 v130, v130, v29\n"
      "v_mul_f32 v131, v131, v29\n"
      "v_mul_f32 v132, v132, v29\n"
      "v_mul_f32 v133, v133, v29\n"
      "v_mul_f32 v134, v134, v29\n"
      "v_mul_f32 v135, v135, v29\n"
      "v_cvt_pk_bf16_f32 v32, v128, v129\n"
      "v_cvt_pk_bf16_f32 v33, v130, v131\n"
      "v_cvt_pk_bf16_f32 v34, v132, v133\n"
      "v_cvt_pk_bf16_f32 v35, v134, v135\n"
      "v_lshlrev_b32 v128, 16, v32\n"
      "v_and_b32 v129, 0xffff0000, v32\n"
      "v_lshlrev_b32 v130, 16, v33\n"
      "v_and_b32 v131, 0xffff0000, v33\n"
      "v_lshlrev_b32 v132, 16, v34\n"
      "v_and_b32 v133, 0xffff0000, v34\n"
      "v_lshlrev_b32 v134, 16, v35\n"
      "v_and_b32 v135, 0xffff0000, v35\n"
      "v_add_f32 v128, v128, v40\n"
      "v_add_f32 v129, v129, v41\n"
      "v_add_f32 v130, v130, v42\n"
      "v_add_f32 v131, v131, v43\n"
      "v_add_f32 v132, v132, v44\n"
      "v_add_f32 v133, v133, v45\n"
      "v_add_f32 v134, v134, v46\n"
      "v_add_f32 v135, v135, v47\n"
      "v_cvt_pk_bf16_f32 v32, v128, v129\n"
      "v_cvt_pk_bf16_f32 v33, v130, v131\n"
      "v_cvt_pk_bf16_f32 v34, v132, v133\n"
      "v_cvt_pk_bf16_f32 v35, v134, v135\n"
      "v_lshlrev_b32 v128, 16, v32\n"
      "v_and_b32 v129, 0xffff0000, v32\n"
      "v_lshlrev_b32 v130, 16, v33\n"
      "v_and_b32 v131, 0xffff0000, v33\n"
      "v_lshlrev_b32 v132, 16, v34\n"
      "v_and_b32 v133, 0xffff0000, v34\n"
      "v_lshlrev_b32 v134, 16, v35\n"
      "v_and_b32 v135, 0xffff0000, v35\n"
      "v_mul_f32 v32, 0xbfb8aa3b, v128\n"
      "v_mul_f32 v33, 0xbfb8aa3b, v129\n"
      "v_mul_f32 v34, 0xbfb8aa3b, v130\n"
      "v_mul_f32 v35, 0xbfb8aa3b, v131\n"
      "v_mul_f32 v36, 0xbfb8aa3b, v132\n"
      "v_mul_f32 v37, 0xbfb8aa3b, v133\n"
      "v_mul_f32 v38, 0xbfb8aa3b, v134\n"
      "v_mul_f32 v39, 0xbfb8aa3b, v135\n"
      "v_exp_f32 v32, v32\n"
      "v_exp_f32 v33, v33\n"
      "v_exp_f32 v34, v34\n"
      "v_exp_f32 v35, v35\n"
      "v_exp_f32 v36, v36\n"
      "v_exp_f32 v37, v37\n"
      "v_exp_f32 v38, v38\n"
      "v_exp_f32 v39, v39\n"
      "v_add_f32 v32, 1.0, v32\n"
      "v_add_f32 v33, 1.0, v33\n"
      "v_add_f32 v34, 1.0, v34\n"
      "v_add_f32 v35, 1.0, v35\n"
      "v_add_f32 v36, 1.0, v36\n"
      "v_add_f32 v37, 1.0, v37\n"
      "v_add_f32 v38, 1.0, v38\n"
      "v_add_f32 v39, 1.0, v39\n"
      "v_rcp_f32 v32, v32\n"
      "v_rcp_f32 v33, v33\n"
      "v_rcp_f32 v34, v34\n"
      "v_rcp_f32 v35, v35\n"
      "v_rcp_f32 v36, v36\n"
      "v_rcp_f32 v37, v37\n"
      "v_rcp_f32 v38, v38\n"
      "v_rcp_f32 v39, v39\n"
      "v_mul_f32 v128, v128, v32\n"
      "v_mul_f32 v129, v129, v33\n"
      "v_mul_f32 v130, v130, v34\n"
      "v_mul_f32 v131, v131, v35\n"
      "v_mul_f32 v132, v132, v36\n"
      "v_mul_f32 v133, v133, v37\n"
      "v_mul_f32 v134, v134, v38\n"
      "v_mul_f32 v135, v135, v39\n"
      "s_nop 2\n"
      ".Lnofix2:\n"
      "s_cmp_eq_u32 s85, 3\n"
      "s_cbranch_scc0 .Lnofix3\n"
      "v_sub_f32 v136, v104, v30\n"
      "v_sub_f32 v137, v105, v30\n"
      "v_sub_f32 v138, v106, v30\n"
      "v_sub_f32 v139, v107, v30\n"
      "v_sub_f32 v140, v108, v30\n"
      "v_sub_f32 v141, v109, v30\n"
      "v_sub_f32 v142, v110, v30\n"
      "v_sub_f32 v143, v111, v30\n"
      "v_cvt_pk_bf16_f32 v32, v136, v137\n"
      "v_cvt_pk_bf16_f32 v33, v138, v139\n"
      "v_cvt_pk_bf16_f32 v34, v140, v141\n"
      "v_cvt_pk_bf16_f32 v35, v142, v143\n"
      "v_lshlrev_b32 v136, 16, v32\n"
      "v_and_b32 v137, 0xffff0000, v32\n"
      "v_lshlrev_b32 v138, 16, v33\n"
      "v_and_b32 v139, 0xffff0000, v33\n"
      "v_lshlrev_b32 v140, 16, v34\n"
      "v_and_b32 v141, 0xffff0000, v34\n"
      "v_lshlrev_b32 v142, 16, v35\n"
      "v_and_b32 v143, 0xffff0000, v35\n"
      "v_mul_f32 v136, v136, v31\n"
      "v_mul_f32 v137, v137, v31\n"
      "v_mul_f32 v138, v138, v31\n"
      "v_mul_f32 v139, v139, v31\n"
      "v_mul_f32 v140, v140, v31\n"
      "v_mul_f32 v141, v141, v31\n"
      "v_mul_f32 v142, v142, v31\n"
      "v_mul_f32 v143, v143, v31\n"
      "v_cvt_pk_bf16_f32 v32, v136, v137\n"
      "v_cvt_pk_bf16_f32 v33, v138, v139\n"
      "v_cvt_pk_bf16_f32 v34, v140, v141\n"
      "v_cvt_pk_bf16_f32 v35, v142, v143\n"
      "v_lshlrev_b32 v136, 16, v32\n"
      "v_and_b32 v137, 0xffff0000, v32\n"
      "v_lshlrev_b32 v138, 16, v33\n"
      "v_and_b32 v139, 0xffff0000, v33\n"
      "v_lshlrev_b32 v140, 16, v34\n"
      "v_and_b32 v141, 0xffff0000, v34\n"
      "v_lshlrev_b32 v142, 16, v35\n"
      "v_and_b32 v143, 0xffff0000, v35\n"
      "v_add_f32 v136, v136, v40\n"
      "v_add_f32 v137, v137, v41\n"
      "v_add_f32 v138, v138, v42\n"
      "v_add_f32 v139, v139, v43\n"
      "v_add_f32 v140, v140, v44\n"
      "v_add_f32 v141, v141, v45\n"
      "v_add_f32 v142, v142, v46\n"
      "v_add_f32 v143, v143, v47\n"
      "v_cvt_pk_bf16_f32 v32, v136, v137\n"
      "v_cvt_pk_bf16_f32 v33, v138, v139\n"
      "v_cvt_pk_bf16_f32 v34, v140, v141\n"
      "v_cvt_pk_bf16_f32 v35, v142, v143\n"
      "v_lshlrev_b32 v136, 16, v32\n"
      "v_and_b32 v137, 0xffff0000, v32\n"
      "v_lshlrev_b32 v138, 16, v33\n"
      "v_and_b32 v139, 0xffff0000, v33\n"
      "v_lshlrev_b32 v140, 16, v34\n"
      "v_and_b32 v141, 0xffff0000, v34\n"
      "v_lshlrev_b32 v142, 16, v35\n"
      "v_and_b32 v143, 0xffff0000, v35\n"
      "v_mul_f32 v32, 0xbfb8aa3b, v136\n"
      "v_mul_f32 v33, 0xbfb8aa3b, v137\n"
      "v_mul_f32 v34, 0xbfb8aa3b, v138\n"
      "v_mul_f32 v35, 0xbfb8aa3b, v139\n"
      "v_mul_f32 v36, 0xbfb8aa3b, v140\n"
      "v_mul_f32 v37, 0xbfb8aa3b, v141\n"
      "v_mul_f32 v38, 0xbfb8aa3b, v142\n"
      "v_mul_f32 v39, 0xbfb8aa3b, v143\n"
      "v_exp_f32 v32, v32\n"
      "v_exp_f32 v33, v33\n"
      "v_exp_f32 v34, v34\n"
      "v_exp_f32 v35, v35\n"
      "v_exp_f32 v36, v36\n"
      "v_exp_f32 v37, v37\n"
      "v_exp_f32 v38, v38\n"
      "v_exp_f32 v39, v39\n"
      "v_add_f32 v32, 1.0, v32\n"
      "v_add_f32 v33, 1.0, v33\n"
      "v_add_f32 v34, 1.0, v34\n"
      "v_add_f32 v35, 1.0, v35\n"
      "v_add_f32 v36, 1.0, v36\n"
      "v_add_f32 v37, 1.0, v37\n"
      "v_add_f32 v38, 1.0, v38\n"
      "v_add_f32 v39, 1.0, v39\n"
      "v_rcp_f32 v32, v32\n"
      "v_rcp_f32 v33, v33\n"
      "v_rcp_f32 v34, v34\n"
      "v_rcp_f32 v35, v35\n"
      "v_rcp_f32 v36, v36\n"
      "v_rcp_f32 v37, v37\n"
      "v_rcp_f32 v38, v38\n"
      "v_rcp_f32 v39, v39\n"
      "v_mul_f32 v136, v136, v32\n"
      "v_mul_f32 v137, v137, v33\n"
      "v_mul_f32 v138, v138, v34\n"
      "v_mul_f32 v139, v139, v35\n"
      "v_mul_f32 v140, v140, v36\n"
      "v_mul_f32 v141, v141, v37\n"
      "v_mul_f32 v142, v142, v38\n"
      "v_mul_f32 v143, v143, v39\n"
      "s_nop 2\n"
      ".Lnofix3:\n"
      ".Lnoy:\n"
      "ds_read_b128 v[48:51], v10 offset:0\n"
      "ds_read_b128 v[52:55], v11 offset:0\n"
      "ds_read_b128 v[56:59], v10 offset:2048\n"
      "ds_read_b128 v[60:63], v11 offset:2048\n"
      "ds_read_b128 v[64:67], v10 offset:4096\n"
      "ds_read_b128 v[68:71], v11 offset:4096\n"
      "ds_read_b128 v[72:75], v10 offset:6144\n"
      "ds_read_b128 v[76:79], v11 offset:6144\n"
      "v_add_u32 v10, 8192, v10\n"
      "v_add_u32 v11, 8192, v11\n"
      "s_waitcnt lgkmcnt(0)\n"
      "v_pk_fma_f32 v[80:81], v[48:49], v[112:113], v[80:81]\n"
      "v_pk_fma_f32 v[82:83], v[50:51], v[114:115], v[82:83]\n"
      "v_pk_fma_f32 v[84:85], v[52:53], v[116:117], v[84:85]\n"
      "v_pk_fma_f32 v[86:87], v[54:55], v[118:119], v[86:87]\n"
      "v_pk_fma_f32 v[88:89], v[56:57], v[120:121], v[88:89]\n"
      "v_pk_fma_f32 v[90:91], v[58:59], v[122:123], v[90:91]\n"
      "v_pk_fma_f32 v[92:93], v[60:61], v[124:125], v[92:93]\n"
      "v_pk_fma_f32 v[94:95], v[62:63], v[126:127], v[94:95]\n"
      "v_pk_fma_f32 v[96:97], v[64:65], v[128:129], v[96:97]\n"
      "v_pk_fma_f32 v[98:99], v[66:67], v[130:131], v[98:99]\n"
      "v_pk_fma_f32 v[100:101], v[68:69], v[132:133], v[100:101]\n"
      "v_pk_fma_f32 v[102:103], v[70:71], v[134:135], v[102:103]\n"
      "v_pk_fma_f32 v[104:105], v[72:73], v[136:137], v[104:105]\n"
      "v_pk_fma_f32 v[106:107], v[74:75], v[138:139], v[106:107]\n"
      "v_pk_fma_f32 v[108:109], v[76:77], v[140:141], v[108:109]\n"
      "v_pk_fma_f32 v[110:111], v[78:79], v[142:143], v[110:111]\n"
      "s_barrier\n"
      "ds_read_b128 v[48:51], v10 offset:0\n"
      "ds_read_b128 v[52:55], v11 offset:0\n"
      "ds_read_b128 v[56:59], v10 offset:2048\n"
      "ds_read_b128 v[60:63], v11 offset:2048\n"
      "ds_read_b128 v[64:67], v10 offset:4096\n"
      "ds_read_b128 v[68:71], v11 offset:4096\n"
      "ds_read_b128 v[72:75], v10 offset:6144\n"
      "ds_read_b128 v[76:79], v11 offset:6144\n"
      "s_waitcnt lgkmcnt(6)\n"
      "v_pk_fma_f32 v[48:49], v[48:49], v[112:113], v[80:81]\n"
      "v_pk_fma_f32 v[50:51], v[50:51], v[114:115], v[82:83]\n"
      "v_pk_fma_f32 v[52:53], v[52:53], v[116:117], v[84:85]\n"
      "v_pk_fma_f32 v[54:55], v[54:55], v[118:119], v[86:87]\n"
      "v_cvt_pk_bf16_f32 v16, v48, v49\n"
      "v_cvt_pk_bf16_f32 v17, v50, v51\n"
      "v_cvt_pk_bf16_f32 v18, v52, v53\n"
      "v_cvt_pk_bf16_f32 v19, v54, v55\n"
      "s_add_u32 s80, s94, 0\n"
      "buffer_store_dwordx4 v[16:19], v153, s[72:75], s80 offen\n"
      "s_waitcnt lgkmcnt(4)\n"
      "v_pk_fma_f32 v[56:57], v[56:57], v[120:121], v[88:89]\n"
      "v_pk_fma_f32 v[58:59], v[58:59], v[122:123], v[90:91]\n"
      "v_pk_fma_f32 v[60:61], v[60:61], v[124:125], v[92:93]\n"
      "v_pk_fma_f32 v[62:63], v[62:63], v[126:127], v[94:95]\n"
      "v_cvt_pk_bf16_f32 v20, v56, v57\n"
      "v_cvt_pk_bf16_f32 v21, v58, v59\n"
      "v_cvt_pk_bf16_f32 v22, v60, v61\n"
      "v_cvt_pk_bf16_f32 v23, v62, v63\n"
      "s_add_u32 s80, s94, 32768\n"
      "buffer_store_dwordx4 v[20:23], v153, s[72:75], s80 offen\n"
      "s_waitcnt lgkmcnt(2)\n"
      "v_pk_fma_f32 v[64:65], v[64:65], v[128:129], v[96:97]\n"
      "v_pk_fma_f32 v[66:67], v[66:67], v[130:131], v[98:99]\n"
      "v_pk_fma_f32 v[68:69], v[68:69], v[132:133], v[100:101]\n"
      "v_pk_fma_f32 v[70:71], v[70:71], v[134:135], v[102:103]\n"
      "v_cvt_pk_bf16_f32 v24, v64, v65\n"
      "v_cvt_pk_bf16_f32 v25, v66, v67\n"
      "v_cvt_pk_bf16_f32 v26, v68, v69\n"
      "v_cvt_pk_bf16_f32 v27, v70, v71\n"
      "s_add_u32 s80, s94, 65536\n"
      "buffer_store_dwordx4 v[24:27], v153, s[72:75], s80 offen\n"
      "s_waitcnt lgkmcnt(0)\n"
      "v_pk_fma_f32 v[72:73], v[72:73], v[136:137], v[104:105]\n"
      "v_pk_fma_f32 v[74:75], v[74:75], v[138:139], v[106:107]\n"
      "v_pk_fma_f32 v[76:77], v[76:77], v[140:141], v[108:109]\n"
      "v_pk_fma_f32 v[78:79], v[78:79], v[142:143], v[110:111]\n"
      "v_cvt_pk_bf16_f32 v28, v72, v73\n"
      "v_cvt_pk_bf16_f32 v29, v74, v75\n"
      "v_cvt_pk_bf16_f32 v30, v76, v77\n"
      "v_cvt_pk_bf16_f32 v31, v78, v79\n"
      "s_add_u32 s80, s94, 98304\n"
      "buffer_store_dwordx4 v[28:31], v153, s[72:75], s80 offen\n"
      "s_endpgm\n"
      "s_nop 0\n"
      ".Lfallback:\n"
      "s_waitcnt vmcnt(0) lgkmcnt(0)\n"
      "v_mov_b32 v156, 0xff800000\n"
      "v_mov_b32 v158, 0xff800000\n"
      "s_mov_b32 s80, 65536\n"
      "s_nop 1\n"
      ".Lfb1:\n"
      "buffer_load_dwordx4 a[24:27], v145, s[48:51], s80 offen offset:0 sc1\n"
      "buffer_load_dwordx4 a[28:31], v145, s[48:51], s80 offen offset:1024 sc1\n"
      "s_waitcnt vmcnt(0)\n"
      "v_mfma_f32_32x32x16_bf16 v[16:31], a[24:27], a[8:11], 0\n"
      "v_mfma_f32_32x32x16_bf16 v[16:31], a[28:31], a[12:15], v[16:31]\n"
      "v_mfma_f32_32x32x16_bf16 v[48:63], a[24:27], a[16:19], 0\n"
      "v_mfma_f32_32x32x16_bf16 v[48:63], a[28:31], a[20:23], v[48:63]\n"
      "s_nop 9\n"
      "v_max3_f32 v156, v156, v16, v17\n"
      "v_max3_f32 v156, v156, v18, v19\n"
      "v_max3_f32 v156, v156, v20, v21\n"
      "v_max3_f32 v156, v156, v22, v23\n"
      "v_max3_f32 v156, v156, v24, v25\n"
      "v_max3_f32 v156, v156, v26, v27\n"
      "v_max3_f32 v156, v156, v28, v29\n"
      "v_max3_f32 v156, v156, v30, v31\n"
      "v_max3_f32 v158, v158, v48, v49\n"
      "v_max3_f32 v158, v158, v50, v51\n"
      "v_max3_f32 v158, v158, v52, v53\n"
      "v_max3_f32 v158, v158, v54, v55\n"
      "v_max3_f32 v158, v158, v56, v57\n"
      "v_max3_f32 v158, v158, v58, v59\n"
      "v_max3_f32 v158, v158, v60, v61\n"
      "v_max3_f32 v158, v158, v62, v63\n"
      "s_add_u32 s80, s80, 4096\n"
      "s_cmp_lt_u32 s80, 327680\n"
      "s_nop 0\n"
      "s_cbranch_scc1 .Lfb1\n"
      "v_mov_b32 v157, v156\n"
      "v_mov_b32 v159, v158\n"
      "s_nop 0\n"
      "v_permlane32_swap_b32 v156, v157\n"
      "v_permlane32_swap_b32 v158, v159\n"
      "v_max_f32 v156, v156, v157\n"
      "v_max_f32 v158, v158, v159\n"
      "v_sub_f32 v80, 0, v156\n"
      "v_mov_b32 v112, 0\n"
      "v_sub_f32 v81, 0, v156\n"
      "v_mov_b32 v113, 0\n"
      "v_sub_f32 v82, 0, v156\n"
      "v_mov_b32 v114, 0\n"
      "v_sub_f32 v83, 0, v156\n"
      "v_mov_b32 v115, 0\n"
      "v_sub_f32 v84, 0, v156\n"
      "v_mov_b32 v116, 0\n"
      "v_sub_f32 v85, 0, v156\n"
      "v_mov_b32 v117, 0\n"
      "v_sub_f32 v86, 0, v156\n"
      "v_mov_b32 v118, 0\n"
      "v_sub_f32 v87, 0, v156\n"
      "v_mov_b32 v119, 0\n"
      "v_sub_f32 v88, 0, v156\n"
      "v_mov_b32 v120, 0\n"
      "v_sub_f32 v89, 0, v156\n"
      "v_mov_b32 v121, 0\n"
      "v_sub_f32 v90, 0, v156\n"
      "v_mov_b32 v122, 0\n"
      "v_sub_f32 v91, 0, v156\n"
      "v_mov_b32 v123, 0\n"
      "v_sub_f32 v92, 0, v156\n"
      "v_mov_b32 v124, 0\n"
      "v_sub_f32 v93, 0, v156\n"
      "v_mov_b32 v125, 0\n"
      "v_sub_f32 v94, 0, v156\n"
      "v_mov_b32 v126, 0\n"
      "v_sub_f32 v95, 0, v156\n"
      "v_mov_b32 v127, 0\n"
      "v_sub_f32 v96, 0, v158\n"
      "v_mov_b32 v128, 0\n"
      "v_sub_f32 v97, 0, v158\n"
      "v_mov_b32 v129, 0\n"
      "v_sub_f32 v98, 0, v158\n"
      "v_mov_b32 v130, 0\n"
      "v_sub_f32 v99, 0, v158\n"
      "v_mov_b32 v131, 0\n"
      "v_sub_f32 v100, 0, v158\n"
      "v_mov_b32 v132, 0\n"
      "v_sub_f32 v101, 0, v158\n"
      "v_mov_b32 v133, 0\n"
      "v_sub_f32 v102, 0, v158\n"
      "v_mov_b32 v134, 0\n"
      "v_sub_f32 v103, 0, v158\n"
      "v_mov_b32 v135, 0\n"
      "v_sub_f32 v104, 0, v158\n"
      "v_mov_b32 v136, 0\n"
      "v_sub_f32 v105, 0, v158\n"
      "v_mov_b32 v137, 0\n"
      "v_sub_f32 v106, 0, v158\n"
      "v_mov_b32 v138, 0\n"
      "v_sub_f32 v107, 0, v158\n"
      "v_mov_b32 v139, 0\n"
      "v_sub_f32 v108, 0, v158\n"
      "v_mov_b32 v140, 0\n"
      "v_sub_f32 v109, 0, v158\n"
      "v_mov_b32 v141, 0\n"
      "v_sub_f32 v110, 0, v158\n"
      "v_mov_b32 v142, 0\n"
      "v_sub_f32 v111, 0, v158\n"
      "v_mov_b32 v143, 0\n"
      "v_accvgpr_write_b32 a0, 0\n"
      "v_accvgpr_write_b32 a1, 0\n"
      "v_accvgpr_write_b32 a2, 0\n"
      "v_accvgpr_write_b32 a3, 0\n"
      "v_accvgpr_write_b32 a4, 0\n"
      "v_accvgpr_write_b32 a5, 0\n"
      "v_accvgpr_write_b32 a6, 0\n"
      "v_accvgpr_write_b32 a7, 0\n"
      "s_mov_b32 s80, 65536\n"
      "s_nop 1\n"
      ".Lfb2:\n"
      "buffer_load_dwordx4 a[24:27], v145, s[48:51], s80 offen offset:0 sc1\n"
      "buffer_load_dwordx4 a[28:31], v145, s[48:51], s80 offen offset:1024 sc1\n"
      "buffer_load_dwordx4 a[32:35], v145, s[48:51], s80 offen offset:2048 sc1\n"
      "buffer_load_dwordx4 a[36:39], v145, s[48:51], s80 offen offset:3072 sc1\n"
      "s_waitcnt vmcnt(0)\n"
      "v_mfma_f32_32x32x16_bf16 v[16:31], a[24:27], a[8:11], v[80:95]\n"
      "v_mfma_f32_32x32x16_bf16 v[16:31], a[28:31], a[12:15], v[16:31]\n"
      "v_mfma_f32_32x32x16_bf16 v[48:63], a[24:27], a[16:19], v[96:111]\n"
      "v_mfma_f32_32x32x16_bf16 v[48:63], a[28:31], a[20:23], v[48:63]\n"
      "s_nop 9\n"
      "v_exp_f32 v16, v16\n"
      "v_exp_f32 v17, v17\n"
      "v_exp_f32 v18, v18\n"
      "v_exp_f32 v19, v19\n"
      "v_exp_f32 v20, v20\n"
      "v_exp_f32 v21, v21\n"
      "v_exp_f32 v22, v22\n"
      "v_exp_f32 v23, v23\n"
      "v_exp_f32 v24, v24\n"
      "v_exp_f32 v25, v25\n"
      "v_exp_f32 v26, v26\n"
      "v_exp_f32 v27, v27\n"
      "v_exp_f32 v28, v28\n"
      "v_exp_f32 v29, v29\n"
      "v_exp_f32 v30, v30\n"
      "v_exp_f32 v31, v31\n"
      "v_smfmac_f32_32x32x32_bf16 v[112:127], a[32:35], v[16:23], v144\n"
      "v_smfmac_f32_16x16x64_bf16 a[0:3], a[40:43], v[16:23], v144\n"
      "v_smfmac_f32_32x32x32_bf16 v[112:127], a[36:39], v[24:31], v144\n"
      "v_smfmac_f32_16x16x64_bf16 a[0:3], a[40:43], v[24:31], v144\n"
      "v_exp_f32 v48, v48\n"
      "v_exp_f32 v49, v49\n"
      "v_exp_f32 v50, v50\n"
      "v_exp_f32 v51, v51\n"
      "v_exp_f32 v52, v52\n"
      "v_exp_f32 v53, v53\n"
      "v_exp_f32 v54, v54\n"
      "v_exp_f32 v55, v55\n"
      "v_exp_f32 v56, v56\n"
      "v_exp_f32 v57, v57\n"
      "v_exp_f32 v58, v58\n"
      "v_exp_f32 v59, v59\n"
      "v_exp_f32 v60, v60\n"
      "v_exp_f32 v61, v61\n"
      "v_exp_f32 v62, v62\n"
      "v_exp_f32 v63, v63\n"
      "v_smfmac_f32_32x32x32_bf16 v[128:143], a[32:35], v[48:55], v144\n"
      "v_smfmac_f32_16x16x64_bf16 a[4:7], a[40:43], v[48:55], v144\n"
      "v_smfmac_f32_32x32x32_bf16 v[128:143], a[36:39], v[56:63], v144\n"
      "v_smfmac_f32_16x16x64_bf16 a[4:7], a[40:43], v[56:63], v144\n"
      "s_add_u32 s80, s80, 4096\n"
      "s_cmp_lt_u32 s80, 327680\n"
      "s_nop 8\n"
      "s_cbranch_scc1 .Lfb2\n"
      "s_branch .Lnormalize\n"
      ".Lhelper:\n"
      "s_setprio 3\n"
      "s_add_u32 s35, s31, s79\n"
      "s_and_b32 s35, s35, 7\n"
      "v_lshlrev_b32 v9, 4, v1\n"
      "s_cmp_eq_u32 s34, 0\n"
      "s_nop 1\n"
      "s_cbranch_scc1 .Lexch\n"
      "s_lshl_b32 s83, s33, 16\n"
      "s_lshl_b32 s88, s33, 13\n"
      "s_lshl_b32 s87, s31, 17\n"
      "s_lshl_b32 s86, s79, 8\n"
      "s_lshl_b32 s85, s35, 13\n"
      "v_lshrrev_b32 v10, 4, v1\n"
      "v_and_b32 v11, 15, v1\n"
      "v_lshl_add_u32 v12, v10, 11, s83\n"
      "v_xor_b32 v13, v11, v10\n"
      "v_lshlrev_b32 v13, 4, v13\n"
      "v_add_u32 v24, v12, v13\n"
      "v_xor_b32 v15, 64, v13\n"
      "v_add_u32 v25, 7168, v12\n"
      "v_add_u32 v25, v25, v15\n"
      "v_xor_b32 v15, 128, v13\n"
      "v_add_u32 v26, 14336, v12\n"
      "v_add_u32 v26, v26, v15\n"
      "v_xor_b32 v15, 192, v13\n"
      "v_add_u32 v27, 21504, v12\n"
      "v_add_u32 v27, v27, v15\n"
      "v_lshlrev_b32 v16, 2, v10\n"
      "v_xor_b32 v16, v16, v11\n"
      "v_lshl_add_u32 v28, v16, 4, v12\n"
      "v_add_u32 v29, 7168, v28\n"
      "v_add_u32 v30, 14336, v28\n"
      "v_add_u32 v31, 21504, v28\n"
      "s_waitcnt lgkmcnt(0)\n"
      "s_add_u32 s40, s8, s87\n"
      "s_addc_u32 s41, s9, 0\n"
      "s_and_b32 s41, s41, 0xffff\n"
      "s_mov_b32 s42, 131072\n"
      "s_mov_b32 s43, 0x20000\n"
      "s_add_u32 m0, s88, 0\n"
      "s_add_u32 s89, s35, 0\n"
      "s_and_b32 s89, s89, 7\n"
      "s_lshl_b32 s89, s89, 8\n"
      "buffer_load_dwordx4 v24, s[40:43], s89 offen offset:0 lds\n"
      "buffer_load_dwordx4 v25, s[40:43], s89 offen offset:1024 lds\n"
      "buffer_load_dwordx4 v26, s[40:43], s89 offen offset:2048 lds\n"
      "buffer_load_dwordx4 v27, s[40:43], s89 offen offset:3072 lds\n"
      "s_add_u32 m0, s88, 4096\n"
      "s_add_u32 s89, s89, 32768\n"
      "buffer_load_dwordx4 v24, s[40:43], s89 offen offset:0 lds\n"
      "buffer_load_dwordx4 v25, s[40:43], s89 offen offset:1024 lds\n"
      "buffer_load_dwordx4 v26, s[40:43], s89 offen offset:2048 lds\n"
      "buffer_load_dwordx4 v27, s[40:43], s89 offen offset:3072 lds\n"
      "s_cmp_eq_u32 s33, 0\n"
      "s_cselect_b32 s68, s12, s14\n"
      "s_cselect_b32 s69, s13, s15\n"
      "s_and_b32 s69, s69, 0xffff\n"
      "s_mov_b32 s70, 65536\n"
      "s_add_u32 m0, s88, 16384\n"
      "s_nop 0\n"
      "buffer_load_dwordx4 v9, s[68:71], s85 offen offset:0 lds\n"
      "buffer_load_dwordx4 v9, s[68:71], s85 offen offset:1024 lds\n"
      "buffer_load_dwordx4 v9, s[68:71], s85 offen offset:2048 lds\n"
      "buffer_load_dwordx4 v9, s[68:71], s85 offen offset:3072 lds\n"
      "s_add_u32 m0, s88, 20480\n"
      "s_add_u32 s85, s85, 4096\n"
      "buffer_load_dwordx4 v9, s[68:71], s85 offen offset:0 lds\n"
      "buffer_load_dwordx4 v9, s[68:71], s85 offen offset:1024 lds\n"
      "buffer_load_dwordx4 v9, s[68:71], s85 offen offset:2048 lds\n"
      "buffer_load_dwordx4 v9, s[68:71], s85 offen offset:3072 lds\n"
      "s_load_dword s36, s[28:29], 0\n"
      "s_add_u32 s44, s10, s86\n"
      "s_addc_u32 s45, s11, 0\n"
      "s_and_b32 s45, s45, 0xffff\n"
      "s_mov_b32 s46, 2097152\n"
      "s_mov_b32 s47, 0x20000\n"
      "s_sub_u32 s46, s46, s86\n"
      "s_add_u32 m0, s88, 32768\n"
      "s_add_u32 s89, s35, 0\n"
      "s_and_b32 s89, s89, 7\n"
      "s_lshl_b32 s89, s89, 18\n"
      "buffer_load_dwordx4 v28, s[44:47], s89 offen offset:0 lds\n"
      "buffer_load_dwordx4 v29, s[44:47], s89 offen offset:1024 lds\n"
      "buffer_load_dwordx4 v30, s[44:47], s89 offen offset:2048 lds\n"
      "buffer_load_dwordx4 v31, s[44:47], s89 offen offset:3072 lds\n"
      "s_add_u32 m0, s88, 36864\n"
      "s_add_u32 s89, s89, 32768\n"
      "buffer_load_dwordx4 v28, s[44:47], s89 offen offset:0 lds\n"
      "buffer_load_dwordx4 v29, s[44:47], s89 offen offset:1024 lds\n"
      "buffer_load_dwordx4 v30, s[44:47], s89 offen offset:2048 lds\n"
      "buffer_load_dwordx4 v31, s[44:47], s89 offen offset:3072 lds\n"
      "s_add_u32 m0, s88, 49152\n"
      "s_add_u32 s89, s89, 98304\n"
      "buffer_load_dwordx4 v28, s[44:47], s89 offen offset:0 lds\n"
      "buffer_load_dwordx4 v29, s[44:47], s89 offen offset:1024 lds\n"
      "buffer_load_dwordx4 v30, s[44:47], s89 offen offset:2048 lds\n"
      "buffer_load_dwordx4 v31, s[44:47], s89 offen offset:3072 lds\n"
      "s_add_u32 m0, s88, 53248\n"
      "s_add_u32 s89, s89, 32768\n"
      "buffer_load_dwordx4 v28, s[44:47], s89 offen offset:0 lds\n"
      "buffer_load_dwordx4 v29, s[44:47], s89 offen offset:1024 lds\n"
      "buffer_load_dwordx4 v30, s[44:47], s89 offen offset:2048 lds\n"
      "buffer_load_dwordx4 v31, s[44:47], s89 offen offset:3072 lds\n"
      "s_mov_b32 s48, s28\n"
      "s_and_b32 s49, s29, 0xffff\n"
      "s_mov_b32 s50, 6553600\n"
      "s_mov_b32 s51, 0x20000\n"
      "s_and_b32 s80, s39, 7\n"
      "s_mul_i32 s80, s80, 28\n"
      "s_lshr_b32 s81, s39, 3\n"
      "s_add_u32 s7, s80, s81\n"
      "s_cmp_lt_u32 s81, 28\n"
      "s_cselect_b32 s82, 7340032, 0\n"
      "s_cselect_b32 s7, s7, 0x200000\n"
      "s_mov_b32 s56, s20\n"
      "s_and_b32 s57, s21, 0xffff\n"
      "s_mov_b32 s58, s82\n"
      "s_mov_b32 s59, 0x20000\n"
      "s_mov_b32 s60, s22\n"
      "s_and_b32 s61, s23, 0xffff\n"
      "s_mov_b32 s62, s82\n"
      "s_mov_b32 s63, 0x20000\n"
      "s_mov_b32 s64, s24\n"
      "s_and_b32 s65, s25, 0xffff\n"
      "s_mov_b32 s66, s82\n"
      "s_mov_b32 s67, 0x20000\n"
      "s_and_b32 s83, s7, 0xff\n"
      "s_and_b32 s84, s83, 3\n"
      "s_lshl_b32 s84, s84, 2\n"
      "s_add_u32 s84, s84, s30\n"
      "s_mul_i32 s95, s84, 458752\n"
      "s_lshl_b32 s96, s84, 7\n"
      "s_lshr_b32 s84, s83, 2\n"
      "s_lshl_b32 s83, s84, 7\n"
      "s_add_u32 s95, s95, s83\n"
      "s_lshl_b32 s84, s84, 17\n"
      "s_add_u32 s96, s96, s84\n"
      "s_lshl_b32 s94, s79, 8\n"
      "s_lshl_b32 s81, s30, 6\n"
      "s_add_u32 s94, s94, s81\n"
      "s_add_u32 s52, s16, s94\n"
      "s_addc_u32 s53, s17, 0\n"
      "s_and_b32 s53, s53, 0xffff\n"
      "s_mov_b32 s54, 2097152\n"
      "s_mov_b32 s55, 0x20000\n"
      "s_sub_u32 s54, s54, s94\n"
      "s_mul_i32 s5, s30, 7168\n"
      "s_add_u32 s5, s5, 131072\n"
      "s_lshl_b32 s6, s30, 14\n"
      "s_add_u32 s6, s6, 65536\n"
      "s_lshl_b32 s4, s30, 12\n"
      "v_add_u32 v2, s4, v9\n"
      "v_and_b32 v10, 31, v1\n"
      "v_lshrrev_b32 v11, 5, v1\n"
      "v_lshrrev_b32 v12, 2, v1\n"
      "v_lshlrev_b32 v12, 11, v12\n"
      "v_and_b32 v13, 3, v1\n"
      "v_lshl_add_u32 v3, v13, 4, v12\n"
      "v_bfe_u32 v15, v1, 4, 1\n"
      "v_lshlrev_b32 v15, 5, v15\n"
      "v_lshl_add_u32 v15, v13, 3, v15\n"
      "v_bfe_u32 v12, v1, 2, 2\n"
      "v_lshl_add_u32 v14, v11, 2, v12\n"
      "v_lshl_add_u32 v14, v14, 6, v15\n"
      "v_add_u32 v4, s5, v14\n"
      "v_lshl_add_u32 v14, v11, 3, v12\n"
      "v_lshl_add_u32 v14, v14, 7, v15\n"
      "v_add_u32 v7, s6, v14\n"
      "v_lshrrev_b32 v14, 4, v1\n"
      "v_lshl_add_u32 v14, v14, 3, v12\n"
      "v_lshlrev_b32 v13, 3, v13\n"
      "v_lshl_add_u32 v14, v14, 7, v13\n"
      "v_add_u32 v6, s6, v14\n"
      "v_lshrrev_b32 v12, 3, v1\n"
      "v_mul_u32_u24 v13, 7168, v12\n"
      "v_and_b32 v14, 7, v1\n"
      "v_lshl_add_u32 v5, v14, 4, v13\n"
      "v_lshlrev_b32 v13, 11, v12\n"
      "v_lshl_add_u32 v8, v14, 4, v13\n"
      "s_add_u32 m0, s88, 65536\n"
      "s_add_u32 s89, s35, 1\n"
      "s_and_b32 s89, s89, 7\n"
      "s_lshl_b32 s89, s89, 8\n"
      "buffer_load_dwordx4 v24, s[40:43], s89 offen offset:0 lds\n"
      "buffer_load_dwordx4 v25, s[40:43], s89 offen offset:1024 lds\n"
      "buffer_load_dwordx4 v26, s[40:43], s89 offen offset:2048 lds\n"
      "buffer_load_dwordx4 v27, s[40:43], s89 offen offset:3072 lds\n"
      "s_add_u32 m0, s88, 69632\n"
      "s_add_u32 s89, s89, 32768\n"
      "buffer_load_dwordx4 v24, s[40:43], s89 offen offset:0 lds\n"
      "buffer_load_dwordx4 v25, s[40:43], s89 offen offset:1024 lds\n"
      "buffer_load_dwordx4 v26, s[40:43], s89 offen offset:2048 lds\n"
      "buffer_load_dwordx4 v27, s[40:43], s89 offen offset:3072 lds\n"
      "s_waitcnt vmcnt(24) lgkmcnt(0)\n"
      "s_add_u32 s36, s36, 1\n"
      "s_barrier\n"
      "s_add_u32 m0, s88, 81920\n"
      "s_add_u32 s89, s35, 1\n"
      "s_and_b32 s89, s89, 7\n"
      "s_lshl_b32 s89, s89, 18\n"
      "buffer_load_dwordx4 v28, s[44:47], s89 offen offset:0 lds\n"
      "buffer_load_dwordx4 v29, s[44:47], s89 offen offset:1024 lds\n"
      "buffer_load_dwordx4 v30, s[44:47], s89 offen offset:2048 lds\n"
      "buffer_load_dwordx4 v31, s[44:47], s89 offen offset:3072 lds\n"
      "s_add_u32 m0, s88, 86016\n"
      "s_add_u32 s89, s89, 32768\n"
      "buffer_load_dwordx4 v28, s[44:47], s89 offen offset:0 lds\n"
      "buffer_load_dwordx4 v29, s[44:47], s89 offen offset:1024 lds\n"
      "buffer_load_dwordx4 v30, s[44:47], s89 offen offset:2048 lds\n"
      "buffer_load_dwordx4 v31, s[44:47], s89 offen offset:3072 lds\n"
      "s_add_u32 m0, s88, 98304\n"
      "s_add_u32 s89, s89, 98304\n"
      "buffer_load_dwordx4 v28, s[44:47], s89 offen offset:0 lds\n"
      "buffer_load_dwordx4 v29, s[44:47], s89 offen offset:1024 lds\n"
      "buffer_load_dwordx4 v30, s[44:47], s89 offen offset:2048 lds\n"
      "buffer_load_dwordx4 v31, s[44:47], s89 offen offset:3072 lds\n"
      "s_add_u32 m0, s88, 102400\n"
      "s_add_u32 s89, s89, 32768\n"
      "buffer_load_dwordx4 v28, s[44:47], s89 offen offset:0 lds\n"
      "buffer_load_dwordx4 v29, s[44:47], s89 offen offset:1024 lds\n"
      "buffer_load_dwordx4 v30, s[44:47], s89 offen offset:2048 lds\n"
      "buffer_load_dwordx4 v31, s[44:47], s89 offen offset:3072 lds\n"
      "s_waitcnt vmcnt(24)\n"
      "s_barrier\n"
      "s_add_u32 m0, s88, 114688\n"
      "s_add_u32 s89, s35, 2\n"
      "s_and_b32 s89, s89, 7\n"
      "s_lshl_b32 s89, s89, 8\n"
      "buffer_load_dwordx4 v24, s[40:43], s89 offen offset:0 lds\n"
      "buffer_load_dwordx4 v25, s[40:43], s89 offen offset:1024 lds\n"
      "buffer_load_dwordx4 v26, s[40:43], s89 offen offset:2048 lds\n"
      "buffer_load_dwordx4 v27, s[40:43], s89 offen offset:3072 lds\n"
      "s_add_u32 m0, s88, 118784\n"
      "s_add_u32 s89, s89, 32768\n"
      "buffer_load_dwordx4 v24, s[40:43], s89 offen offset:0 lds\n"
      "buffer_load_dwordx4 v25, s[40:43], s89 offen offset:1024 lds\n"
      "buffer_load_dwordx4 v26, s[40:43], s89 offen offset:2048 lds\n"
      "buffer_load_dwordx4 v27, s[40:43], s89 offen offset:3072 lds\n"
      "s_add_u32 m0, s88, 131072\n"
      "s_add_u32 s89, s35, 2\n"
      "s_and_b32 s89, s89, 7\n"
      "s_lshl_b32 s89, s89, 18\n"
      "buffer_load_dwordx4 v28, s[44:47], s89 offen offset:0 lds\n"
      "buffer_load_dwordx4 v29, s[44:47], s89 offen offset:1024 lds\n"
      "buffer_load_dwordx4 v30, s[44:47], s89 offen offset:2048 lds\n"
      "buffer_load_dwordx4 v31, s[44:47], s89 offen offset:3072 lds\n"
      "s_add_u32 m0, s88, 135168\n"
      "s_add_u32 s89, s89, 32768\n"
      "buffer_load_dwordx4 v28, s[44:47], s89 offen offset:0 lds\n"
      "buffer_load_dwordx4 v29, s[44:47], s89 offen offset:1024 lds\n"
      "buffer_load_dwordx4 v30, s[44:47], s89 offen offset:2048 lds\n"
      "buffer_load_dwordx4 v31, s[44:47], s89 offen offset:3072 lds\n"
      "v_mov_b32 v16, 148288\n"
      "s_lshl_b32 s80, s33, 2\n"
      "v_add_u32 v17, s80, v16\n"
      "s_waitcnt vmcnt(16)\n"
      "v_mov_b32 v18, 1\n"
      "ds_write_b32 v17, v18 offset:16\n"
      "ds_read_b128 v[20:23], v16 offset:0\n"
      "s_waitcnt lgkmcnt(0)\n"
      "v_min_u32 v18, v20, v21\n"
      "v_min3_u32 v18, v18, v22, v23\n"
      "v_cmp_gt_u32 vcc, 1, v18\n"
      "s_nop 0\n"
      "s_cbranch_vccz .Lss1_ok\n"
      "s_nop 2\n"
      "s_mov_b32 s90, 0x100000\n"
      ".Lss1:\n"
      "s_sleep 2\n"
      "ds_read_b128 v[20:23], v16 offset:0\n"
      "s_waitcnt lgkmcnt(0)\n"
      "v_min_u32 v18, v20, v21\n"
      "v_min3_u32 v18, v18, v22, v23\n"
      "v_cmp_gt_u32 vcc, 1, v18\n"
      "s_sub_u32 s90, s90, 1\n"
      "s_nop 3\n"
      "s_cbranch_vccz .Lss1_ok\n"
      "s_cbranch_scc0 .Lss1\n"
      ".Lss1_ok:\n"
      "s_add_u32 m0, s88, 0\n"
      "s_add_u32 s89, s89, 98304\n"
      "buffer_load_dwordx4 v28, s[44:47], s89 offen offset:0 lds\n"
      "buffer_load_dwordx4 v29, s[44:47], s89 offen offset:1024 lds\n"
      "buffer_load_dwordx4 v30, s[44:47], s89 offen offset:2048 lds\n"
      "buffer_load_dwordx4 v31, s[44:47], s89 offen offset:3072 lds\n"
      "s_add_u32 m0, s88, 4096\n"
      "s_add_u32 s89, s89, 32768\n"
      "buffer_load_dwordx4 v28, s[44:47], s89 offen offset:0 lds\n"
      "buffer_load_dwordx4 v29, s[44:47], s89 offen offset:1024 lds\n"
      "buffer_load_dwordx4 v30, s[44:47], s89 offen offset:2048 lds\n"
      "buffer_load_dwordx4 v31, s[44:47], s89 offen offset:3072 lds\n"
      "s_add_u32 m0, s88, 16384\n"
      "s_add_u32 s89, s35, 3\n"
      "s_and_b32 s89, s89, 7\n"
      "s_lshl_b32 s89, s89, 8\n"
      "buffer_load_dwordx4 v24, s[40:43], s89 offen offset:0 lds\n"
      "buffer_load_dwordx4 v25, s[40:43], s89 offen offset:1024 lds\n"
      "buffer_load_dwordx4 v26, s[40:43], s89 offen offset:2048 lds\n"
      "buffer_load_dwordx4 v27, s[40:43], s89 offen offset:3072 lds\n"
      "s_add_u32 m0, s88, 20480\n"
      "s_add_u32 s89, s89, 32768\n"
      "buffer_load_dwordx4 v24, s[40:43], s89 offen offset:0 lds\n"
      "buffer_load_dwordx4 v25, s[40:43], s89 offen offset:1024 lds\n"
      "buffer_load_dwordx4 v26, s[40:43], s89 offen offset:2048 lds\n"
      "buffer_load_dwordx4 v27, s[40:43], s89 offen offset:3072 lds\n"
      "s_add_u32 m0, s88, 32768\n"
      "s_add_u32 s89, s35, 3\n"
      "s_and_b32 s89, s89, 7\n"
      "s_lshl_b32 s89, s89, 18\n"
      "buffer_load_dwordx4 v28, s[44:47], s89 offen offset:0 lds\n"
      "buffer_load_dwordx4 v29, s[44:47], s89 offen offset:1024 lds\n"
      "buffer_load_dwordx4 v30, s[44:47], s89 offen offset:2048 lds\n"
      "buffer_load_dwordx4 v31, s[44:47], s89 offen offset:3072 lds\n"
      "s_add_u32 m0, s88, 36864\n"
      "s_add_u32 s89, s89, 32768\n"
      "buffer_load_dwordx4 v28, s[44:47], s89 offen offset:0 lds\n"
      "buffer_load_dwordx4 v29, s[44:47], s89 offen offset:1024 lds\n"
      "buffer_load_dwordx4 v30, s[44:47], s89 offen offset:2048 lds\n"
      "buffer_load_dwordx4 v31, s[44:47], s89 offen offset:3072 lds\n"
      "s_add_u32 m0, s88, 49152\n"
      "s_add_u32 s89, s89, 98304\n"
      "buffer_load_dwordx4 v28, s[44:47], s89 offen offset:0 lds\n"
      "buffer_load_dwordx4 v29, s[44:47], s89 offen offset:1024 lds\n"
      "buffer_load_dwordx4 v30, s[44:47], s89 offen offset:2048 lds\n"
      "buffer_load_dwordx4 v31, s[44:47], s89 offen offset:3072 lds\n"
      "s_add_u32 m0, s88, 53248\n"
      "s_add_u32 s89, s89, 32768\n"
      "buffer_load_dwordx4 v28, s[44:47], s89 offen offset:0 lds\n"
      "buffer_load_dwordx4 v29, s[44:47], s89 offen offset:1024 lds\n"
      "buffer_load_dwordx4 v30, s[44:47], s89 offen offset:2048 lds\n"
      "buffer_load_dwordx4 v31, s[44:47], s89 offen offset:3072 lds\n"
      "s_waitcnt vmcnt(24)\n"
      "v_mov_b32 v18, 2\n"
      "ds_write_b32 v17, v18 offset:16\n"
      "ds_read_b128 v[20:23], v16 offset:0\n"
      "s_waitcnt lgkmcnt(0)\n"
      "v_min_u32 v18, v20, v21\n"
      "v_min3_u32 v18, v18, v22, v23\n"
      "v_cmp_gt_u32 vcc, 2, v18\n"
      "s_nop 0\n"
      "s_cbranch_vccz .Lss2_ok\n"
      "s_nop 2\n"
      "s_mov_b32 s90, 0x100000\n"
      ".Lss2:\n"
      "s_sleep 2\n"
      "ds_read_b128 v[20:23], v16 offset:0\n"
      "s_waitcnt lgkmcnt(0)\n"
      "v_min_u32 v18, v20, v21\n"
      "v_min3_u32 v18, v18, v22, v23\n"
      "v_cmp_gt_u32 vcc, 2, v18\n"
      "s_sub_u32 s90, s90, 1\n"
      "s_nop 3\n"
      "s_cbranch_vccz .Lss2_ok\n"
      "s_cbranch_scc0 .Lss2\n"
      ".Lss2_ok:\n"
      "s_add_u32 m0, s88, 65536\n"
      "s_add_u32 s89, s35, 4\n"
      "s_and_b32 s89, s89, 7\n"
      "s_lshl_b32 s89, s89, 8\n"
      "buffer_load_dwordx4 v24, s[40:43], s89 offen offset:0 lds\n"
      "buffer_load_dwordx4 v25, s[40:43], s89 offen offset:1024 lds\n"
      "buffer_load_dwordx4 v26, s[40:43], s89 offen offset:2048 lds\n"
      "buffer_load_dwordx4 v27, s[40:43], s89 offen offset:3072 lds\n"
      "s_add_u32 m0, s88, 69632\n"
      "s_add_u32 s89, s89, 32768\n"
      "buffer_load_dwordx4 v24, s[40:43], s89 offen offset:0 lds\n"
      "buffer_load_dwordx4 v25, s[40:43], s89 offen offset:1024 lds\n"
      "buffer_load_dwordx4 v26, s[40:43], s89 offen offset:2048 lds\n"
      "buffer_load_dwordx4 v27, s[40:43], s89 offen offset:3072 lds\n"
      "s_add_u32 m0, s88, 81920\n"
      "s_add_u32 s89, s35, 4\n"
      "s_and_b32 s89, s89, 7\n"
      "s_lshl_b32 s89, s89, 18\n"
      "buffer_load_dwordx4 v28, s[44:47], s89 offen offset:0 lds\n"
      "buffer_load_dwordx4 v29, s[44:47], s89 offen offset:1024 lds\n"
      "buffer_load_dwordx4 v30, s[44:47], s89 offen offset:2048 lds\n"
      "buffer_load_dwordx4 v31, s[44:47], s89 offen offset:3072 lds\n"
      "s_add_u32 m0, s88, 86016\n"
      "s_add_u32 s89, s89, 32768\n"
      "buffer_load_dwordx4 v28, s[44:47], s89 offen offset:0 lds\n"
      "buffer_load_dwordx4 v29, s[44:47], s89 offen offset:1024 lds\n"
      "buffer_load_dwordx4 v30, s[44:47], s89 offen offset:2048 lds\n"
      "buffer_load_dwordx4 v31, s[44:47], s89 offen offset:3072 lds\n"
      "s_add_u32 m0, s88, 98304\n"
      "s_add_u32 s89, s89, 98304\n"
      "buffer_load_dwordx4 v28, s[44:47], s89 offen offset:0 lds\n"
      "buffer_load_dwordx4 v29, s[44:47], s89 offen offset:1024 lds\n"
      "buffer_load_dwordx4 v30, s[44:47], s89 offen offset:2048 lds\n"
      "buffer_load_dwordx4 v31, s[44:47], s89 offen offset:3072 lds\n"
      "s_add_u32 m0, s88, 102400\n"
      "s_add_u32 s89, s89, 32768\n"
      "buffer_load_dwordx4 v28, s[44:47], s89 offen offset:0 lds\n"
      "buffer_load_dwordx4 v29, s[44:47], s89 offen offset:1024 lds\n"
      "buffer_load_dwordx4 v30, s[44:47], s89 offen offset:2048 lds\n"
      "buffer_load_dwordx4 v31, s[44:47], s89 offen offset:3072 lds\n"
      "s_waitcnt vmcnt(24)\n"
      "v_mov_b32 v18, 3\n"
      "ds_write_b32 v17, v18 offset:16\n"
      "ds_read_b128 v[20:23], v16 offset:0\n"
      "s_waitcnt lgkmcnt(0)\n"
      "v_min_u32 v18, v20, v21\n"
      "v_min3_u32 v18, v18, v22, v23\n"
      "v_cmp_gt_u32 vcc, 3, v18\n"
      "s_nop 0\n"
      "s_cbranch_vccz .Lss3_ok\n"
      "s_nop 2\n"
      "s_mov_b32 s90, 0x100000\n"
      ".Lss3:\n"
      "s_sleep 2\n"
      "ds_read_b128 v[20:23], v16 offset:0\n"
      "s_waitcnt lgkmcnt(0)\n"
      "v_min_u32 v18, v20, v21\n"
      "v_min3_u32 v18, v18, v22, v23\n"
      "v_cmp_gt_u32 vcc, 3, v18\n"
      "s_sub_u32 s90, s90, 1\n"
      "s_nop 3\n"
      "s_cbranch_vccz .Lss3_ok\n"
      "s_cbranch_scc0 .Lss3\n"
      ".Lss3_ok:\n"
      "s_add_u32 m0, s88, 114688\n"
      "s_add_u32 s89, s35, 5\n"
      "s_and_b32 s89, s89, 7\n"
      "s_lshl_b32 s89, s89, 8\n"
      "buffer_load_dwordx4 v24, s[40:43], s89 offen offset:0 lds\n"
      "buffer_load_dwordx4 v25, s[40:43], s89 offen offset:1024 lds\n"
      "buffer_load_dwordx4 v26, s[40:43], s89 offen offset:2048 lds\n"
      "buffer_load_dwordx4 v27, s[40:43], s89 offen offset:3072 lds\n"
      "s_add_u32 m0, s88, 118784\n"
      "s_add_u32 s89, s89, 32768\n"
      "buffer_load_dwordx4 v24, s[40:43], s89 offen offset:0 lds\n"
      "buffer_load_dwordx4 v25, s[40:43], s89 offen offset:1024 lds\n"
      "buffer_load_dwordx4 v26, s[40:43], s89 offen offset:2048 lds\n"
      "buffer_load_dwordx4 v27, s[40:43], s89 offen offset:3072 lds\n"
      "s_add_u32 m0, s88, 131072\n"
      "s_add_u32 s89, s35, 5\n"
      "s_and_b32 s89, s89, 7\n"
      "s_lshl_b32 s89, s89, 18\n"
      "buffer_load_dwordx4 v28, s[44:47], s89 offen offset:0 lds\n"
      "buffer_load_dwordx4 v29, s[44:47], s89 offen offset:1024 lds\n"
      "buffer_load_dwordx4 v30, s[44:47], s89 offen offset:2048 lds\n"
      "buffer_load_dwordx4 v31, s[44:47], s89 offen offset:3072 lds\n"
      "s_add_u32 m0, s88, 135168\n"
      "s_add_u32 s89, s89, 32768\n"
      "buffer_load_dwordx4 v28, s[44:47], s89 offen offset:0 lds\n"
      "buffer_load_dwordx4 v29, s[44:47], s89 offen offset:1024 lds\n"
      "buffer_load_dwordx4 v30, s[44:47], s89 offen offset:2048 lds\n"
      "buffer_load_dwordx4 v31, s[44:47], s89 offen offset:3072 lds\n"
      "s_add_u32 m0, s88, 0\n"
      "s_add_u32 s89, s89, 98304\n"
      "buffer_load_dwordx4 v28, s[44:47], s89 offen offset:0 lds\n"
      "buffer_load_dwordx4 v29, s[44:47], s89 offen offset:1024 lds\n"
      "buffer_load_dwordx4 v30, s[44:47], s89 offen offset:2048 lds\n"
      "buffer_load_dwordx4 v31, s[44:47], s89 offen offset:3072 lds\n"
      "s_add_u32 m0, s88, 4096\n"
      "s_add_u32 s89, s89, 32768\n"
      "buffer_load_dwordx4 v28, s[44:47], s89 offen offset:0 lds\n"
      "buffer_load_dwordx4 v29, s[44:47], s89 offen offset:1024 lds\n"
      "buffer_load_dwordx4 v30, s[44:47], s89 offen offset:2048 lds\n"
      "buffer_load_dwordx4 v31, s[44:47], s89 offen offset:3072 lds\n"
      "s_waitcnt vmcnt(24)\n"
      "v_mov_b32 v18, 4\n"
      "ds_write_b32 v17, v18 offset:16\n"
      "ds_read_b128 v[20:23], v16 offset:0\n"
      "s_waitcnt lgkmcnt(0)\n"
      "v_min_u32 v18, v20, v21\n"
      "v_min3_u32 v18, v18, v22, v23\n"
      "v_cmp_gt_u32 vcc, 4, v18\n"
      "s_nop 0\n"
      "s_cbranch_vccz .Lss4_ok\n"
      "s_nop 2\n"
      "s_mov_b32 s90, 0x100000\n"
      ".Lss4:\n"
      "s_sleep 2\n"
      "ds_read_b128 v[20:23], v16 offset:0\n"
      "s_waitcnt lgkmcnt(0)\n"
      "v_min_u32 v18, v20, v21\n"
      "v_min3_u32 v18, v18, v22, v23\n"
      "v_cmp_gt_u32 vcc, 4, v18\n"
      "s_sub_u32 s90, s90, 1\n"
      "s_nop 3\n"
      "s_cbranch_vccz .Lss4_ok\n"
      "s_cbranch_scc0 .Lss4\n"
      ".Lss4_ok:\n"
      "s_add_u32 m0, s88, 16384\n"
      "s_add_u32 s89, s35, 6\n"
      "s_and_b32 s89, s89, 7\n"
      "s_lshl_b32 s89, s89, 8\n"
      "buffer_load_dwordx4 v24, s[40:43], s89 offen offset:0 lds\n"
      "buffer_load_dwordx4 v25, s[40:43], s89 offen offset:1024 lds\n"
      "buffer_load_dwordx4 v26, s[40:43], s89 offen offset:2048 lds\n"
      "buffer_load_dwordx4 v27, s[40:43], s89 offen offset:3072 lds\n"
      "s_add_u32 m0, s88, 20480\n"
      "s_add_u32 s89, s89, 32768\n"
      "buffer_load_dwordx4 v24, s[40:43], s89 offen offset:0 lds\n"
      "buffer_load_dwordx4 v25, s[40:43], s89 offen offset:1024 lds\n"
      "buffer_load_dwordx4 v26, s[40:43], s89 offen offset:2048 lds\n"
      "buffer_load_dwordx4 v27, s[40:43], s89 offen offset:3072 lds\n"
      "s_add_u32 m0, s88, 32768\n"
      "s_add_u32 s89, s35, 6\n"
      "s_and_b32 s89, s89, 7\n"
      "s_lshl_b32 s89, s89, 18\n"
      "buffer_load_dwordx4 v28, s[44:47], s89 offen offset:0 lds\n"
      "buffer_load_dwordx4 v29, s[44:47], s89 offen offset:1024 lds\n"
      "buffer_load_dwordx4 v30, s[44:47], s89 offen offset:2048 lds\n"
      "buffer_load_dwordx4 v31, s[44:47], s89 offen offset:3072 lds\n"
      "s_add_u32 m0, s88, 36864\n"
      "s_add_u32 s89, s89, 32768\n"
      "buffer_load_dwordx4 v28, s[44:47], s89 offen offset:0 lds\n"
      "buffer_load_dwordx4 v29, s[44:47], s89 offen offset:1024 lds\n"
      "buffer_load_dwordx4 v30, s[44:47], s89 offen offset:2048 lds\n"
      "buffer_load_dwordx4 v31, s[44:47], s89 offen offset:3072 lds\n"
      "s_add_u32 m0, s88, 49152\n"
      "s_add_u32 s89, s89, 98304\n"
      "buffer_load_dwordx4 v28, s[44:47], s89 offen offset:0 lds\n"
      "buffer_load_dwordx4 v29, s[44:47], s89 offen offset:1024 lds\n"
      "buffer_load_dwordx4 v30, s[44:47], s89 offen offset:2048 lds\n"
      "buffer_load_dwordx4 v31, s[44:47], s89 offen offset:3072 lds\n"
      "s_add_u32 m0, s88, 53248\n"
      "s_add_u32 s89, s89, 32768\n"
      "buffer_load_dwordx4 v28, s[44:47], s89 offen offset:0 lds\n"
      "buffer_load_dwordx4 v29, s[44:47], s89 offen offset:1024 lds\n"
      "buffer_load_dwordx4 v30, s[44:47], s89 offen offset:2048 lds\n"
      "buffer_load_dwordx4 v31, s[44:47], s89 offen offset:3072 lds\n"
      "s_waitcnt vmcnt(24)\n"
      "v_mov_b32 v18, 5\n"
      "ds_write_b32 v17, v18 offset:16\n"
      "ds_read_b128 v[20:23], v16 offset:0\n"
      "s_waitcnt lgkmcnt(0)\n"
      "v_min_u32 v18, v20, v21\n"
      "v_min3_u32 v18, v18, v22, v23\n"
      "v_cmp_gt_u32 vcc, 5, v18\n"
      "s_nop 0\n"
      "s_cbranch_vccz .Lss5_ok\n"
      "s_nop 2\n"
      "s_mov_b32 s90, 0x100000\n"
      ".Lss5:\n"
      "s_sleep 2\n"
      "ds_read_b128 v[20:23], v16 offset:0\n"
      "s_waitcnt lgkmcnt(0)\n"
      "v_min_u32 v18, v20, v21\n"
      "v_min3_u32 v18, v18, v22, v23\n"
      "v_cmp_gt_u32 vcc, 5, v18\n"
      "s_sub_u32 s90, s90, 1\n"
      "s_nop 3\n"
      "s_cbranch_vccz .Lss5_ok\n"
      "s_cbranch_scc0 .Lss5\n"
      ".Lss5_ok:\n"
      "s_add_u32 m0, s88, 65536\n"
      "s_add_u32 s89, s35, 7\n"
      "s_and_b32 s89, s89, 7\n"
      "s_lshl_b32 s89, s89, 8\n"
      "buffer_load_dwordx4 v24, s[40:43], s89 offen offset:0 lds\n"
      "buffer_load_dwordx4 v25, s[40:43], s89 offen offset:1024 lds\n"
      "buffer_load_dwordx4 v26, s[40:43], s89 offen offset:2048 lds\n"
      "buffer_load_dwordx4 v27, s[40:43], s89 offen offset:3072 lds\n"
      "s_add_u32 m0, s88, 69632\n"
      "s_add_u32 s89, s89, 32768\n"
      "buffer_load_dwordx4 v24, s[40:43], s89 offen offset:0 lds\n"
      "buffer_load_dwordx4 v25, s[40:43], s89 offen offset:1024 lds\n"
      "buffer_load_dwordx4 v26, s[40:43], s89 offen offset:2048 lds\n"
      "buffer_load_dwordx4 v27, s[40:43], s89 offen offset:3072 lds\n"
      "s_add_u32 m0, s88, 81920\n"
      "s_add_u32 s89, s35, 7\n"
      "s_and_b32 s89, s89, 7\n"
      "s_lshl_b32 s89, s89, 18\n"
      "buffer_load_dwordx4 v28, s[44:47], s89 offen offset:0 lds\n"
      "buffer_load_dwordx4 v29, s[44:47], s89 offen offset:1024 lds\n"
      "buffer_load_dwordx4 v30, s[44:47], s89 offen offset:2048 lds\n"
      "buffer_load_dwordx4 v31, s[44:47], s89 offen offset:3072 lds\n"
      "s_add_u32 m0, s88, 86016\n"
      "s_add_u32 s89, s89, 32768\n"
      "buffer_load_dwordx4 v28, s[44:47], s89 offen offset:0 lds\n"
      "buffer_load_dwordx4 v29, s[44:47], s89 offen offset:1024 lds\n"
      "buffer_load_dwordx4 v30, s[44:47], s89 offen offset:2048 lds\n"
      "buffer_load_dwordx4 v31, s[44:47], s89 offen offset:3072 lds\n"
      "s_add_u32 m0, s88, 98304\n"
      "s_add_u32 s89, s89, 98304\n"
      "buffer_load_dwordx4 v28, s[44:47], s89 offen offset:0 lds\n"
      "buffer_load_dwordx4 v29, s[44:47], s89 offen offset:1024 lds\n"
      "buffer_load_dwordx4 v30, s[44:47], s89 offen offset:2048 lds\n"
      "buffer_load_dwordx4 v31, s[44:47], s89 offen offset:3072 lds\n"
      "s_add_u32 m0, s88, 102400\n"
      "s_add_u32 s89, s89, 32768\n"
      "buffer_load_dwordx4 v28, s[44:47], s89 offen offset:0 lds\n"
      "buffer_load_dwordx4 v29, s[44:47], s89 offen offset:1024 lds\n"
      "buffer_load_dwordx4 v30, s[44:47], s89 offen offset:2048 lds\n"
      "buffer_load_dwordx4 v31, s[44:47], s89 offen offset:3072 lds\n"
      "s_waitcnt vmcnt(24)\n"
      "v_mov_b32 v18, 6\n"
      "ds_write_b32 v17, v18 offset:16\n"
      "s_waitcnt vmcnt(0)\n"
      "v_mov_b32 v18, 7\n"
      "ds_write_b32 v17, v18 offset:16\n"
      "s_branch .Lhjoin\n"
      ".Lexch:\n"
      "s_setprio 1\n"
      "s_waitcnt lgkmcnt(0)\n"
      "s_load_dword s36, s[28:29], 0\n"
      "s_mov_b32 s48, s28\n"
      "s_and_b32 s49, s29, 0xffff\n"
      "s_mov_b32 s50, 6553600\n"
      "s_mov_b32 s51, 0x20000\n"
      "v_mov_b32 v111, 0xffffff00\n"
      "s_and_b32 s80, s39, 7\n"
      "s_mul_i32 s80, s80, 28\n"
      "s_lshr_b32 s81, s39, 3\n"
      "s_add_u32 s7, s80, s81\n"
      "s_cmp_lt_u32 s81, 28\n"
      "s_cselect_b32 s82, 7340032, 0\n"
      "s_cselect_b32 s7, s7, 0x200000\n"
      "s_mov_b32 s56, s20\n"
      "s_and_b32 s57, s21, 0xffff\n"
      "s_mov_b32 s58, s82\n"
      "s_mov_b32 s59, 0x20000\n"
      "s_mov_b32 s60, s22\n"
      "s_and_b32 s61, s23, 0xffff\n"
      "s_mov_b32 s62, s82\n"
      "s_mov_b32 s63, 0x20000\n"
      "s_mov_b32 s64, s24\n"
      "s_and_b32 s65, s25, 0xffff\n"
      "s_mov_b32 s66, s82\n"
      "s_mov_b32 s67, 0x20000\n"
      "s_and_b32 s83, s7, 0xff\n"
      "s_and_b32 s84, s83, 3\n"
      "s_lshl_b32 s84, s84, 2\n"
      "s_add_u32 s84, s84, s30\n"
      "s_mul_i32 s95, s84, 458752\n"
      "s_lshl_b32 s96, s84, 7\n"
      "s_lshr_b32 s84, s83, 2\n"
      "s_lshl_b32 s83, s84, 7\n"
      "s_add_u32 s95, s95, s83\n"
      "s_lshl_b32 s84, s84, 17\n"
      "s_add_u32 s96, s96, s84\n"
      "s_lshl_b32 s94, s79, 8\n"
      "s_lshl_b32 s81, s30, 6\n"
      "s_add_u32 s94, s94, s81\n"
      "s_add_u32 s52, s16, s94\n"
      "s_addc_u32 s53, s17, 0\n"
      "s_and_b32 s53, s53, 0xffff\n"
      "s_mov_b32 s54, 2097152\n"
      "s_mov_b32 s55, 0x20000\n"
      "s_sub_u32 s54, s54, s94\n"
      "s_mul_i32 s5, s30, 7168\n"
      "s_add_u32 s5, s5, 131072\n"
      "s_lshl_b32 s6, s30, 14\n"
      "s_add_u32 s6, s6, 65536\n"
      "s_lshl_b32 s4, s30, 12\n"
      "v_add_u32 v2, s4, v9\n"
      "v_and_b32 v10, 31, v1\n"
      "v_lshrrev_b32 v11, 5, v1\n"
      "v_lshrrev_b32 v12, 2, v1\n"
      "v_lshlrev_b32 v12, 11, v12\n"
      "v_and_b32 v13, 3, v1\n"
      "v_lshl_add_u32 v3, v13, 4, v12\n"
      "v_bfe_u32 v15, v1, 4, 1\n"
      "v_lshlrev_b32 v15, 5, v15\n"
      "v_lshl_add_u32 v15, v13, 3, v15\n"
      "v_bfe_u32 v12, v1, 2, 2\n"
      "v_lshl_add_u32 v14, v11, 2, v12\n"
      "v_lshl_add_u32 v14, v14, 6, v15\n"
      "v_add_u32 v4, s5, v14\n"
      "v_lshl_add_u32 v14, v11, 3, v12\n"
      "v_lshl_add_u32 v14, v14, 7, v15\n"
      "v_add_u32 v7, s6, v14\n"
      "v_lshrrev_b32 v14, 4, v1\n"
      "v_lshl_add_u32 v14, v14, 3, v12\n"
      "v_lshlrev_b32 v13, 3, v13\n"
      "v_lshl_add_u32 v14, v14, 7, v13\n"
      "v_add_u32 v6, s6, v14\n"
      "v_lshrrev_b32 v12, 3, v1\n"
      "v_mul_u32_u24 v13, 7168, v12\n"
      "v_and_b32 v14, 7, v1\n"
      "v_lshl_add_u32 v5, v14, 4, v13\n"
      "v_lshlrev_b32 v13, 11, v12\n"
      "v_lshl_add_u32 v8, v14, 4, v13\n"
      "s_lshl_b32 s77, s31, 17\n"
      "s_lshl_b32 s81, s79, 14\n"
      "s_add_u32 s77, s77, s81\n"
      "s_lshl_b32 s81, s33, 10\n"
      "s_add_u32 s77, s77, s81\n"
      "s_add_u32 s77, s77, 2359296\n"
      "s_lshl_b32 s78, s31, 13\n"
      "s_lshl_b32 s81, s33, 8\n"
      "s_add_u32 s78, s78, s81\n"
      "s_add_u32 s78, s78, 786432\n"
      "s_lshl_b32 s20, s31, 11\n"
      "s_lshl_b32 s81, s79, 5\n"
      "s_add_u32 s20, s20, s81\n"
      "s_add_u32 s20, s20, 1048576\n"
      "s_lshl_b32 s82, s34, 31\n"
      "v_add_u32 v16, s82, v9\n"
      "v_add_u32 v17, 4096, v16\n"
      "v_add_u32 v18, 8192, v16\n"
      "v_add_u32 v19, 12288, v16\n"
      "v_lshl_add_u32 v20, v10, 3, s82\n"
      "v_lshl_add_u32 v20, v11, 9, v20\n"
      "v_add_u32 v22, 4096, v20\n"
      "v_lshrrev_b32 v21, 3, v1\n"
      "v_lshlrev_b32 v21, 8, v21\n"
      "v_and_b32 v12, 7, v1\n"
      "v_lshl_add_u32 v21, v12, 2, v21\n"
      "v_add_u32 v21, s82, v21\n"
      "s_lshl_b32 s97, s31, 1\n"
      "s_add_u32 s97, s97, s33\n"
      "s_lshl_b32 s18, s97, 12\n"
      "s_add_u32 s18, s18, 65536\n"
      "s_and_b32 s83, s79, 3\n"
      "s_lshl_b32 s84, s83, 7\n"
      "s_add_u32 s19, s18, s84\n"
      "s_lshl_b32 s84, s83, 9\n"
      "s_add_u32 s18, s18, s84\n"
      "s_lshr_b32 s83, s79, 2\n"
      "s_lshl_b32 s84, s83, 31\n"
      "s_xor_b32 s83, s84, 0x80000000\n"
      "v_lshl_add_u32 v80, v10, 4, s84\n"
      "v_lshl_add_u32 v80, v11, 30, v80\n"
      "v_bfe_u32 v13, v1, 3, 2\n"
      "v_lshl_add_u32 v81, v13, 9, s83\n"
      "v_lshl_add_u32 v81, v12, 4, v81\n"
      "v_lshl_add_u32 v81, v11, 30, v81\n"
      "s_lshl_b32 s84, s30, 9\n"
      "s_add_u32 s84, s84, 158336\n"
      "v_lshl_add_u32 v82, v10, 1, s84\n"
      "v_lshl_add_u32 v82, v11, 8, v82\n"
      "v_lshl_add_u32 v83, v12, 6, s84\n"
      "v_lshl_add_u32 v83, v13, 4, v83\n"
      "s_lshl_b32 s80, s30, 5\n"
      "s_add_u32 s80, s80, 157696\n"
      "v_lshl_add_u32 v84, v12, 2, s80\n"
      "v_lshl_add_u32 v85, v11, 4, s80\n"
      "s_lshl_b32 s80, s33, 8\n"
      "s_add_u32 s80, s80, 163328\n"
      "v_lshl_add_u32 v87, v10, 3, s80\n"
      "s_lshl_b32 s0, s79, 14\n"
      "s_lshl_b32 s81, s33, 8\n"
      "s_add_u32 s0, s0, s81\n"
      "s_add_u32 s0, s0, 1114112\n"
      "s_lshl_b32 s80, s31, 9\n"
      "s_add_u32 s1, s0, s80\n"
      "s_and_b32 s80, s31, 24\n"
      "s_lshl_b32 s80, s80, 9\n"
      "s_add_u32 s0, s0, s80\n"
      "v_lshlrev_b32 v94, 2, v1\n"
      "s_lshl_b32 s80, s33, 8\n"
      "s_add_u32 s81, s80, 151552\n"
      "v_add_u32 v95, s81, v94\n"
      "s_add_u32 s81, s80, 157824\n"
      "v_add_u32 v96, s81, v94\n"
      "v_mov_b32 v97, 148288\n"
      "s_lshl_b32 s80, s33, 2\n"
      "s_add_u32 s80, s80, 148312\n"
      "v_mov_b32 v90, s80\n"
      "v_mov_b32 v91, 1\n"
      "s_lshl_b32 s80, s34, 31\n"
      "v_add_u32 v99, s80, v94\n"
      "s_waitcnt lgkmcnt(0)\n"
      "s_add_u32 s36, s36, 1\n"
      "s_and_b32 s80, s36, 0xff\n"
      "v_mov_b32 v110, s80\n"
      "s_barrier\n"
      "s_barrier\n"
      "ds_read_b128 v[100:103], v97 offset:0\n"
      "s_waitcnt lgkmcnt(0)\n"
      "v_min_u32 v98, v100, v101\n"
      "v_min3_u32 v98, v98, v102, v103\n"
      "v_cmp_gt_u32 vcc, 2, v98\n"
      "s_nop 0\n"
      "s_cbranch_vccz .Lscp_ok\n"
      "s_nop 2\n"
      "s_mov_b32 s90, 0x100000\n"
      ".Lscp:\n"
      "s_sleep 2\n"
      "ds_read_b128 v[100:103], v97 offset:0\n"
      "s_waitcnt lgkmcnt(0)\n"
      "v_min_u32 v98, v100, v101\n"
      "v_min3_u32 v98, v98, v102, v103\n"
      "v_cmp_gt_u32 vcc, 2, v98\n"
      "s_sub_u32 s90, s90, 1\n"
      "s_nop 3\n"
      "s_cbranch_vccz .Lscp_ok\n"
      "s_cbranch_scc0 .Lscp\n"
      ".Lscp_ok:\n"
      "ds_read_b32 v98, v95\n"
      "s_waitcnt lgkmcnt(0)\n"
      "v_and_or_b32 v98, v98, v111, v110\n"
      "buffer_store_dword v98, v94, s[48:51], s1 offen sc1\n"
      "buffer_load_dwordx4 v[112:115], v16, s[48:51], s77 offen offset:0 sc1\n"
      "buffer_load_dwordx4 v[116:119], v16, s[48:51], s77 offen offset:2048 sc1\n"
      "buffer_load_dwordx4 v[120:123], v17, s[48:51], s77 offen offset:0 sc1\n"
      "buffer_load_dwordx4 v[124:127], v17, s[48:51], s77 offen offset:2048 sc1\n"
      "buffer_load_dwordx4 v[128:131], v18, s[48:51], s77 offen offset:0 sc1\n"
      "buffer_load_dwordx4 v[132:135], v18, s[48:51], s77 offen offset:2048 sc1\n"
      "buffer_load_dwordx4 v[136:139], v19, s[48:51], s77 offen offset:0 sc1\n"
      "buffer_load_dwordx4 v[140:143], v19, s[48:51], s77 offen offset:2048 sc1\n"
      "buffer_load_dwordx2 v[144:145], v20, s[48:51], s78 offen offset:0 sc1\n"
      "buffer_load_dwordx2 v[146:147], v20, s[48:51], s78 offen offset:1024 sc1\n"
      "buffer_load_dwordx2 v[148:149], v20, s[48:51], s78 offen offset:2048 sc1\n"
      "buffer_load_dwordx2 v[150:151], v20, s[48:51], s78 offen offset:3072 sc1\n"
      "buffer_load_dwordx2 v[152:153], v22, s[48:51], s78 offen offset:0 sc1\n"
      "buffer_load_dwordx2 v[154:155], v22, s[48:51], s78 offen offset:1024 sc1\n"
      "buffer_load_dwordx2 v[156:157], v22, s[48:51], s78 offen offset:2048 sc1\n"
      "buffer_load_dwordx2 v[158:159], v22, s[48:51], s78 offen offset:3072 sc1\n"
      "buffer_load_dword v88, v21, s[48:51], s20 offen sc1\n"
      "s_waitcnt vmcnt(0)\n"
      "v_xor_b32 v12, v112, v110\n"
      "v_xor_b32 v13, v88, v110\n"
      "v_bitop3_b32 v13, v13, v113, v110 bitop3:0xf6\n"
      "v_bitop3_b32 v12, v12, v114, v110 bitop3:0xf6\n"
      "v_bitop3_b32 v13, v13, v115, v110 bitop3:0xf6\n"
      "v_bitop3_b32 v12, v12, v116, v110 bitop3:0xf6\n"
      "v_bitop3_b32 v13, v13, v117, v110 bitop3:0xf6\n"
      "v_bitop3_b32 v12, v12, v118, v110 bitop3:0xf6\n"
      "v_bitop3_b32 v13, v13, v119, v110 bitop3:0xf6\n"
      "v_bitop3_b32 v12, v12, v120, v110 bitop3:0xf6\n"
      "v_bitop3_b32 v13, v13, v121, v110 bitop3:0xf6\n"
      "v_bitop3_b32 v12, v12, v122, v110 bitop3:0xf6\n"
      "v_bitop3_b32 v13, v13, v123, v110 bitop3:0xf6\n"
      "v_bitop3_b32 v12, v12, v124, v110 bitop3:0xf6\n"
      "v_bitop3_b32 v13, v13, v125, v110 bitop3:0xf6\n"
      "v_bitop3_b32 v12, v12, v126, v110 bitop3:0xf6\n"
      "v_bitop3_b32 v13, v13, v127, v110 bitop3:0xf6\n"
      "v_bitop3_b32 v12, v12, v128, v110 bitop3:0xf6\n"
      "v_bitop3_b32 v13, v13, v129, v110 bitop3:0xf6\n"
      "v_bitop3_b32 v12, v12, v130, v110 bitop3:0xf6\n"
      "v_bitop3_b32 v13, v13, v131, v110 bitop3:0xf6\n"
      "v_bitop3_b32 v12, v12, v132, v110 bitop3:0xf6\n"
      "v_bitop3_b32 v13, v13, v133, v110 bitop3:0xf6\n"
      "v_bitop3_b32 v12, v12, v134, v110 bitop3:0xf6\n"
      "v_bitop3_b32 v13, v13, v135, v110 bitop3:0xf6\n"
      "v_bitop3_b32 v12, v12, v136, v110 bitop3:0xf6\n"
      "v_bitop3_b32 v13, v13, v137, v110 bitop3:0xf6\n"
      "v_bitop3_b32 v12, v12, v138, v110 bitop3:0xf6\n"
      "v_bitop3_b32 v13, v13, v139, v110 bitop3:0xf6\n"
      "v_bitop3_b32 v12, v12, v140, v110 bitop3:0xf6\n"
      "v_bitop3_b32 v13, v13, v141, v110 bitop3:0xf6\n"
      "v_bitop3_b32 v12, v12, v142, v110 bitop3:0xf6\n"
      "v_bitop3_b32 v13, v13, v143, v110 bitop3:0xf6\n"
      "v_bitop3_b32 v12, v12, v144, v110 bitop3:0xf6\n"
      "v_bitop3_b32 v13, v13, v145, v110 bitop3:0xf6\n"
      "v_bitop3_b32 v12, v12, v146, v110 bitop3:0xf6\n"
      "v_bitop3_b32 v13, v13, v147, v110 bitop3:0xf6\n"
      "v_bitop3_b32 v12, v12, v148, v110 bitop3:0xf6\n"
      "v_bitop3_b32 v13, v13, v149, v110 bitop3:0xf6\n"
      "v_bitop3_b32 v12, v12, v150, v110 bitop3:0xf6\n"
      "v_bitop3_b32 v13, v13, v151, v110 bitop3:0xf6\n"
      "v_bitop3_b32 v12, v12, v152, v110 bitop3:0xf6\n"
      "v_bitop3_b32 v13, v13, v153, v110 bitop3:0xf6\n"
      "v_bitop3_b32 v12, v12, v154, v110 bitop3:0xf6\n"
      "v_bitop3_b32 v13, v13, v155, v110 bitop3:0xf6\n"
      "v_bitop3_b32 v12, v12, v156, v110 bitop3:0xf6\n"
      "v_bitop3_b32 v13, v13, v157, v110 bitop3:0xf6\n"
      "v_bitop3_b32 v12, v12, v158, v110 bitop3:0xf6\n"
      "v_bitop3_b32 v13, v13, v159, v110 bitop3:0xf6\n"
      "v_or_b32 v12, v12, v13\n"
      "v_and_b32 v12, 0xff, v12\n"
      "v_cmp_ne_u32 vcc, 0, v12\n"
      "s_nop 0\n"
      "s_cbranch_vccz .Lpollp_ok\n"
      "s_nop 2\n"
      "s_mov_b32 s90, 0x20000\n"
      ".Lpollp:\n"
      "s_sleep 1\n"
      "buffer_load_dwordx4 v[112:115], v16, s[48:51], s77 offen offset:0 sc1\n"
      "buffer_load_dwordx4 v[116:119], v16, s[48:51], s77 offen offset:2048 sc1\n"
      "buffer_load_dwordx4 v[120:123], v17, s[48:51], s77 offen offset:0 sc1\n"
      "buffer_load_dwordx4 v[124:127], v17, s[48:51], s77 offen offset:2048 sc1\n"
      "buffer_load_dwordx4 v[128:131], v18, s[48:51], s77 offen offset:0 sc1\n"
      "buffer_load_dwordx4 v[132:135], v18, s[48:51], s77 offen offset:2048 sc1\n"
      "buffer_load_dwordx4 v[136:139], v19, s[48:51], s77 offen offset:0 sc1\n"
      "buffer_load_dwordx4 v[140:143], v19, s[48:51], s77 offen offset:2048 sc1\n"
      "buffer_load_dwordx2 v[144:145], v20, s[48:51], s78 offen offset:0 sc1\n"
      "buffer_load_dwordx2 v[146:147], v20, s[48:51], s78 offen offset:1024 sc1\n"
      "buffer_load_dwordx2 v[148:149], v20, s[48:51], s78 offen offset:2048 sc1\n"
      "buffer_load_dwordx2 v[150:151], v20, s[48:51], s78 offen offset:3072 sc1\n"
      "buffer_load_dwordx2 v[152:153], v22, s[48:51], s78 offen offset:0 sc1\n"
      "buffer_load_dwordx2 v[154:155], v22, s[48:51], s78 offen offset:1024 sc1\n"
      "buffer_load_dwordx2 v[156:157], v22, s[48:51], s78 offen offset:2048 sc1\n"
      "buffer_load_dwordx2 v[158:159], v22, s[48:51], s78 offen offset:3072 sc1\n"
      "buffer_load_dword v88, v21, s[48:51], s20 offen sc1\n"
      "s_waitcnt vmcnt(0)\n"
      "v_xor_b32 v12, v112, v110\n"
      "v_xor_b32 v13, v88, v110\n"
      "v_bitop3_b32 v13, v13, v113, v110 bitop3:0xf6\n"
      "v_bitop3_b32 v12, v12, v114, v110 bitop3:0xf6\n"
      "v_bitop3_b32 v13, v13, v115, v110 bitop3:0xf6\n"
      "v_bitop3_b32 v12, v12, v116, v110 bitop3:0xf6\n"
      "v_bitop3_b32 v13, v13, v117, v110 bitop3:0xf6\n"
      "v_bitop3_b32 v12, v12, v118, v110 bitop3:0xf6\n"
      "v_bitop3_b32 v13, v13, v119, v110 bitop3:0xf6\n"
      "v_bitop3_b32 v12, v12, v120, v110 bitop3:0xf6\n"
      "v_bitop3_b32 v13, v13, v121, v110 bitop3:0xf6\n"
      "v_bitop3_b32 v12, v12, v122, v110 bitop3:0xf6\n"
      "v_bitop3_b32 v13, v13, v123, v110 bitop3:0xf6\n"
      "v_bitop3_b32 v12, v12, v124, v110 bitop3:0xf6\n"
      "v_bitop3_b32 v13, v13, v125, v110 bitop3:0xf6\n"
      "v_bitop3_b32 v12, v12, v126, v110 bitop3:0xf6\n"
      "v_bitop3_b32 v13, v13, v127, v110 bitop3:0xf6\n"
      "v_bitop3_b32 v12, v12, v128, v110 bitop3:0xf6\n"
      "v_bitop3_b32 v13, v13, v129, v110 bitop3:0xf6\n"
      "v_bitop3_b32 v12, v12, v130, v110 bitop3:0xf6\n"
      "v_bitop3_b32 v13, v13, v131, v110 bitop3:0xf6\n"
      "v_bitop3_b32 v12, v12, v132, v110 bitop3:0xf6\n"
      "v_bitop3_b32 v13, v13, v133, v110 bitop3:0xf6\n"
      "v_bitop3_b32 v12, v12, v134, v110 bitop3:0xf6\n"
      "v_bitop3_b32 v13, v13, v135, v110 bitop3:0xf6\n"
      "v_bitop3_b32 v12, v12, v136, v110 bitop3:0xf6\n"
      "v_bitop3_b32 v13, v13, v137, v110 bitop3:0xf6\n"
      "v_bitop3_b32 v12, v12, v138, v110 bitop3:0xf6\n"
      "v_bitop3_b32 v13, v13, v139, v110 bitop3:0xf6\n"
      "v_bitop3_b32 v12, v12, v140, v110 bitop3:0xf6\n"
      "v_bitop3_b32 v13, v13, v141, v110 bitop3:0xf6\n"
      "v_bitop3_b32 v12, v12, v142, v110 bitop3:0xf6\n"
      "v_bitop3_b32 v13, v13, v143, v110 bitop3:0xf6\n"
      "v_bitop3_b32 v12, v12, v144, v110 bitop3:0xf6\n"
      "v_bitop3_b32 v13, v13, v145, v110 bitop3:0xf6\n"
      "v_bitop3_b32 v12, v12, v146, v110 bitop3:0xf6\n"
      "v_bitop3_b32 v13, v13, v147, v110 bitop3:0xf6\n"
      "v_bitop3_b32 v12, v12, v148, v110 bitop3:0xf6\n"
      "v_bitop3_b32 v13, v13, v149, v110 bitop3:0xf6\n"
      "v_bitop3_b32 v12, v12, v150, v110 bitop3:0xf6\n"
      "v_bitop3_b32 v13, v13, v151, v110 bitop3:0xf6\n"
      "v_bitop3_b32 v12, v12, v152, v110 bitop3:0xf6\n"
      "v_bitop3_b32 v13, v13, v153, v110 bitop3:0xf6\n"
      "v_bitop3_b32 v12, v12, v154, v110 bitop3:0xf6\n"
      "v_bitop3_b32 v13, v13, v155, v110 bitop3:0xf6\n"
      "v_bitop3_b32 v12, v12, v156, v110 bitop3:0xf6\n"
      "v_bitop3_b32 v13, v13, v157, v110 bitop3:0xf6\n"
      "v_bitop3_b32 v12, v12, v158, v110 bitop3:0xf6\n"
      "v_bitop3_b32 v13, v13, v159, v110 bitop3:0xf6\n"
      "v_or_b32 v12, v12, v13\n"
      "v_and_b32 v12, 0xff, v12\n"
      "v_cmp_ne_u32 vcc, 0, v12\n"
      "s_sub_u32 s90, s90, 1\n"
      "s_nop 3\n"
      "s_cbranch_vccz .Lpollp_ok\n"
      "s_cbranch_scc0 .Lpollp\n"
      ".Lpollp_ok:\n"
      "v_and_b32 v144, v111, v144\n"
      "v_and_b32 v145, v111, v145\n"
      "v_and_b32 v146, v111, v146\n"
      "v_and_b32 v147, v111, v147\n"
      "v_and_b32 v148, v111, v148\n"
      "v_and_b32 v149, v111, v149\n"
      "v_and_b32 v150, v111, v150\n"
      "v_and_b32 v151, v111, v151\n"
      "v_and_b32 v152, v111, v152\n"
      "v_and_b32 v153, v111, v153\n"
      "v_and_b32 v154, v111, v154\n"
      "v_and_b32 v155, v111, v155\n"
      "v_and_b32 v156, v111, v156\n"
      "v_and_b32 v157, v111, v157\n"
      "v_and_b32 v158, v111, v158\n"
      "v_and_b32 v159, v111, v159\n"
      "v_and_b32 v88, v111, v88\n"
      "v_and_b32 v112, v111, v112\n"
      "v_and_b32 v113, v111, v113\n"
      "v_and_b32 v114, v111, v114\n"
      "v_and_b32 v115, v111, v115\n"
      "v_and_b32 v116, v111, v116\n"
      "v_and_b32 v117, v111, v117\n"
      "v_and_b32 v118, v111, v118\n"
      "v_and_b32 v119, v111, v119\n"
      "v_and_b32 v120, v111, v120\n"
      "v_and_b32 v121, v111, v121\n"
      "v_and_b32 v122, v111, v122\n"
      "v_and_b32 v123, v111, v123\n"
      "v_and_b32 v124, v111, v124\n"
      "v_and_b32 v125, v111, v125\n"
      "v_and_b32 v126, v111, v126\n"
      "v_and_b32 v127, v111, v127\n"
      "v_and_b32 v128, v111, v128\n"
      "v_and_b32 v129, v111, v129\n"
      "v_and_b32 v130, v111, v130\n"
      "v_and_b32 v131, v111, v131\n"
      "v_and_b32 v132, v111, v132\n"
      "v_and_b32 v133, v111, v133\n"
      "v_and_b32 v134, v111, v134\n"
      "v_and_b32 v135, v111, v135\n"
      "v_and_b32 v136, v111, v136\n"
      "v_and_b32 v137, v111, v137\n"
      "v_and_b32 v138, v111, v138\n"
      "v_and_b32 v139, v111, v139\n"
      "v_and_b32 v140, v111, v140\n"
      "v_and_b32 v141, v111, v141\n"
      "v_and_b32 v142, v111, v142\n"
      "v_and_b32 v143, v111, v143\n"
      "v_add_f32 v144, v144, v152\n"
      "v_add_f32 v145, v145, v153\n"
      "v_add_f32 v146, v146, v154\n"
      "v_add_f32 v147, v147, v155\n"
      "v_add_f32 v148, v148, v156\n"
      "v_add_f32 v149, v149, v157\n"
      "v_add_f32 v150, v150, v158\n"
      "v_add_f32 v151, v151, v159\n"
      "v_add_f32 v144, v144, v148\n"
      "v_add_f32 v145, v145, v149\n"
      "v_add_f32 v146, v146, v150\n"
      "v_add_f32 v147, v147, v151\n"
      "v_add_f32 v144, v144, v146\n"
      "v_add_f32 v145, v145, v147\n"
      "v_add_f32_dpp v88, v88, v88 row_ror:8 row_mask:0xf bank_mask:0xf\n"
      "v_mov_b32 v146, v144\n"
      "v_mov_b32 v147, v145\n"
      "v_mov_b32 v93, v88\n"
      "v_permlane32_swap_b32 v144, v146\n"
      "v_permlane32_swap_b32 v145, v147\n"
      "v_permlane16_swap_b32 v88, v93\n"
      "v_add_f32 v88, v88, v93\n"
      "v_mul_f32 v148, 0x3a800000, v144\n"
      "v_mov_b32 v93, v88\n"
      "v_mul_f32 v149, 0x3a800000, v146\n"
      "v_add_f32 v112, v112, v128\n"
      "v_add_f32 v113, v113, v129\n"
      "v_add_f32 v114, v114, v130\n"
      "v_add_f32 v115, v115, v131\n"
      "v_add_f32 v116, v116, v132\n"
      "v_add_f32 v117, v117, v133\n"
      "v_add_f32 v118, v118, v134\n"
      "v_add_f32 v119, v119, v135\n"
      "v_add_f32 v120, v120, v136\n"
      "v_add_f32 v121, v121, v137\n"
      "v_add_f32 v122, v122, v138\n"
      "v_add_f32 v123, v123, v139\n"
      "v_add_f32 v124, v124, v140\n"
      "v_add_f32 v125, v125, v141\n"
      "v_add_f32 v126, v126, v142\n"
      "v_add_f32 v127, v127, v143\n"
      "v_permlane32_swap_b32 v88, v93\n"
      "v_fma_f32 v149, -v148, v148, v149\n"
      "v_add_f32 v88, v88, v93\n"
      "v_max_f32 v149, 0, v149\n"
      "ds_write_b32 v84, v88\n"
      "ds_read_b128 v[152:155], v85\n"
      "v_add_f32 v112, v112, v120\n"
      "v_cvt_pk_bf16_f32 v149, v149, v149\n"
      "v_add_f32 v113, v113, v121\n"
      "v_cvt_pk_bf16_f32 v34, v148, v148\n"
      "v_add_f32 v114, v114, v122\n"
      "v_lshlrev_b32 v149, 16, v149\n"
      "v_add_f32 v115, v115, v123\n"
      "v_lshlrev_b32 v34, 16, v34\n"
      "v_add_f32 v116, v116, v124\n"
      "v_add_f32 v149, 0x3727c5ac, v149\n"
      "v_add_f32 v117, v117, v125\n"
      "v_cvt_pk_bf16_f32 v149, v149, v149\n"
      "v_add_f32 v118, v118, v126\n"
      "v_lshlrev_b32 v149, 16, v149\n"
      "v_add_f32 v119, v119, v127\n"
      "v_sqrt_f32 v149, v149\n"
      "v_add_f32 v112, v112, v116\n"
      "v_cvt_pk_bf16_f32 v149, v149, v149\n"
      "v_add_f32 v113, v113, v117\n"
      "v_lshlrev_b32 v149, 16, v149\n"
      "v_add_f32 v114, v114, v118\n"
      "v_rcp_f32 v35, v149\n"
      "v_add_f32 v115, v115, v119\n"
      "s_waitcnt lgkmcnt(0)\n"
      "v_fma_f32 v152, -v34, v152, v112\n"
      "v_fma_f32 v153, -v34, v153, v113\n"
      "v_fma_f32 v154, -v34, v154, v114\n"
      "v_fma_f32 v155, -v34, v155, v115\n"
      "ds_write_b64 v87, v[34:35]\n"
      "v_mul_f32 v152, v152, v35\n"
      "v_mul_f32 v153, v153, v35\n"
      "v_mul_f32 v154, v154, v35\n"
      "v_mul_f32 v155, v155, v35\n"
      "v_cvt_pk_bf16_f32 v156, v152, v153\n"
      "v_cvt_pk_bf16_f32 v157, v154, v155\n"
      "ds_write_b16 v82, v156\n"
      "ds_write_b16_d16_hi v82, v156 offset:64\n"
      "ds_write_b16 v82, v157 offset:128\n"
      "ds_write_b16_d16_hi v82, v157 offset:192\n"
      "ds_read_b128 v[116:119], v83\n"
      "v_mov_b32 v158, v156\n"
      "v_mov_b32 v159, v157\n"
      "v_fma_f32 v120, -v34, v147, v145\n"
      "v_permlane32_swap_b32 v156, v158\n"
      "v_permlane32_swap_b32 v157, v159\n"
      "buffer_store_dwordx4 v[156:159], v80, s[48:51], s18 offen sc1\n"
      "s_waitcnt lgkmcnt(0)\n"
      "buffer_store_dwordx4 v[116:119], v81, s[48:51], s19 offen offset:2048 sc1\n"
      "buffer_load_dword v100, v94, s[48:51], s0 offen offset:0 sc1\n"
      "buffer_load_dword v101, v94, s[48:51], s0 offen offset:512 sc1\n"
      "buffer_load_dword v102, v94, s[48:51], s0 offen offset:1024 sc1\n"
      "buffer_load_dword v103, v94, s[48:51], s0 offen offset:1536 sc1\n"
      "buffer_load_dword v104, v94, s[48:51], s0 offen offset:2048 sc1\n"
      "buffer_load_dword v105, v94, s[48:51], s0 offen offset:2560 sc1\n"
      "buffer_load_dword v106, v94, s[48:51], s0 offen offset:3072 sc1\n"
      "buffer_load_dword v107, v94, s[48:51], s0 offen offset:3584 sc1\n"
      "s_lshl_b32 s80, s31, 9\n"
      "s_lshl_b32 s81, s79, 6\n"
      "s_add_u32 s80, s80, s81\n"
      "s_lshl_b32 s81, s33, 5\n"
      "s_add_u32 s80, s80, s81\n"
      "s_add_u32 s80, s80, 16384\n"
      "v_lshlrev_b32 v12, 26, v1\n"
      "v_mov_b32 v13, s36\n"
      "s_waitcnt vmcnt(8)\n"
      "buffer_store_dword v13, v12, s[48:51], s80 offen sc1\n"
      "s_waitcnt vmcnt(1)\n"
      "v_xor_b32 v12, v100, v110\n"
      "v_bitop3_b32 v12, v12, v101, v110 bitop3:0xf6\n"
      "v_bitop3_b32 v12, v12, v102, v110 bitop3:0xf6\n"
      "v_bitop3_b32 v12, v12, v103, v110 bitop3:0xf6\n"
      "v_bitop3_b32 v12, v12, v104, v110 bitop3:0xf6\n"
      "v_bitop3_b32 v12, v12, v105, v110 bitop3:0xf6\n"
      "v_bitop3_b32 v12, v12, v106, v110 bitop3:0xf6\n"
      "v_bitop3_b32 v12, v12, v107, v110 bitop3:0xf6\n"
      "v_and_b32 v12, 0xff, v12\n"
      "v_cmp_ne_u32 vcc, 0, v12\n"
      "s_nop 0\n"
      "s_cbranch_vccz .Lpollq_ok\n"
      "s_nop 2\n"
      "s_mov_b32 s90, 0x20000\n"
      ".Lpollq:\n"
      "s_sleep 1\n"
      "buffer_load_dword v100, v94, s[48:51], s0 offen offset:0 sc1\n"
      "buffer_load_dword v101, v94, s[48:51], s0 offen offset:512 sc1\n"
      "buffer_load_dword v102, v94, s[48:51], s0 offen offset:1024 sc1\n"
      "buffer_load_dword v103, v94, s[48:51], s0 offen offset:1536 sc1\n"
      "buffer_load_dword v104, v94, s[48:51], s0 offen offset:2048 sc1\n"
      "buffer_load_dword v105, v94, s[48:51], s0 offen offset:2560 sc1\n"
      "buffer_load_dword v106, v94, s[48:51], s0 offen offset:3072 sc1\n"
      "buffer_load_dword v107, v94, s[48:51], s0 offen offset:3584 sc1\n"
      "s_waitcnt vmcnt(0)\n"
      "v_xor_b32 v12, v100, v110\n"
      "v_bitop3_b32 v12, v12, v101, v110 bitop3:0xf6\n"
      "v_bitop3_b32 v12, v12, v102, v110 bitop3:0xf6\n"
      "v_bitop3_b32 v12, v12, v103, v110 bitop3:0xf6\n"
      "v_bitop3_b32 v12, v12, v104, v110 bitop3:0xf6\n"
      "v_bitop3_b32 v12, v12, v105, v110 bitop3:0xf6\n"
      "v_bitop3_b32 v12, v12, v106, v110 bitop3:0xf6\n"
      "v_bitop3_b32 v12, v12, v107, v110 bitop3:0xf6\n"
      "v_and_b32 v12, 0xff, v12\n"
      "v_cmp_ne_u32 vcc, 0, v12\n"
      "s_sub_u32 s90, s90, 1\n"
      "s_nop 3\n"
      "s_cbranch_vccz .Lpollq_ok\n"
      "s_cbranch_scc0 .Lpollq\n"
      ".Lpollq_ok:\n"
      "v_and_b32 v100, v111, v100\n"
      "v_and_b32 v101, v111, v101\n"
      "v_and_b32 v102, v111, v102\n"
      "v_and_b32 v103, v111, v103\n"
      "v_and_b32 v104, v111, v104\n"
      "v_and_b32 v105, v111, v105\n"
      "v_and_b32 v106, v111, v106\n"
      "v_and_b32 v107, v111, v107\n"
      "v_add_f32 v100, v100, v104\n"
      "v_add_f32 v101, v101, v105\n"
      "v_add_f32 v102, v102, v106\n"
      "v_add_f32 v103, v103, v107\n"
      "v_add_f32 v100, v100, v102\n"
      "v_add_f32 v101, v101, v103\n"
      "v_add_f32 v100, v100, v101\n"
      "ds_write_b32 v96, v100\n"
      "ds_write_b32 v90, v91\n"
      "v_mul_f32 v120, v120, v35\n"
      "v_mov_b32 v121, s36\n"
      "v_cvt_pk_bf16_f32 v120, v120, v120\n"
      "v_or_b32 v122, v34, v110\n"
      "v_and_b32 v120, 0xffff, v120\n"
      "v_or_b32 v123, v149, v110\n"
      "v_lshrrev_b32 v125, 15, v120\n"
      "s_lshl_b32 s83, s97, 5\n"
      "v_sub_u32 v125, 0, v125\n"
      "s_sub_u32 s83, 0xffff, s83\n"
      "v_or_b32 v125, 0x8000, v125\n"
      "v_sub_u32 v124, s83, v10\n"
      "v_xor_b32 v120, v120, v125\n"
      "v_lshl_or_b32 v120, v120, 16, v124\n"
      "v_mov_b32 v126, v120\n"
      "s_min_u32 s84, s79, 1\n"
      "s_lshl_b32 s84, s84, 31\n"
      "s_lshl_b32 s85, s97, 4\n"
      "s_add_u32 s85, s85, 4096\n"
      "v_mov_b32 v124, 0x80000000\n"
      "v_mov_b32 v125, s84\n"
      "v_max_u32_dpp v126, v126, v126 row_shr:1 row_mask:0xf bank_mask:0xf bound_ctrl:0\n"
      "s_nop 1\n"
      "v_max_u32_dpp v126, v126, v126 row_shr:2 row_mask:0xf bank_mask:0xf bound_ctrl:0\n"
      "s_nop 1\n"
      "v_max_u32_dpp v126, v126, v126 row_shr:4 row_mask:0xf bank_mask:0xf bound_ctrl:0\n"
      "s_nop 1\n"
      "v_max_u32_dpp v126, v126, v126 row_shr:8 row_mask:0xf bank_mask:0xf bound_ctrl:0\n"
      "s_nop 1\n"
      "v_max_u32_dpp v126, v126, v126 row_bcast:15 row_mask:0xa bank_mask:0xf bound_ctrl:0\n"
      "s_nop 0\n"
      "v_readlane_b32 s83, v126, 31\n"
      "s_nop 1\n"
      "v_cmp_eq_u32 vcc, s83, v120\n"
      "s_nop 1\n"
      "v_cndmask_b32 v124, v124, v125, vcc\n"
      "buffer_store_dwordx4 v[120:123], v124, s[48:51], s85 offen sc1\n"
      "s_nop 1\n"
      ".Lhjoin:\n"
      "s_lshr_b32 s80, s31, 1\n"
      "s_lshl_b32 s81, s80, 10\n"
      "v_lshl_add_u32 v16, v1, 5, s81\n"
      "v_and_b32 v16, 0x3fff, v16\n"
      "s_mov_b32 s93, 16384\n"
      "s_lshl_b32 s97, s80, 14\n"
      "s_sub_u32 s97, s97, 16384\n"
      "v_mov_b32 v18, 148288\n"
      "ds_read_b128 v[20:23], v18 offset:0\n"
      "s_waitcnt lgkmcnt(0)\n"
      "v_min_u32 v19, v20, v21\n"
      "v_min3_u32 v19, v19, v22, v23\n"
      "v_cmp_gt_u32 vcc, 7, v19\n"
      "s_nop 0\n"
      "s_cbranch_vccz .Lsk_ok\n"
      "s_nop 2\n"
      "s_mov_b32 s90, 0x100000\n"
      ".Lsk:\n"
      "s_sleep 2\n"
      "ds_read_b128 v[20:23], v18 offset:0\n"
      "s_waitcnt lgkmcnt(0)\n"
      "v_min_u32 v19, v20, v21\n"
      "v_min3_u32 v19, v19, v22, v23\n"
      "v_cmp_gt_u32 vcc, 7, v19\n"
      "s_sub_u32 s90, s90, 1\n"
      "s_nop 3\n"
      "s_cbranch_vccz .Lsk_ok\n"
      "s_cbranch_scc0 .Lsk\n"
      ".Lsk_ok:\n"
      "buffer_load_dword v17, v16, s[48:51], s93 offen sc1\n"
      "s_waitcnt vmcnt(0)\n"
      "v_xor_b32 v17, s36, v17\n"
      "v_cmp_ne_u32 vcc, 0, v17\n"
      "s_nop 0\n"
      "s_cbranch_vccz .Lpollkv_ok\n"
      "s_nop 2\n"
      "s_mov_b32 s90, 0x20000\n"
      ".Lpollkv:\n"
      "s_sleep 1\n"
      "buffer_load_dword v17, v16, s[48:51], s93 offen sc1\n"
      "s_waitcnt vmcnt(0)\n"
      "v_xor_b32 v17, s36, v17\n"
      "v_cmp_ne_u32 vcc, 0, v17\n"
      "s_sub_u32 s90, s90, 1\n"
      "s_nop 3\n"
      "s_cbranch_vccz .Lpollkv_ok\n"
      "s_cbranch_scc0 .Lpollkv\n"
      ".Lpollkv_ok:\n"
      "s_add_u32 s97, s97, 16384\n"
      "s_add_u32 m0, s4, 0\n"
      "s_and_b32 s97, s97, 0x3ffff\n"
      "s_add_u32 s91, s97, 65536\n"
      "buffer_load_dwordx4 v2, s[48:51], s91 offen offset:0 sc1 lds\n"
      "buffer_load_dwordx4 v2, s[48:51], s91 offen offset:1024 sc1 lds\n"
      "buffer_load_dwordx4 v2, s[48:51], s91 offen offset:2048 sc1 lds\n"
      "buffer_load_dwordx4 v2, s[48:51], s91 offen offset:3072 sc1 lds\n"
      "s_add_u32 s97, s97, 16384\n"
      "s_add_u32 m0, s4, 16384\n"
      "s_and_b32 s97, s97, 0x3ffff\n"
      "s_add_u32 s91, s97, 65536\n"
      "buffer_load_dwordx4 v2, s[48:51], s91 offen offset:0 sc1 lds\n"
      "buffer_load_dwordx4 v2, s[48:51], s91 offen offset:1024 sc1 lds\n"
      "buffer_load_dwordx4 v2, s[48:51], s91 offen offset:2048 sc1 lds\n"
      "buffer_load_dwordx4 v2, s[48:51], s91 offen offset:3072 sc1 lds\n"
      "s_lshl_b32 s80, s30, 12\n"
      "v_lshl_add_u32 v16, v1, 5, s80\n"
      "s_waitcnt vmcnt(4)\n"
      "s_barrier\n"
      "s_setprio 0\n"
      "buffer_load_dword v17, v16, s[48:51], s93 offen offset:0 sc1\n"
      "buffer_load_dword v18, v16, s[48:51], s93 offen offset:2048 sc1\n"
      "s_mov_b32 s90, 0\n"
      "s_add_u32 m0, s5, 0\n"
      "s_mov_b32 s92, 0\n"
      "buffer_load_dwordx4 v3, s[52:55], s92 offen lds\n"
      "s_add_u32 m0, s5, 1024\n"
      "s_mov_b32 s92, 32768\n"
      "buffer_load_dwordx4 v3, s[52:55], s92 offen lds\n"
      "s_add_u32 m0, s5, 2048\n"
      "s_mov_b32 s92, 131072\n"
      "buffer_load_dwordx4 v3, s[52:55], s92 offen lds\n"
      "s_add_u32 m0, s5, 3072\n"
      "s_mov_b32 s92, 163840\n"
      "buffer_load_dwordx4 v3, s[52:55], s92 offen lds\n"
      "s_add_u32 m0, s6, 0\n"
      "s_add_u32 s92, s95, 0\n"
      "buffer_load_dwordx4 v5, s[56:59], s92 offen lds\n"
      "s_add_u32 m0, s6, 8192\n"
      "s_add_u32 s92, s95, 0\n"
      "buffer_load_dwordx4 v5, s[60:63], s92 offen lds\n"
      "s_waitcnt vmcnt(6)\n"
      "v_xor_b32 v17, s36, v17\n"
      "v_xor_b32 v18, s36, v18\n"
      "v_or_b32 v17, v17, v18\n"
      "v_cmp_ne_u32 vcc, 0, v17\n"
      "s_nop 0\n"
      "s_cbranch_vccz .Lpolla_ok\n"
      "s_nop 2\n"
      "s_mov_b32 s90, 0x20000\n"
      ".Lpolla:\n"
      "s_sleep 1\n"
      "buffer_load_dword v17, v16, s[48:51], s93 offen offset:0 sc1\n"
      "buffer_load_dword v18, v16, s[48:51], s93 offen offset:2048 sc1\n"
      "s_waitcnt vmcnt(0)\n"
      "v_xor_b32 v17, s36, v17\n"
      "v_xor_b32 v18, s36, v18\n"
      "v_or_b32 v17, v17, v18\n"
      "v_cmp_ne_u32 vcc, 0, v17\n"
      "s_sub_u32 s90, s90, 1\n"
      "s_nop 3\n"
      "s_cbranch_vccz .Lpolla_ok\n"
      "s_cbranch_scc0 .Lpolla\n"
      ".Lpolla_ok:\n"
      "s_mov_b32 s93, 4096\n"
      "buffer_load_dwordx4 v[16:19], v9, s[48:51], s93 offen sc1\n"
      "s_waitcnt lgkmcnt(0)\n"
      "s_barrier\n"
      "s_add_u32 s97, s97, 16384\n"
      "s_add_u32 m0, s4, 32768\n"
      "s_and_b32 s97, s97, 0x3ffff\n"
      "s_add_u32 s91, s97, 65536\n"
      "buffer_load_dwordx4 v2, s[48:51], s91 offen offset:0 sc1 lds\n"
      "buffer_load_dwordx4 v2, s[48:51], s91 offen offset:1024 sc1 lds\n"
      "buffer_load_dwordx4 v2, s[48:51], s91 offen offset:2048 sc1 lds\n"
      "buffer_load_dwordx4 v2, s[48:51], s91 offen offset:3072 sc1 lds\n"
      "s_add_u32 s97, s97, 16384\n"
      "s_add_u32 m0, s4, 49152\n"
      "s_and_b32 s97, s97, 0x3ffff\n"
      "s_add_u32 s91, s97, 65536\n"
      "buffer_load_dwordx4 v2, s[48:51], s91 offen offset:0 sc1 lds\n"
      "buffer_load_dwordx4 v2, s[48:51], s91 offen offset:1024 sc1 lds\n"
      "buffer_load_dwordx4 v2, s[48:51], s91 offen offset:2048 sc1 lds\n"
      "buffer_load_dwordx4 v2, s[48:51], s91 offen offset:3072 sc1 lds\n"
      "s_add_u32 m0, s5, 4096\n"
      "s_mov_b32 s92, 196608\n"
      "buffer_load_dwordx4 v3, s[52:55], s92 offen lds\n"
      "s_add_u32 m0, s5, 5120\n"
      "s_mov_b32 s92, 262144\n"
      "buffer_load_dwordx4 v3, s[52:55], s92 offen lds\n"
      "s_add_u32 m0, s5, 6144\n"
      "s_mov_b32 s92, 294912\n"
      "buffer_load_dwordx4 v3, s[52:55], s92 offen lds\n"
      "s_add_u32 m0, s6, 1024\n"
      "s_add_u32 s92, s95, 57344\n"
      "buffer_load_dwordx4 v5, s[56:59], s92 offen lds\n"
      "s_add_u32 m0, s6, 9216\n"
      "s_add_u32 s92, s95, 57344\n"
      "buffer_load_dwordx4 v5, s[60:63], s92 offen lds\n"
      "s_add_u32 m0, s6, 2048\n"
      "s_add_u32 s92, s95, 114688\n"
      "buffer_load_dwordx4 v5, s[56:59], s92 offen lds\n"
      "s_waitcnt vmcnt(14)\n"
      "s_and_b32 s80, s36, 0xff\n"
      "v_xor_b32 v20, s36, v17\n"
      "v_xor_b32 v21, s80, v18\n"
      "v_xor_b32 v22, s80, v19\n"
      "v_or_b32 v21, v21, v22\n"
      "v_and_b32 v21, 0xff, v21\n"
      "v_or_b32 v20, v20, v21\n"
      "v_cmp_ne_u32 vcc, 0, v20\n"
      "s_nop 0\n"
      "s_cbranch_vccz .Lpollr_ok\n"
      "s_nop 2\n"
      "s_mov_b32 s90, 0x20000\n"
      ".Lpollr:\n"
      "s_sleep 1\n"
      "buffer_load_dwordx4 v[16:19], v9, s[48:51], s93 offen sc1\n"
      "s_waitcnt vmcnt(0)\n"
      "s_and_b32 s80, s36, 0xff\n"
      "v_xor_b32 v20, s36, v17\n"
      "v_xor_b32 v21, s80, v18\n"
      "v_xor_b32 v22, s80, v19\n"
      "v_or_b32 v21, v21, v22\n"
      "v_and_b32 v21, 0xff, v21\n"
      "v_or_b32 v20, v20, v21\n"
      "v_cmp_ne_u32 vcc, 0, v20\n"
      "s_sub_u32 s90, s90, 1\n"
      "s_nop 3\n"
      "s_cbranch_vccz .Lpollr_ok\n"
      "s_cbranch_scc0 .Lpollr\n"
      ".Lpollr_ok:\n"
      "v_mov_b32 v20, v16\n"
      "v_and_b32 v19, 0xffff0000, v19\n"
      "v_and_b32 v17, 0xffff0000, v18\n"
      "v_rcp_f32 v18, v19\n"
      "v_mov_b32 v22, 0\n"
      "v_lshlrev_b32 v21, 2, v10\n"
      "v_add_u32 v21, 160768, v21\n"
      "ds_write_b32 v21, v22\n"
      "v_max_u32_dpp v20, v20, v20 row_shr:1 row_mask:0xf bank_mask:0xf bound_ctrl:0\n"
      "s_nop 1\n"
      "v_max_u32_dpp v20, v20, v20 row_shr:2 row_mask:0xf bank_mask:0xf bound_ctrl:0\n"
      "s_nop 1\n"
      "v_max_u32_dpp v20, v20, v20 row_shr:4 row_mask:0xf bank_mask:0xf bound_ctrl:0\n"
      "s_nop 1\n"
      "v_max_u32_dpp v20, v20, v20 row_shr:8 row_mask:0xf bank_mask:0xf bound_ctrl:0\n"
      "s_nop 1\n"
      "v_max_u32_dpp v20, v20, v20 row_bcast:15 row_mask:0xa bank_mask:0xf bound_ctrl:0\n"
      "s_nop 1\n"
      "v_max_u32_dpp v20, v20, v20 row_bcast:31 row_mask:0xc bank_mask:0xf bound_ctrl:0\n"
      "s_nop 0\n"
      "v_readlane_b32 s85, v20, 63\n"
      "s_and_b32 s82, s7, 3\n"
      "s_lshl_b32 s82, s82, 9\n"
      "s_lshl_b32 s81, s30, 7\n"
      "v_cmp_eq_u32 vcc, s85, v16\n"
      "s_and_b32 s37, s85, 0xffff\n"
      "s_sub_u32 s37, 0xffff, s37\n"
      "s_and_b32 s37, s37, 2047\n"
      "s_ff1_i32_b64 s84, vcc\n"
      "s_lshl_b32 s80, s37, 11\n"
      "s_add_u32 s80, s80, s81\n"
      "s_add_u32 s80, s80, s82\n"
      "v_lshl_add_u32 v21, v10, 2, s80\n"
      "v_readlane_b32 s86, v17, s84\n"
      "v_readlane_b32 s87, v18, s84\n"
      "global_load_dword v16, v21, s[8:9]\n"
      "ds_read_b64_tr_b16 a[0:1], v4 offset:0\n"
      "ds_read_b64_tr_b16 a[2:3], v4 offset:512\n"
      "ds_read_b64_tr_b16 a[4:5], v4 offset:1024\n"
      "ds_read_b64_tr_b16 a[6:7], v4 offset:1536\n"
      "ds_read_b64_tr_b16 a[8:9], v4 offset:2048\n"
      "ds_read_b64_tr_b16 a[10:11], v4 offset:2560\n"
      "ds_read_b64_tr_b16 a[12:13], v4 offset:3072\n"
      "ds_read_b64_tr_b16 a[14:15], v4 offset:3584\n"
      "s_waitcnt vmcnt(11) lgkmcnt(0)\n"
      "s_barrier\n"
      "s_add_u32 s97, s97, 16384\n"
      "s_add_u32 m0, s4, 0\n"
      "s_and_b32 s97, s97, 0x3ffff\n"
      "s_add_u32 s91, s97, 65536\n"
      "buffer_load_dwordx4 v2, s[48:51], s91 offen offset:0 sc1 lds\n"
      "buffer_load_dwordx4 v2, s[48:51], s91 offen offset:1024 sc1 lds\n"
      "buffer_load_dwordx4 v2, s[48:51], s91 offen offset:2048 sc1 lds\n"
      "buffer_load_dwordx4 v2, s[48:51], s91 offen offset:3072 sc1 lds\n"
      "s_add_u32 m0, s5, 0\n"
      "s_mov_b32 s92, 327680\n"
      "buffer_load_dwordx4 v3, s[52:55], s92 offen lds\n"
      "s_add_u32 m0, s5, 1024\n"
      "s_mov_b32 s92, 393216\n"
      "buffer_load_dwordx4 v3, s[52:55], s92 offen lds\n"
      "s_add_u32 m0, s5, 2048\n"
      "s_mov_b32 s92, 425984\n"
      "buffer_load_dwordx4 v3, s[52:55], s92 offen lds\n"
      "s_add_u32 m0, s5, 3072\n"
      "s_mov_b32 s92, 458752\n"
      "buffer_load_dwordx4 v3, s[52:55], s92 offen lds\n"
      "s_add_u32 m0, s6, 10240\n"
      "s_add_u32 s92, s95, 114688\n"
      "buffer_load_dwordx4 v5, s[60:63], s92 offen lds\n"
      "s_add_u32 m0, s6, 3072\n"
      "s_add_u32 s92, s95, 172032\n"
      "buffer_load_dwordx4 v5, s[56:59], s92 offen lds\n"
      "s_add_u32 m0, s6, 11264\n"
      "s_add_u32 s92, s95, 172032\n"
      "buffer_load_dwordx4 v5, s[60:63], s92 offen lds\n"
      "s_lshr_b32 s80, s85, 16\n"
      "s_bitcmp1_b32 s80, 15\n"
      "s_cselect_b32 s81, 0x8000, -1\n"
      "s_xor_b32 s80, s80, s81\n"
      "s_lshl_b32 s80, s80, 16\n"
      "v_mov_b32 v20, s80\n"
      "v_mul_f32 v20, 0xbfb8aa3b, v20\n"
      "v_exp_f32 v20, v20\n"
      "s_nop 0\n"
      "v_add_f32 v20, 1.0, v20\n"
      "v_rcp_f32 v20, v20\n"
      "s_nop 0\n"
      "v_cvt_pk_bf16_f32 v20, v20, v20\n"
      "v_lshlrev_b32 v20, 16, v20\n"
      "s_nop 0\n"
      "v_readfirstlane_b32 s38, v20\n"
      "v_mov_b32 v20, 163264\n"
      "v_mov_b32 v21, s37\n"
      "ds_write_b32 v20, v21\n"
      "s_waitcnt vmcnt(15)\n"
      "ds_read_b64_tr_b16 a[16:17], v4 offset:4096\n"
      "ds_read_b64_tr_b16 a[18:19], v4 offset:4608\n"
      "ds_read_b64_tr_b16 a[20:21], v4 offset:5120\n"
      "ds_read_b64_tr_b16 a[22:23], v4 offset:5632\n"
      "ds_read_b64_tr_b16 a[24:25], v4 offset:6144\n"
      "ds_read_b64_tr_b16 a[26:27], v4 offset:6656\n"
      "s_waitcnt lgkmcnt(0)\n"
      "s_barrier\n"
      "s_add_u32 s97, s97, 16384\n"
      "s_add_u32 m0, s4, 16384\n"
      "s_and_b32 s97, s97, 0x3ffff\n"
      "s_add_u32 s91, s97, 65536\n"
      "buffer_load_dwordx4 v2, s[48:51], s91 offen offset:0 sc1 lds\n"
      "buffer_load_dwordx4 v2, s[48:51], s91 offen offset:1024 sc1 lds\n"
      "buffer_load_dwordx4 v2, s[48:51], s91 offen offset:2048 sc1 lds\n"
      "buffer_load_dwordx4 v2, s[48:51], s91 offen offset:3072 sc1 lds\n"
      "s_add_u32 m0, s5, 4096\n"
      "s_mov_b32 s92, 524288\n"
      "buffer_load_dwordx4 v3, s[52:55], s92 offen lds\n"
      "s_add_u32 m0, s5, 5120\n"
      "s_mov_b32 s92, 557056\n"
      "buffer_load_dwordx4 v3, s[52:55], s92 offen lds\n"
      "s_add_u32 m0, s5, 6144\n"
      "s_mov_b32 s92, 655360\n"
      "buffer_load_dwordx4 v3, s[52:55], s92 offen lds\n"
      "s_add_u32 m0, s6, 4096\n"
      "s_add_u32 s92, s95, 229376\n"
      "buffer_load_dwordx4 v5, s[56:59], s92 offen lds\n"
      "s_add_u32 m0, s6, 12288\n"
      "s_add_u32 s92, s95, 229376\n"
      "buffer_load_dwordx4 v5, s[60:63], s92 offen lds\n"
      "s_waitcnt vmcnt(20)\n"
      "v_lshlrev_b32 v20, 16, v16\n"
      "v_and_b32 v21, 0xffff0000, v16\n"
      "v_subrev_f32 v20, s86, v20\n"
      "v_subrev_f32 v21, s86, v21\n"
      "v_cvt_pk_bf16_f32 v22, v20, v21\n"
      "v_lshlrev_b32 v20, 16, v22\n"
      "v_and_b32 v21, 0xffff0000, v22\n"
      "v_mul_f32 v20, s87, v20\n"
      "v_mul_f32 v21, s87, v21\n"
      "v_cvt_pk_bf16_f32 v22, v20, v21\n"
      "s_lshl_b32 s80, s30, 7\n"
      "s_add_u32 s80, s80, 159744\n"
      "v_lshl_add_u32 v20, v10, 2, s80\n"
      "ds_write_b32 v20, v22\n"
      "s_waitcnt vmcnt(12)\n"
      "ds_read_b64_tr_b16 a[28:29], v4 offset:0\n"
      "ds_read_b64_tr_b16 a[30:31], v4 offset:512\n"
      "ds_read_b64_tr_b16 a[32:33], v4 offset:1024\n"
      "ds_read_b64_tr_b16 a[34:35], v4 offset:1536\n"
      "ds_read_b64_tr_b16 a[36:37], v4 offset:2048\n"
      "ds_read_b64_tr_b16 a[38:39], v4 offset:2560\n"
      "ds_read_b64_tr_b16 a[40:41], v4 offset:3072\n"
      "ds_read_b64_tr_b16 a[42:43], v4 offset:3584\n"
      "s_waitcnt lgkmcnt(0)\n"
      "s_barrier\n"
      "s_add_u32 s97, s97, 16384\n"
      "s_add_u32 m0, s4, 32768\n"
      "s_and_b32 s97, s97, 0x3ffff\n"
      "s_add_u32 s91, s97, 65536\n"
      "buffer_load_dwordx4 v2, s[48:51], s91 offen offset:0 sc1 lds\n"
      "buffer_load_dwordx4 v2, s[48:51], s91 offen offset:1024 sc1 lds\n"
      "buffer_load_dwordx4 v2, s[48:51], s91 offen offset:2048 sc1 lds\n"
      "buffer_load_dwordx4 v2, s[48:51], s91 offen offset:3072 sc1 lds\n"
      "s_add_u32 m0, s5, 0\n"
      "s_mov_b32 s92, 688128\n"
      "buffer_load_dwordx4 v3, s[52:55], s92 offen lds\n"
      "s_add_u32 m0, s5, 1024\n"
      "s_mov_b32 s92, 720896\n"
      "buffer_load_dwordx4 v3, s[52:55], s92 offen lds\n"
      "s_add_u32 m0, s5, 2048\n"
      "s_mov_b32 s92, 786432\n"
      "buffer_load_dwordx4 v3, s[52:55], s92 offen lds\n"
      "s_add_u32 m0, s5, 3072\n"
      "s_mov_b32 s92, 819200\n"
      "buffer_load_dwordx4 v3, s[52:55], s92 offen lds\n"
      "s_add_u32 m0, s6, 5120\n"
      "s_add_u32 s92, s95, 286720\n"
      "buffer_load_dwordx4 v5, s[56:59], s92 offen lds\n"
      "s_add_u32 m0, s6, 13312\n"
      "s_add_u32 s92, s95, 286720\n"
      "buffer_load_dwordx4 v5, s[60:63], s92 offen lds\n"
      "s_add_u32 m0, s6, 6144\n"
      "s_add_u32 s92, s95, 344064\n"
      "buffer_load_dwordx4 v5, s[56:59], s92 offen lds\n"
      "s_lshl_b32 s80, s30, 7\n"
      "s_add_u32 s80, s80, 159744\n"
      "v_lshrrev_b32 v20, 4, v1\n"
      "v_lshl_add_u32 v20, v20, 4, s80\n"
      "v_mov_b32 v21, 160768\n"
      "v_bfe_u32 v22, v1, 2, 2\n"
      "v_cmp_eq_u32 vcc, 0, v22\n"
      "s_nop 1\n"
      "v_cndmask_b32 v12, v21, v20, vcc\n"
      "v_cmp_eq_u32 vcc, 1, v22\n"
      "s_nop 1\n"
      "v_cndmask_b32 v13, v21, v20, vcc\n"
      "v_cmp_eq_u32 vcc, 2, v22\n"
      "s_nop 1\n"
      "v_cndmask_b32 v14, v21, v20, vcc\n"
      "v_cmp_eq_u32 vcc, 3, v22\n"
      "s_nop 1\n"
      "v_cndmask_b32 v15, v21, v20, vcc\n"
      "s_waitcnt vmcnt(13)\n"
      "ds_read_b64_tr_b16 a[44:45], v4 offset:4096\n"
      "ds_read_b64_tr_b16 a[46:47], v4 offset:4608\n"
      "ds_read_b64_tr_b16 a[48:49], v4 offset:5120\n"
      "ds_read_b64_tr_b16 a[50:51], v4 offset:5632\n"
      "ds_read_b64_tr_b16 a[52:53], v4 offset:6144\n"
      "ds_read_b64_tr_b16 a[54:55], v4 offset:6656\n"
      "s_waitcnt lgkmcnt(0)\n"
      "s_barrier\n"
      "s_add_u32 s97, s97, 16384\n"
      "s_add_u32 m0, s4, 49152\n"
      "s_and_b32 s97, s97, 0x3ffff\n"
      "s_add_u32 s91, s97, 65536\n"
      "buffer_load_dwordx4 v2, s[48:51], s91 offen offset:0 sc1 lds\n"
      "buffer_load_dwordx4 v2, s[48:51], s91 offen offset:1024 sc1 lds\n"
      "buffer_load_dwordx4 v2, s[48:51], s91 offen offset:2048 sc1 lds\n"
      "buffer_load_dwordx4 v2, s[48:51], s91 offen offset:3072 sc1 lds\n"
      "s_add_u32 m0, s5, 4096\n"
      "s_mov_b32 s92, 851968\n"
      "buffer_load_dwordx4 v3, s[52:55], s92 offen lds\n"
      "s_add_u32 m0, s5, 5120\n"
      "s_mov_b32 s92, 917504\n"
      "buffer_load_dwordx4 v3, s[52:55], s92 offen lds\n"
      "s_add_u32 m0, s5, 6144\n"
      "s_mov_b32 s92, 950272\n"
      "buffer_load_dwordx4 v3, s[52:55], s92 offen lds\n"
      "s_add_u32 m0, s6, 14336\n"
      "s_add_u32 s92, s95, 344064\n"
      "buffer_load_dwordx4 v5, s[60:63], s92 offen lds\n"
      "s_add_u32 m0, s6, 7168\n"
      "s_add_u32 s92, s95, 401408\n"
      "buffer_load_dwordx4 v5, s[56:59], s92 offen lds\n"
      "ds_read_b64_tr_b16 v[16:17], v6 offset:0\n"
      "ds_read_b64_tr_b16 v[18:19], v6 offset:512\n"
      "ds_read_b64_tr_b16 v[20:21], v6 offset:8192\n"
      "ds_read_b64_tr_b16 v[22:23], v6 offset:8704\n"
      "ds_read_b128 v[24:27], v12 offset:0\n"
      "ds_read_b64_tr_b16 v[28:29], v6 offset:32\n"
      "ds_read_b64_tr_b16 v[30:31], v6 offset:544\n"
      "ds_read_b64_tr_b16 v[32:33], v6 offset:8224\n"
      "ds_read_b64_tr_b16 v[34:35], v6 offset:8736\n"
      "ds_read_b128 v[36:39], v13 offset:0\n"
      "ds_read_b64_tr_b16 v[40:41], v6 offset:64\n"
      "ds_read_b64_tr_b16 v[42:43], v6 offset:576\n"
      "ds_read_b64_tr_b16 v[44:45], v6 offset:8256\n"
      "ds_read_b64_tr_b16 v[46:47], v6 offset:8768\n"
      "ds_read_b128 v[48:51], v14 offset:0\n"
      "ds_read_b64_tr_b16 v[52:53], v6 offset:96\n"
      "ds_read_b64_tr_b16 v[54:55], v6 offset:608\n"
      "ds_read_b64_tr_b16 v[56:57], v6 offset:8288\n"
      "ds_read_b64_tr_b16 v[58:59], v6 offset:8800\n"
      "ds_read_b128 v[60:63], v15 offset:0\n"
      "s_waitcnt lgkmcnt(15)\n"
      "v_mfma_f32_16x16x32_bf16 v[64:67], v[24:27], v[16:19], 0\n"
      "v_mfma_f32_16x16x32_bf16 v[68:71], v[24:27], v[20:23], 0\n"
      "s_waitcnt lgkmcnt(10)\n"
      "v_mfma_f32_16x16x32_bf16 v[64:67], v[36:39], v[28:31], v[64:67]\n"
      "v_mfma_f32_16x16x32_bf16 v[68:71], v[36:39], v[32:35], v[68:71]\n"
      "s_waitcnt lgkmcnt(5)\n"
      "v_mfma_f32_16x16x32_bf16 v[64:67], v[48:51], v[40:43], v[64:67]\n"
      "v_mfma_f32_16x16x32_bf16 v[68:71], v[48:51], v[44:47], v[68:71]\n"
      "s_waitcnt lgkmcnt(0)\n"
      "v_mfma_f32_16x16x32_bf16 v[64:67], v[60:63], v[52:55], v[64:67]\n"
      "v_mfma_f32_16x16x32_bf16 v[68:71], v[60:63], v[56:59], v[68:71]\n"
      "s_waitcnt vmcnt(12)\n"
      "ds_read_b64_tr_b16 a[56:57], v4 offset:0\n"
      "ds_read_b64_tr_b16 a[58:59], v4 offset:512\n"
      "ds_read_b64_tr_b16 a[60:61], v4 offset:1024\n"
      "ds_read_b64_tr_b16 a[62:63], v4 offset:1536\n"
      "ds_read_b64_tr_b16 a[64:65], v4 offset:2048\n"
      "ds_read_b64_tr_b16 a[66:67], v4 offset:2560\n"
      "ds_read_b64_tr_b16 a[68:69], v4 offset:3072\n"
      "ds_read_b64_tr_b16 a[70:71], v4 offset:3584\n"
      "s_waitcnt lgkmcnt(0)\n"
      "s_barrier\n"
      "s_add_u32 s97, s97, 16384\n"
      "s_add_u32 m0, s4, 0\n"
      "s_and_b32 s97, s97, 0x3ffff\n"
      "s_add_u32 s91, s97, 65536\n"
      "buffer_load_dwordx4 v2, s[48:51], s91 offen offset:0 sc1 lds\n"
      "buffer_load_dwordx4 v2, s[48:51], s91 offen offset:1024 sc1 lds\n"
      "buffer_load_dwordx4 v2, s[48:51], s91 offen offset:2048 sc1 lds\n"
      "buffer_load_dwordx4 v2, s[48:51], s91 offen offset:3072 sc1 lds\n"
      "s_add_u32 m0, s5, 0\n"
      "s_mov_b32 s92, 983040\n"
      "buffer_load_dwordx4 v3, s[52:55], s92 offen lds\n"
      "s_add_u32 m0, s5, 1024\n"
      "s_mov_b32 s92, 1048576\n"
      "buffer_load_dwordx4 v3, s[52:55], s92 offen lds\n"
      "s_add_u32 m0, s5, 2048\n"
      "s_mov_b32 s92, 1081344\n"
      "buffer_load_dwordx4 v3, s[52:55], s92 offen lds\n"
      "s_add_u32 m0, s5, 3072\n"
      "s_mov_b32 s92, 1114112\n"
      "buffer_load_dwordx4 v3, s[52:55], s92 offen lds\n"
      "s_add_u32 m0, s6, 15360\n"
      "s_add_u32 s92, s95, 401408\n"
      "buffer_load_dwordx4 v5, s[60:63], s92 offen lds\n"
      "s_add_u32 m0, s6, 0\n"
      "s_add_u32 s92, s96, 0\n"
      "buffer_load_dwordx4 v8, s[64:67], s92 offen lds\n"
      "s_waitcnt vmcnt(12)\n"
      "ds_read_b64_tr_b16 a[72:73], v4 offset:4096\n"
      "ds_read_b64_tr_b16 a[74:75], v4 offset:4608\n"
      "ds_read_b64_tr_b16 a[76:77], v4 offset:5120\n"
      "ds_read_b64_tr_b16 a[78:79], v4 offset:5632\n"
      "ds_read_b64_tr_b16 a[80:81], v4 offset:6144\n"
      "ds_read_b64_tr_b16 a[82:83], v4 offset:6656\n"
      "s_waitcnt lgkmcnt(0)\n"
      "s_barrier\n"
      "s_add_u32 s97, s97, 16384\n"
      "s_add_u32 m0, s4, 16384\n"
      "s_and_b32 s97, s97, 0x3ffff\n"
      "s_add_u32 s91, s97, 65536\n"
      "buffer_load_dwordx4 v2, s[48:51], s91 offen offset:0 sc1 lds\n"
      "buffer_load_dwordx4 v2, s[48:51], s91 offen offset:1024 sc1 lds\n"
      "buffer_load_dwordx4 v2, s[48:51], s91 offen offset:2048 sc1 lds\n"
      "buffer_load_dwordx4 v2, s[48:51], s91 offen offset:3072 sc1 lds\n"
      "s_add_u32 m0, s5, 4096\n"
      "s_mov_b32 s92, 1179648\n"
      "buffer_load_dwordx4 v3, s[52:55], s92 offen lds\n"
      "s_add_u32 m0, s5, 5120\n"
      "s_mov_b32 s92, 1212416\n"
      "buffer_load_dwordx4 v3, s[52:55], s92 offen lds\n"
      "s_add_u32 m0, s5, 6144\n"
      "s_mov_b32 s92, 1245184\n"
      "buffer_load_dwordx4 v3, s[52:55], s92 offen lds\n"
      "s_add_u32 m0, s6, 1024\n"
      "s_add_u32 s92, s96, 16384\n"
      "buffer_load_dwordx4 v8, s[64:67], s92 offen lds\n"
      "s_add_u32 m0, s6, 2048\n"
      "s_add_u32 s92, s96, 32768\n"
      "buffer_load_dwordx4 v8, s[64:67], s92 offen lds\n"
      "s_add_u32 m0, s6, 3072\n"
      "s_add_u32 s92, s96, 49152\n"
      "buffer_load_dwordx4 v8, s[64:67], s92 offen lds\n"
      "s_waitcnt vmcnt(12)\n"
      "ds_read_b64_tr_b16 a[84:85], v4 offset:0\n"
      "ds_read_b64_tr_b16 a[86:87], v4 offset:512\n"
      "ds_read_b64_tr_b16 a[88:89], v4 offset:1024\n"
      "ds_read_b64_tr_b16 a[90:91], v4 offset:1536\n"
      "ds_read_b64_tr_b16 a[92:93], v4 offset:2048\n"
      "ds_read_b64_tr_b16 a[94:95], v4 offset:2560\n"
      "ds_read_b64_tr_b16 v[72:73], v4 offset:3072\n"
      "ds_read_b64_tr_b16 v[74:75], v4 offset:3584\n"
      "s_waitcnt lgkmcnt(0)\n"
      "s_barrier\n"
      "s_add_u32 s97, s97, 16384\n"
      "s_add_u32 m0, s4, 32768\n"
      "s_and_b32 s97, s97, 0x3ffff\n"
      "s_add_u32 s91, s97, 65536\n"
      "buffer_load_dwordx4 v2, s[48:51], s91 offen offset:0 sc1 lds\n"
      "buffer_load_dwordx4 v2, s[48:51], s91 offen offset:1024 sc1 lds\n"
      "buffer_load_dwordx4 v2, s[48:51], s91 offen offset:2048 sc1 lds\n"
      "buffer_load_dwordx4 v2, s[48:51], s91 offen offset:3072 sc1 lds\n"
      "s_add_u32 m0, s5, 0\n"
      "s_mov_b32 s92, 1310720\n"
      "buffer_load_dwordx4 v3, s[52:55], s92 offen lds\n"
      "s_add_u32 m0, s5, 1024\n"
      "s_mov_b32 s92, 1343488\n"
      "buffer_load_dwordx4 v3, s[52:55], s92 offen lds\n"
      "s_add_u32 m0, s5, 2048\n"
      "s_mov_b32 s92, 1376256\n"
      "buffer_load_dwordx4 v3, s[52:55], s92 offen lds\n"
      "s_add_u32 m0, s5, 3072\n"
      "s_mov_b32 s92, 1441792\n"
      "buffer_load_dwordx4 v3, s[52:55], s92 offen lds\n"
      "s_add_u32 m0, s6, 8192\n"
      "s_add_u32 s92, s96, 65536\n"
      "buffer_load_dwordx4 v8, s[64:67], s92 offen lds\n"
      "s_add_u32 m0, s6, 9216\n"
      "s_add_u32 s92, s96, 81920\n"
      "buffer_load_dwordx4 v8, s[64:67], s92 offen lds\n"
      "s_waitcnt vmcnt(21)\n"
      "ds_read_b64_tr_b16 v[16:17], v6 offset:4096\n"
      "ds_read_b64_tr_b16 v[18:19], v6 offset:4608\n"
      "ds_read_b64_tr_b16 v[20:21], v6 offset:12288\n"
      "ds_read_b64_tr_b16 v[22:23], v6 offset:12800\n"
      "ds_read_b128 v[24:27], v12 offset:64\n"
      "ds_read_b64_tr_b16 v[28:29], v6 offset:4128\n"
      "ds_read_b64_tr_b16 v[30:31], v6 offset:4640\n"
      "ds_read_b64_tr_b16 v[32:33], v6 offset:12320\n"
      "ds_read_b64_tr_b16 v[34:35], v6 offset:12832\n"
      "ds_read_b128 v[36:39], v13 offset:64\n"
      "ds_read_b64_tr_b16 v[40:41], v6 offset:4160\n"
      "ds_read_b64_tr_b16 v[42:43], v6 offset:4672\n"
      "ds_read_b64_tr_b16 v[44:45], v6 offset:12352\n"
      "ds_read_b64_tr_b16 v[46:47], v6 offset:12864\n"
      "ds_read_b128 v[48:51], v14 offset:64\n"
      "ds_read_b64_tr_b16 v[52:53], v6 offset:4192\n"
      "ds_read_b64_tr_b16 v[54:55], v6 offset:4704\n"
      "ds_read_b64_tr_b16 v[56:57], v6 offset:12384\n"
      "ds_read_b64_tr_b16 v[58:59], v6 offset:12896\n"
      "ds_read_b128 v[60:63], v15 offset:64\n"
      "s_waitcnt lgkmcnt(15)\n"
      "v_mfma_f32_16x16x32_bf16 v[64:67], v[24:27], v[16:19], v[64:67]\n"
      "v_mfma_f32_16x16x32_bf16 v[68:71], v[24:27], v[20:23], v[68:71]\n"
      "s_waitcnt lgkmcnt(10)\n"
      "v_mfma_f32_16x16x32_bf16 v[64:67], v[36:39], v[28:31], v[64:67]\n"
      "v_mfma_f32_16x16x32_bf16 v[68:71], v[36:39], v[32:35], v[68:71]\n"
      "s_waitcnt lgkmcnt(5)\n"
      "v_mfma_f32_16x16x32_bf16 v[64:67], v[48:51], v[40:43], v[64:67]\n"
      "v_mfma_f32_16x16x32_bf16 v[68:71], v[48:51], v[44:47], v[68:71]\n"
      "s_waitcnt lgkmcnt(0)\n"
      "v_mfma_f32_16x16x32_bf16 v[64:67], v[60:63], v[52:55], v[64:67]\n"
      "v_mfma_f32_16x16x32_bf16 v[68:71], v[60:63], v[56:59], v[68:71]\n"
      "s_lshl_b32 s80, s30, 9\n"
      "s_add_u32 s80, s80, 160896\n"
      "v_lshl_add_u32 v16, v1, 2, s80\n"
      "s_nop 3\n"
      "ds_write_b32 v16, v64\n"
      "ds_write_b32 v16, v68 offset:256\n"
      "s_waitcnt vmcnt(13)\n"
      "ds_read_b64_tr_b16 v[76:77], v4 offset:4096\n"
      "ds_read_b64_tr_b16 v[78:79], v4 offset:4608\n"
      "ds_read_b64_tr_b16 v[80:81], v4 offset:5120\n"
      "ds_read_b64_tr_b16 v[82:83], v4 offset:5632\n"
      "ds_read_b64_tr_b16 v[84:85], v4 offset:6144\n"
      "ds_read_b64_tr_b16 v[86:87], v4 offset:6656\n"
      "s_waitcnt lgkmcnt(0)\n"
      "s_barrier\n"
      "s_add_u32 s97, s97, 16384\n"
      "s_add_u32 m0, s4, 49152\n"
      "s_and_b32 s97, s97, 0x3ffff\n"
      "s_add_u32 s91, s97, 65536\n"
      "buffer_load_dwordx4 v2, s[48:51], s91 offen offset:0 sc1 lds\n"
      "buffer_load_dwordx4 v2, s[48:51], s91 offen offset:1024 sc1 lds\n"
      "buffer_load_dwordx4 v2, s[48:51], s91 offen offset:2048 sc1 lds\n"
      "buffer_load_dwordx4 v2, s[48:51], s91 offen offset:3072 sc1 lds\n"
      "v_lshlrev_b32 v16, 2, v1\n"
      "v_add_u32 v16, 160896, v16\n"
      "ds_read_b32 v20, v16 offset:0\n"
      "ds_read_b32 v21, v16 offset:256\n"
      "ds_read_b32 v22, v16 offset:512\n"
      "ds_read_b32 v23, v16 offset:768\n"
      "ds_read_b32 v24, v16 offset:1024\n"
      "ds_read_b32 v25, v16 offset:1280\n"
      "ds_read_b32 v26, v16 offset:1536\n"
      "ds_read_b32 v27, v16 offset:1792\n"
      "s_lshl_b32 s80, s7, 10\n"
      "s_lshl_b32 s81, s30, 29\n"
      "s_or_b32 s80, s80, s81\n"
      "v_lshl_add_u32 v17, v1, 4, s80\n"
      "s_mov_b32 s93, 1310720\n"
      "v_mov_b32 v29, s36\n"
      "v_mov_b32 v31, s36\n"
      "s_waitcnt lgkmcnt(0)\n"
      "v_add_f32 v20, v20, v22\n"
      "v_add_f32 v24, v24, v26\n"
      "v_add_f32 v21, v21, v23\n"
      "v_add_f32 v25, v25, v27\n"
      "v_add_f32 v28, v20, v24\n"
      "v_add_f32 v30, v21, v25\n"
      "buffer_store_dwordx4 v[28:31], v17, s[48:51], s93 offen sc1\n"
      "s_add_u32 m0, s5, 4096\n"
      "s_mov_b32 s92, 1474560\n"
      "buffer_load_dwordx4 v3, s[52:55], s92 offen lds\n"
      "s_add_u32 m0, s5, 5120\n"
      "s_mov_b32 s92, 1507328\n"
      "buffer_load_dwordx4 v3, s[52:55], s92 offen lds\n"
      "s_add_u32 m0, s5, 6144\n"
      "s_mov_b32 s92, 1572864\n"
      "buffer_load_dwordx4 v3, s[52:55], s92 offen lds\n"
      "s_add_u32 m0, s6, 10240\n"
      "s_add_u32 s92, s96, 98304\n"
      "buffer_load_dwordx4 v8, s[64:67], s92 offen lds\n"
      "s_add_u32 m0, s6, 11264\n"
      "s_add_u32 s92, s96, 114688\n"
      "buffer_load_dwordx4 v8, s[64:67], s92 offen lds\n"
      "s_waitcnt vmcnt(12)\n"
      "ds_read_b64_tr_b16 v[88:89], v4 offset:0\n"
      "ds_read_b64_tr_b16 v[90:91], v4 offset:512\n"
      "ds_read_b64_tr_b16 v[92:93], v4 offset:1024\n"
      "ds_read_b64_tr_b16 v[94:95], v4 offset:1536\n"
      "ds_read_b64_tr_b16 v[96:97], v4 offset:2048\n"
      "ds_read_b64_tr_b16 v[98:99], v4 offset:2560\n"
      "ds_read_b64_tr_b16 v[100:101], v4 offset:3072\n"
      "ds_read_b64_tr_b16 v[102:103], v4 offset:3584\n"
      "s_waitcnt lgkmcnt(0)\n"
      "s_barrier\n"
      "s_add_u32 s97, s97, 16384\n"
      "s_add_u32 m0, s4, 0\n"
      "s_and_b32 s97, s97, 0x3ffff\n"
      "s_add_u32 s91, s97, 65536\n"
      "buffer_load_dwordx4 v2, s[48:51], s91 offen offset:0 sc1 lds\n"
      "buffer_load_dwordx4 v2, s[48:51], s91 offen offset:1024 sc1 lds\n"
      "buffer_load_dwordx4 v2, s[48:51], s91 offen offset:2048 sc1 lds\n"
      "buffer_load_dwordx4 v2, s[48:51], s91 offen offset:3072 sc1 lds\n"
      "s_and_b32 s80, s7, 0xfc\n"
      "s_lshl_b32 s80, s80, 10\n"
      "s_add_u32 s93, s80, 1310720\n"
      "buffer_load_dwordx4 v[16:19], v9, s[48:51], s93 offen offset:0 sc1\n"
      "buffer_load_dwordx4 v[20:23], v9, s[48:51], s93 offen offset:1024 sc1\n"
      "buffer_load_dwordx4 v[24:27], v9, s[48:51], s93 offen offset:2048 sc1\n"
      "buffer_load_dwordx4 v[28:31], v9, s[48:51], s93 offen offset:3072 sc1\n"
      "s_add_u32 m0, s5, 0\n"
      "s_mov_b32 s92, 1605632\n"
      "buffer_load_dwordx4 v3, s[52:55], s92 offen lds\n"
      "s_add_u32 m0, s5, 1024\n"
      "s_mov_b32 s92, 1638400\n"
      "buffer_load_dwordx4 v3, s[52:55], s92 offen lds\n"
      "s_add_u32 m0, s5, 2048\n"
      "s_mov_b32 s92, 1703936\n"
      "buffer_load_dwordx4 v3, s[52:55], s92 offen lds\n"
      "s_add_u32 m0, s5, 3072\n"
      "s_mov_b32 s92, 1736704\n"
      "buffer_load_dwordx4 v3, s[52:55], s92 offen lds\n"
      "s_waitcnt vmcnt(14)\n"
      "ds_read_b64_tr_b16 v[104:105], v4 offset:4096\n"
      "ds_read_b64_tr_b16 v[106:107], v4 offset:4608\n"
      "ds_read_b64_tr_b16 v[108:109], v4 offset:5120\n"
      "ds_read_b64_tr_b16 v[110:111], v4 offset:5632\n"
      "ds_read_b64_tr_b16 v[112:113], v4 offset:6144\n"
      "ds_read_b64_tr_b16 v[114:115], v4 offset:6656\n"
      "s_waitcnt lgkmcnt(0)\n"
      "s_barrier\n"
      "s_add_u32 s97, s97, 16384\n"
      "s_add_u32 m0, s4, 16384\n"
      "s_and_b32 s97, s97, 0x3ffff\n"
      "s_add_u32 s91, s97, 65536\n"
      "buffer_load_dwordx4 v2, s[48:51], s91 offen offset:0 sc1 lds\n"
      "buffer_load_dwordx4 v2, s[48:51], s91 offen offset:1024 sc1 lds\n"
      "buffer_load_dwordx4 v2, s[48:51], s91 offen offset:2048 sc1 lds\n"
      "buffer_load_dwordx4 v2, s[48:51], s91 offen offset:3072 sc1 lds\n"
      "s_add_u32 m0, s5, 4096\n"
      "s_mov_b32 s92, 1769472\n"
      "buffer_load_dwordx4 v3, s[52:55], s92 offen lds\n"
      "s_add_u32 m0, s5, 5120\n"
      "s_mov_b32 s92, 1835008\n"
      "buffer_load_dwordx4 v3, s[52:55], s92 offen lds\n"
      "s_add_u32 m0, s5, 6144\n"
      "s_mov_b32 s92, 1867776\n"
      "buffer_load_dwordx4 v3, s[52:55], s92 offen lds\n"
      "s_waitcnt vmcnt(11)\n"
      "v_xor_b32 v17, s36, v17\n"
      "v_xor_b32 v19, s36, v19\n"
      "v_xor_b32 v21, s36, v21\n"
      "v_xor_b32 v23, s36, v23\n"
      "v_xor_b32 v25, s36, v25\n"
      "v_xor_b32 v27, s36, v27\n"
      "v_xor_b32 v29, s36, v29\n"
      "v_xor_b32 v31, s36, v31\n"
      "v_or3_b32 v17, v17, v19, v21\n"
      "v_or3_b32 v17, v17, v23, v25\n"
      "v_or3_b32 v17, v17, v27, v29\n"
      "v_or_b32 v17, v17, v31\n"
      "v_cmp_ne_u32 vcc, 0, v17\n"
      "s_cmp_lt_u32 s7, 224\n"
      "s_cselect_b64 vcc, vcc, 0\n"
      "s_nop 0\n"
      "s_cbranch_vccz .Lpollg_ok\n"
      "s_nop 0\n"
      "s_mov_b32 s90, 0x20000\n"
      ".Lpollg:\n"
      "s_sleep 1\n"
      "buffer_load_dwordx4 v[16:19], v9, s[48:51], s93 offen offset:0 sc1\n"
      "buffer_load_dwordx4 v[20:23], v9, s[48:51], s93 offen offset:1024 sc1\n"
      "buffer_load_dwordx4 v[24:27], v9, s[48:51], s93 offen offset:2048 sc1\n"
      "buffer_load_dwordx4 v[28:31], v9, s[48:51], s93 offen offset:3072 sc1\n"
      "s_waitcnt vmcnt(0)\n"
      "v_xor_b32 v17, s36, v17\n"
      "v_xor_b32 v19, s36, v19\n"
      "v_xor_b32 v21, s36, v21\n"
      "v_xor_b32 v23, s36, v23\n"
      "v_xor_b32 v25, s36, v25\n"
      "v_xor_b32 v27, s36, v27\n"
      "v_xor_b32 v29, s36, v29\n"
      "v_xor_b32 v31, s36, v31\n"
      "v_or3_b32 v17, v17, v19, v21\n"
      "v_or3_b32 v17, v17, v23, v25\n"
      "v_or3_b32 v17, v17, v27, v29\n"
      "v_or_b32 v17, v17, v31\n"
      "v_cmp_ne_u32 vcc, 0, v17\n"
      "s_cmp_lt_u32 s7, 224\n"
      "s_cselect_b64 vcc, vcc, 0\n"
      "s_sub_u32 s90, s90, 1\n"
      "s_nop 1\n"
      "s_cbranch_vccz .Lpollg_ok\n"
      "s_cbranch_scc0 .Lpollg\n"
      ".Lpollg_ok:\n"
      "v_add_f32 v16, v16, v20\n"
      "v_add_f32 v24, v24, v28\n"
      "v_add_f32 v18, v18, v22\n"
      "v_add_f32 v26, v26, v30\n"
      "v_add_f32 v16, v16, v24\n"
      "v_add_f32 v18, v18, v26\n"
      "v_cvt_pk_bf16_f32 v16, v16, v16\n"
      "v_cvt_pk_bf16_f32 v18, v18, v18\n"
      "v_lshlrev_b32 v16, 16, v16\n"
      "v_lshlrev_b32 v18, 16, v18\n"
      "v_mul_f32 v17, 0xbfb8aa3b, v16\n"
      "v_exp_f32 v17, v17\n"
      "s_nop 0\n"
      "v_add_f32 v17, 1.0, v17\n"
      "v_rcp_f32 v17, v17\n"
      "s_nop 0\n"
      "v_mul_f32 v16, v16, v17\n"
      "v_cvt_pk_bf16_f32 v16, v16, v16\n"
      "v_lshlrev_b32 v16, 16, v16\n"
      "v_mul_f32 v16, v16, v18\n"
      "v_cvt_pk_bf16_f32 v16, v16, v16\n"
      "s_lshl_b32 s80, s30, 7\n"
      "s_add_u32 s80, s80, 160256\n"
      "v_lshl_add_u32 v17, v1, 1, s80\n"
      "ds_write_b16 v17, v16\n"
      "v_lshl_add_u32 v12, v11, 4, s80\n"
      "s_waitcnt vmcnt(7)\n"
      "ds_read_b64_tr_b16 v[116:117], v4 offset:0\n"
      "ds_read_b64_tr_b16 v[118:119], v4 offset:512\n"
      "ds_read_b64_tr_b16 v[120:121], v4 offset:1024\n"
      "ds_read_b64_tr_b16 v[122:123], v4 offset:1536\n"
      "ds_read_b64_tr_b16 v[124:125], v4 offset:2048\n"
      "ds_read_b64_tr_b16 v[126:127], v4 offset:2560\n"
      "ds_read_b64_tr_b16 v[128:129], v4 offset:3072\n"
      "ds_read_b64_tr_b16 v[130:131], v4 offset:3584\n"
      "s_waitcnt lgkmcnt(0)\n"
      "s_barrier\n"
      "s_add_u32 s97, s97, 16384\n"
      "s_add_u32 m0, s4, 32768\n"
      "s_and_b32 s97, s97, 0x3ffff\n"
      "s_add_u32 s91, s97, 65536\n"
      "buffer_load_dwordx4 v2, s[48:51], s91 offen offset:0 sc1 lds\n"
      "buffer_load_dwordx4 v2, s[48:51], s91 offen offset:1024 sc1 lds\n"
      "buffer_load_dwordx4 v2, s[48:51], s91 offen offset:2048 sc1 lds\n"
      "buffer_load_dwordx4 v2, s[48:51], s91 offen offset:3072 sc1 lds\n"
      "s_add_u32 m0, s5, 0\n"
      "s_mov_b32 s92, 1900544\n"
      "buffer_load_dwordx4 v3, s[52:55], s92 offen lds\n"
      "s_add_u32 m0, s5, 1024\n"
      "s_mov_b32 s92, 1966080\n"
      "buffer_load_dwordx4 v3, s[52:55], s92 offen lds\n"
      "s_add_u32 m0, s5, 2048\n"
      "s_mov_b32 s92, 1998848\n"
      "buffer_load_dwordx4 v3, s[52:55], s92 offen lds\n"
      "s_add_u32 m0, s5, 3072\n"
      "s_mov_b32 s92, 2031616\n"
      "buffer_load_dwordx4 v3, s[52:55], s92 offen lds\n"
      "ds_read_b128 v[16:19], v12 offset:0\n"
      "ds_read_b64_tr_b16 v[20:21], v7 offset:0\n"
      "ds_read_b64_tr_b16 v[22:23], v7 offset:512\n"
      "ds_read_b64_tr_b16 v[24:25], v7 offset:64\n"
      "ds_read_b64_tr_b16 v[26:27], v7 offset:576\n"
      "s_waitcnt lgkmcnt(0)\n"
      "v_mfma_f32_32x32x16_bf16 v[32:47], v[16:19], v[20:23], 0\n"
      "v_mfma_f32_32x32x16_bf16 v[48:63], v[16:19], v[24:27], 0\n"
      "ds_read_b128 v[16:19], v12 offset:32\n"
      "ds_read_b64_tr_b16 v[20:21], v7 offset:2048\n"
      "ds_read_b64_tr_b16 v[22:23], v7 offset:2560\n"
      "ds_read_b64_tr_b16 v[24:25], v7 offset:2112\n"
      "ds_read_b64_tr_b16 v[26:27], v7 offset:2624\n"
      "s_waitcnt lgkmcnt(0)\n"
      "v_mfma_f32_32x32x16_bf16 v[32:47], v[16:19], v[20:23], v[32:47]\n"
      "v_mfma_f32_32x32x16_bf16 v[48:63], v[16:19], v[24:27], v[48:63]\n"
      "ds_read_b128 v[16:19], v12 offset:64\n"
      "ds_read_b64_tr_b16 v[20:21], v7 offset:8192\n"
      "ds_read_b64_tr_b16 v[22:23], v7 offset:8704\n"
      "ds_read_b64_tr_b16 v[24:25], v7 offset:8256\n"
      "ds_read_b64_tr_b16 v[26:27], v7 offset:8768\n"
      "s_waitcnt lgkmcnt(0)\n"
      "v_mfma_f32_32x32x16_bf16 v[32:47], v[16:19], v[20:23], v[32:47]\n"
      "v_mfma_f32_32x32x16_bf16 v[48:63], v[16:19], v[24:27], v[48:63]\n"
      "ds_read_b128 v[16:19], v12 offset:96\n"
      "ds_read_b64_tr_b16 v[20:21], v7 offset:10240\n"
      "ds_read_b64_tr_b16 v[22:23], v7 offset:10752\n"
      "ds_read_b64_tr_b16 v[24:25], v7 offset:10304\n"
      "ds_read_b64_tr_b16 v[26:27], v7 offset:10816\n"
      "s_waitcnt lgkmcnt(0)\n"
      "v_mfma_f32_32x32x16_bf16 v[32:47], v[16:19], v[20:23], v[32:47]\n"
      "v_mfma_f32_32x32x16_bf16 v[48:63], v[16:19], v[24:27], v[48:63]\n"
      "s_lshr_b32 s80, s7, 2\n"
      "s_lshl_b32 s80, s80, 12\n"
      "s_and_b32 s81, s7, 3\n"
      "s_lshl_b32 s81, s81, 10\n"
      "s_add_u32 s80, s80, s81\n"
      "s_lshl_b32 s81, s30, 8\n"
      "s_add_u32 s80, s80, s81\n"
      "v_lshl_add_u32 v28, v1, 2, s80\n"
      "s_mov_b32 s93, 524288\n"
      "s_nop 2\n"
      "v_permlane32_swap_b32 v32, v48\n"
      "buffer_store_dword v32, v28, s[48:51], s93 offen sc1\n"
      "s_waitcnt vmcnt(9)\n"
      "ds_read_b64_tr_b16 v[132:133], v4 offset:4096\n"
      "ds_read_b64_tr_b16 v[134:135], v4 offset:4608\n"
      "ds_read_b64_tr_b16 v[136:137], v4 offset:5120\n"
      "ds_read_b64_tr_b16 v[138:139], v4 offset:5632\n"
      "ds_read_b64_tr_b16 v[140:141], v4 offset:6144\n"
      "ds_read_b64_tr_b16 v[142:143], v4 offset:6656\n"
      "s_waitcnt lgkmcnt(0)\n"
      "s_barrier\n"
      "s_add_u32 s97, s97, 16384\n"
      "s_add_u32 m0, s4, 49152\n"
      "s_and_b32 s97, s97, 0x3ffff\n"
      "s_add_u32 s91, s97, 65536\n"
      "buffer_load_dwordx4 v2, s[48:51], s91 offen offset:0 sc1 lds\n"
      "buffer_load_dwordx4 v2, s[48:51], s91 offen offset:1024 sc1 lds\n"
      "buffer_load_dwordx4 v2, s[48:51], s91 offen offset:2048 sc1 lds\n"
      "buffer_load_dwordx4 v2, s[48:51], s91 offen offset:3072 sc1 lds\n"
      "s_lshl_b32 s80, s7, 4\n"
      "s_lshl_b32 s81, s30, 2\n"
      "s_add_u32 s80, s80, s81\n"
      "v_lshl_add_u32 v16, v1, 26, s80\n"
      "v_mov_b32 v17, s36\n"
      "s_mov_b32 s93, 12288\n"
      "s_waitcnt vmcnt(4)\n"
      "buffer_store_dword v17, v16, s[48:51], s93 offen sc1\n"
      "s_add_u32 m0, s6, 0\n"
      "s_mov_b32 s92, 65536\n"
      "buffer_load_dwordx4 v3, s[52:55], s92 offen lds\n"
      "s_add_u32 m0, s6, 1024\n"
      "s_mov_b32 s92, 98304\n"
      "buffer_load_dwordx4 v3, s[52:55], s92 offen lds\n"
      "s_add_u32 m0, s6, 2048\n"
      "s_mov_b32 s92, 229376\n"
      "buffer_load_dwordx4 v3, s[52:55], s92 offen lds\n"
      "s_add_u32 m0, s6, 3072\n"
      "s_mov_b32 s92, 360448\n"
      "buffer_load_dwordx4 v3, s[52:55], s92 offen lds\n"
      "s_add_u32 m0, s6, 4096\n"
      "s_mov_b32 s92, 491520\n"
      "buffer_load_dwordx4 v3, s[52:55], s92 offen lds\n"
      "s_add_u32 m0, s6, 5120\n"
      "s_mov_b32 s92, 589824\n"
      "buffer_load_dwordx4 v3, s[52:55], s92 offen lds\n"
      "s_add_u32 m0, s6, 6144\n"
      "s_mov_b32 s92, 622592\n"
      "buffer_load_dwordx4 v3, s[52:55], s92 offen lds\n"
      "s_add_u32 m0, s6, 7168\n"
      "s_mov_b32 s92, 753664\n"
      "buffer_load_dwordx4 v3, s[52:55], s92 offen lds\n"
      "s_add_u32 m0, s6, 8192\n"
      "s_mov_b32 s92, 884736\n"
      "buffer_load_dwordx4 v3, s[52:55], s92 offen lds\n"
      "s_add_u32 m0, s6, 9216\n"
      "s_mov_b32 s92, 1015808\n"
      "buffer_load_dwordx4 v3, s[52:55], s92 offen lds\n"
      "s_add_u32 m0, s6, 10240\n"
      "s_mov_b32 s92, 1146880\n"
      "buffer_load_dwordx4 v3, s[52:55], s92 offen lds\n"
      "s_add_u32 m0, s6, 11264\n"
      "s_mov_b32 s92, 1277952\n"
      "buffer_load_dwordx4 v3, s[52:55], s92 offen lds\n"
      "s_add_u32 m0, s6, 12288\n"
      "s_mov_b32 s92, 1409024\n"
      "buffer_load_dwordx4 v3, s[52:55], s92 offen lds\n"
      "s_add_u32 m0, s6, 13312\n"
      "s_mov_b32 s92, 1540096\n"
      "buffer_load_dwordx4 v3, s[52:55], s92 offen lds\n"
      "s_add_u32 m0, s6, 14336\n"
      "s_mov_b32 s92, 1671168\n"
      "buffer_load_dwordx4 v3, s[52:55], s92 offen lds\n"
      "s_add_u32 m0, s6, 15360\n"
      "s_mov_b32 s92, 1802240\n"
      "buffer_load_dwordx4 v3, s[52:55], s92 offen lds\n"
      "ds_read_b64_tr_b16 v[144:145], v4 offset:0\n"
      "ds_read_b64_tr_b16 v[146:147], v4 offset:512\n"
      "ds_read_b64_tr_b16 v[148:149], v4 offset:1024\n"
      "ds_read_b64_tr_b16 v[150:151], v4 offset:1536\n"
      "ds_read_b64_tr_b16 v[152:153], v4 offset:2048\n"
      "ds_read_b64_tr_b16 v[154:155], v4 offset:2560\n"
      "ds_read_b64_tr_b16 v[156:157], v4 offset:3072\n"
      "ds_read_b64_tr_b16 v[158:159], v4 offset:3584\n"
      "s_waitcnt lgkmcnt(0)\n"
      "s_barrier\n"
      "s_waitcnt lgkmcnt(0)\n"
      "s_add_u32 m0, s5, 0\n"
      "s_mov_b32 s92, 1933312\n"
      "buffer_load_dwordx4 v3, s[52:55], s92 offen lds\n"
      "s_add_u32 m0, s5, 1024\n"
      "s_mov_b32 s92, 2064384\n"
      "buffer_load_dwordx4 v3, s[52:55], s92 offen lds\n"
      "s_waitcnt vmcnt(0) lgkmcnt(0)\n"
      "s_barrier\n"
      "s_lshr_b32 s84, s37, 6\n"
      "s_cmp_eq_u32 s84, s31\n"
      "s_cbranch_scc0 .Lnoyh\n"
      "v_min_u32 v17, 55, v1\n"
      "v_lshlrev_b32 v17, 6, v17\n"
      "s_lshr_b32 s81, s94, 7\n"
      "s_lshl_b32 s81, s81, 2\n"
      "s_add_u32 s81, s81, 12288\n"
      "buffer_load_dword v16, v17, s[48:51], s81 offen sc1\n"
      "s_waitcnt vmcnt(0)\n"
      "v_xor_b32 v16, s36, v16\n"
      "v_cmp_ne_u32 vcc, 0, v16\n"
      "s_nop 0\n"
      "s_cbranch_vccz .Lpolly_ok\n"
      "s_nop 2\n"
      "s_mov_b32 s90, 0x20000\n"
      ".Lpolly:\n"
      "s_sleep 1\n"
      "buffer_load_dword v16, v17, s[48:51], s81 offen sc1\n"
      "s_waitcnt vmcnt(0)\n"
      "v_xor_b32 v16, s36, v16\n"
      "v_cmp_ne_u32 vcc, 0, v16\n"
      "s_sub_u32 s90, s90, 1\n"
      "s_nop 3\n"
      "s_cbranch_vccz .Lpolly_ok\n"
      "s_cbranch_scc0 .Lpolly\n"
      ".Lpolly_ok:\n"
      "v_lshrrev_b32 v12, 3, v1\n"
      "v_lshlrev_b32 v12, 12, v12\n"
      "v_and_b32 v15, 7, v1\n"
      "v_lshl_add_u32 v12, v15, 4, v12\n"
      "s_lshl_b32 s81, s94, 1\n"
      "s_add_u32 s81, s81, 524288\n"
      "buffer_load_dwordx4 v[16:19], v12, s[48:51], s81 offen sc1\n"
      "s_add_u32 s81, s81, 32768\n"
      "buffer_load_dwordx4 v[20:23], v12, s[48:51], s81 offen sc1\n"
      "s_add_u32 s81, s81, 32768\n"
      "buffer_load_dwordx4 v[24:27], v12, s[48:51], s81 offen sc1\n"
      "s_add_u32 s81, s81, 32768\n"
      "buffer_load_dwordx4 v[28:31], v12, s[48:51], s81 offen sc1\n"
      "s_add_u32 s81, s81, 32768\n"
      "buffer_load_dwordx4 v[32:35], v12, s[48:51], s81 offen sc1\n"
      "s_add_u32 s81, s81, 32768\n"
      "buffer_load_dwordx4 v[36:39], v12, s[48:51], s81 offen sc1\n"
      "s_add_u32 s81, s81, 32768\n"
      "buffer_load_dwordx4 v[40:43], v12, s[48:51], s81 offen sc1\n"
      "s_waitcnt vmcnt(0)\n"
      "v_add_f32 v16, v16, v20\n"
      "v_add_f32 v17, v17, v21\n"
      "v_add_f32 v18, v18, v22\n"
      "v_add_f32 v19, v19, v23\n"
      "v_add_f32 v16, v16, v24\n"
      "v_add_f32 v17, v17, v25\n"
      "v_add_f32 v18, v18, v26\n"
      "v_add_f32 v19, v19, v27\n"
      "v_add_f32 v16, v16, v28\n"
      "v_add_f32 v17, v17, v29\n"
      "v_add_f32 v18, v18, v30\n"
      "v_add_f32 v19, v19, v31\n"
      "v_add_f32 v16, v16, v32\n"
      "v_add_f32 v17, v17, v33\n"
      "v_add_f32 v18, v18, v34\n"
      "v_add_f32 v19, v19, v35\n"
      "v_add_f32 v16, v16, v36\n"
      "v_add_f32 v17, v17, v37\n"
      "v_add_f32 v18, v18, v38\n"
      "v_add_f32 v19, v19, v39\n"
      "v_add_f32 v16, v16, v40\n"
      "v_add_f32 v17, v17, v41\n"
      "v_add_f32 v18, v18, v42\n"
      "v_add_f32 v19, v19, v43\n"
      "v_add_f32_dpp v16, v16, v16 row_ror:8 row_mask:0xf bank_mask:0xf\n"
      "v_add_f32_dpp v17, v17, v17 row_ror:8 row_mask:0xf bank_mask:0xf\n"
      "v_add_f32_dpp v18, v18, v18 row_ror:8 row_mask:0xf bank_mask:0xf\n"
      "v_add_f32_dpp v19, v19, v19 row_ror:8 row_mask:0xf bank_mask:0xf\n"
      "v_mov_b32 v20, v16\n"
      "v_mov_b32 v21, v17\n"
      "v_mov_b32 v22, v18\n"
      "v_mov_b32 v23, v19\n"
      "v_permlane16_swap_b32 v16, v20\n"
      "v_permlane16_swap_b32 v17, v21\n"
      "v_permlane16_swap_b32 v18, v22\n"
      "v_permlane16_swap_b32 v19, v23\n"
      "v_add_f32 v16, v16, v20\n"
      "v_add_f32 v17, v17, v21\n"
      "v_add_f32 v18, v18, v22\n"
      "v_add_f32 v19, v19, v23\n"
      "v_mov_b32 v20, v16\n"
      "v_mov_b32 v21, v17\n"
      "v_mov_b32 v22, v18\n"
      "v_mov_b32 v23, v19\n"
      "v_permlane32_swap_b32 v16, v20\n"
      "v_permlane32_swap_b32 v17, v21\n"
      "v_permlane32_swap_b32 v18, v22\n"
      "v_permlane32_swap_b32 v19, v23\n"
      "v_add_f32 v16, v16, v20\n"
      "v_add_f32 v17, v17, v21\n"
      "v_add_f32 v18, v18, v22\n"
      "v_add_f32 v19, v19, v23\n"
      "v_cvt_pk_bf16_f32 v16, v16, v16\n"
      "v_lshlrev_b32 v16, 16, v16\n"
      "v_cvt_pk_bf16_f32 v17, v17, v17\n"
      "v_lshlrev_b32 v17, 16, v17\n"
      "v_cvt_pk_bf16_f32 v18, v18, v18\n"
      "v_lshlrev_b32 v18, 16, v18\n"
      "v_cvt_pk_bf16_f32 v19, v19, v19\n"
      "v_lshlrev_b32 v19, 16, v19\n"
      "v_mul_f32 v16, s38, v16\n"
      "v_mul_f32 v17, s38, v17\n"
      "v_mul_f32 v18, s38, v18\n"
      "v_mul_f32 v19, s38, v19\n"
      "v_cvt_pk_bf16_f32 v16, v16, v16\n"
      "v_lshlrev_b32 v16, 16, v16\n"
      "v_cvt_pk_bf16_f32 v17, v17, v17\n"
      "v_lshlrev_b32 v17, 16, v17\n"
      "v_cvt_pk_bf16_f32 v18, v18, v18\n"
      "v_lshlrev_b32 v18, 16, v18\n"
      "v_cvt_pk_bf16_f32 v19, v19, v19\n"
      "v_lshlrev_b32 v19, 16, v19\n"
      "s_lshl_b32 s85, s30, 7\n"
      "s_add_u32 s85, s85, 160896\n"
      "v_lshl_add_u32 v12, v15, 4, s85\n"
      "ds_write_b128 v12, v[16:19]\n"
      "s_nop 1\n"
      ".Lnoyh:\n"
      "s_lshl_b32 s83, s31, 11\n"
      "s_add_u32 s83, s83, 1572864\n"
      "s_lshl_b32 s80, s30, 9\n"
      "v_and_b32 v13, 7, v1\n"
      "v_lshl_add_u32 v13, v13, 6, s80\n"
      "s_lshl_b32 s80, s30, 15\n"
      "s_lshl_b32 s82, s31, 17\n"
      "s_add_u32 s82, s82, s80\n"
      "s_add_u32 s82, s82, 2359296\n"
      "v_add_u32 v14, 0x10000, v9\n"
      "v_mov_b32 v15, v9\n"
      "s_lshl_b32 s85, s30, 15\n"
      "s_add_u32 s85, s85, 8192\n"
      "v_and_b32 v2, 31, v1\n"
      "v_lshrrev_b32 v3, 5, v1\n"
      "v_and_b32 v4, 7, v1\n"
      "v_xor_b32 v3, v3, v4\n"
      "v_lshlrev_b32 v3, 4, v3\n"
      "v_lshl_add_u32 v2, v2, 7, v3\n"
      "v_add_u32 v6, s85, v2\n"
      "v_xor_b32 v7, 32, v6\n"
      "v_xor_b32 v8, 64, v6\n"
      "v_xor_b32 v9, 96, v6\n"
      "s_setprio 3\n"
      "s_waitcnt lgkmcnt(0)\n"
      "s_barrier\n"
      "buffer_load_dword v12, v13, s[48:51], s83 offen sc1\n"
      "s_waitcnt vmcnt(0)\n"
      "v_xor_b32 v12, s36, v12\n"
      "v_cmp_ne_u32 vcc, 0, v12\n"
      "s_nop 0\n"
      "s_cbranch_vccz .Lpollo_ok\n"
      "s_nop 2\n"
      "s_mov_b32 s90, 0x20000\n"
      ".Lpollo:\n"
      "s_sleep 1\n"
      "buffer_load_dword v12, v13, s[48:51], s83 offen sc1\n"
      "s_waitcnt vmcnt(0)\n"
      "v_xor_b32 v12, s36, v12\n"
      "v_cmp_ne_u32 vcc, 0, v12\n"
      "s_sub_u32 s90, s90, 1\n"
      "s_nop 3\n"
      "s_cbranch_vccz .Lpollo_ok\n"
      "s_cbranch_scc0 .Lpollo\n"
      ".Lpollo_ok:\n"
      "s_add_u32 m0, s80, 0\n"
      "s_add_u32 s81, s82, 0\n"
      "buffer_load_dwordx4 v15, s[48:51], s81 offen offset:0 sc1 lds\n"
      "buffer_load_dwordx4 v15, s[48:51], s81 offen offset:1024 sc1 lds\n"
      "buffer_load_dwordx4 v15, s[48:51], s81 offen offset:2048 sc1 lds\n"
      "buffer_load_dwordx4 v15, s[48:51], s81 offen offset:3072 sc1 lds\n"
      "s_add_u32 m0, s80, 4096\n"
      "s_add_u32 s81, s82, 4096\n"
      "buffer_load_dwordx4 v15, s[48:51], s81 offen offset:0 sc1 lds\n"
      "buffer_load_dwordx4 v15, s[48:51], s81 offen offset:1024 sc1 lds\n"
      "buffer_load_dwordx4 v15, s[48:51], s81 offen offset:2048 sc1 lds\n"
      "buffer_load_dwordx4 v15, s[48:51], s81 offen offset:3072 sc1 lds\n"
      "s_add_u32 m0, s80, 8192\n"
      "s_add_u32 s81, s82, 8192\n"
      "buffer_load_dwordx4 v15, s[48:51], s81 offen offset:0 sc1 lds\n"
      "buffer_load_dwordx4 v15, s[48:51], s81 offen offset:1024 sc1 lds\n"
      "buffer_load_dwordx4 v15, s[48:51], s81 offen offset:2048 sc1 lds\n"
      "buffer_load_dwordx4 v15, s[48:51], s81 offen offset:3072 sc1 lds\n"
      "s_add_u32 m0, s80, 12288\n"
      "s_add_u32 s81, s82, 12288\n"
      "buffer_load_dwordx4 v15, s[48:51], s81 offen offset:0 sc1 lds\n"
      "buffer_load_dwordx4 v15, s[48:51], s81 offen offset:1024 sc1 lds\n"
      "buffer_load_dwordx4 v15, s[48:51], s81 offen offset:2048 sc1 lds\n"
      "buffer_load_dwordx4 v15, s[48:51], s81 offen offset:3072 sc1 lds\n"
      "s_waitcnt vmcnt(8)\n"
      "s_barrier\n"
      "s_add_u32 m0, s80, 16384\n"
      "s_add_u32 s81, s82, 16384\n"
      "buffer_load_dwordx4 v15, s[48:51], s81 offen offset:0 sc1 lds\n"
      "buffer_load_dwordx4 v15, s[48:51], s81 offen offset:1024 sc1 lds\n"
      "buffer_load_dwordx4 v15, s[48:51], s81 offen offset:2048 sc1 lds\n"
      "buffer_load_dwordx4 v15, s[48:51], s81 offen offset:3072 sc1 lds\n"
      "s_add_u32 m0, s80, 20480\n"
      "s_add_u32 s81, s82, 20480\n"
      "buffer_load_dwordx4 v15, s[48:51], s81 offen offset:0 sc1 lds\n"
      "buffer_load_dwordx4 v15, s[48:51], s81 offen offset:1024 sc1 lds\n"
      "buffer_load_dwordx4 v15, s[48:51], s81 offen offset:2048 sc1 lds\n"
      "buffer_load_dwordx4 v15, s[48:51], s81 offen offset:3072 sc1 lds\n"
      "ds_read_b128 v[48:51], v15 offset:0\n"
      "ds_read_b128 v[52:55], v15 offset:1024\n"
      "ds_read_b128 v[56:59], v15 offset:2048\n"
      "ds_read_b128 v[60:63], v15 offset:3072\n"
      "ds_read_b128 v[64:67], v15 offset:32768\n"
      "s_waitcnt lgkmcnt(4)\n"
      "v_mfma_f32_32x32x16_bf16 v[16:31], a[0:3], v[48:51], 0\n"
      "ds_read_b128 v[68:71], v15 offset:33792\n"
      "s_waitcnt lgkmcnt(4)\n"
      "v_mfma_f32_32x32x16_bf16 v[32:47], a[0:3], v[52:55], 0\n"
      "ds_read_b128 v[48:51], v15 offset:34816\n"
      "s_waitcnt lgkmcnt(4)\n"
      "v_mfma_f32_32x32x16_bf16 v[16:31], a[4:7], v[56:59], v[16:31]\n"
      "ds_read_b128 v[52:55], v15 offset:35840\n"
      "s_waitcnt lgkmcnt(4)\n"
      "v_mfma_f32_32x32x16_bf16 v[32:47], a[4:7], v[60:63], v[32:47]\n"
      "ds_read_b128 v[56:59], v14 offset:0\n"
      "s_waitcnt lgkmcnt(4)\n"
      "v_mfma_f32_32x32x16_bf16 v[16:31], a[44:47], v[64:67], v[16:31]\n"
      "ds_read_b128 v[60:63], v14 offset:1024\n"
      "s_waitcnt lgkmcnt(4)\n"
      "v_mfma_f32_32x32x16_bf16 v[32:47], a[44:47], v[68:71], v[32:47]\n"
      "ds_read_b128 v[64:67], v14 offset:2048\n"
      "s_waitcnt lgkmcnt(4)\n"
      "v_mfma_f32_32x32x16_bf16 v[16:31], a[48:51], v[48:51], v[16:31]\n"
      "ds_read_b128 v[68:71], v14 offset:3072\n"
      "s_waitcnt lgkmcnt(4)\n"
      "v_mfma_f32_32x32x16_bf16 v[32:47], a[48:51], v[52:55], v[32:47]\n"
      "ds_read_b128 v[48:51], v14 offset:4096\n"
      "s_waitcnt lgkmcnt(4)\n"
      "v_mfma_f32_32x32x16_bf16 v[16:31], a[88:91], v[56:59], v[16:31]\n"
      "ds_read_b128 v[52:55], v14 offset:5120\n"
      "s_waitcnt lgkmcnt(4)\n"
      "v_mfma_f32_32x32x16_bf16 v[32:47], a[88:91], v[60:63], v[32:47]\n"
      "ds_read_b128 v[56:59], v14 offset:32768\n"
      "s_waitcnt lgkmcnt(4)\n"
      "v_mfma_f32_32x32x16_bf16 v[16:31], a[92:95], v[64:67], v[16:31]\n"
      "ds_read_b128 v[60:63], v14 offset:33792\n"
      "s_waitcnt lgkmcnt(4)\n"
      "v_mfma_f32_32x32x16_bf16 v[32:47], a[92:95], v[68:71], v[32:47]\n"
      "ds_read_b128 v[64:67], v14 offset:34816\n"
      "s_waitcnt lgkmcnt(4)\n"
      "v_mfma_f32_32x32x16_bf16 v[16:31], v[72:75], v[48:51], v[16:31]\n"
      "ds_read_b128 v[68:71], v14 offset:35840\n"
      "s_waitcnt lgkmcnt(4)\n"
      "v_mfma_f32_32x32x16_bf16 v[32:47], v[72:75], v[52:55], v[32:47]\n"
      "ds_read_b128 v[48:51], v14 offset:36864\n"
      "s_waitcnt lgkmcnt(4)\n"
      "v_mfma_f32_32x32x16_bf16 v[16:31], v[112:115], v[56:59], v[16:31]\n"
      "ds_read_b128 v[52:55], v14 offset:37888\n"
      "s_waitcnt lgkmcnt(4)\n"
      "v_mfma_f32_32x32x16_bf16 v[32:47], v[112:115], v[60:63], v[32:47]\n"
      "s_waitcnt lgkmcnt(3)\n"
      "v_mfma_f32_32x32x16_bf16 v[16:31], v[116:119], v[64:67], v[16:31]\n"
      "s_waitcnt lgkmcnt(2)\n"
      "v_mfma_f32_32x32x16_bf16 v[32:47], v[116:119], v[68:71], v[32:47]\n"
      "s_waitcnt vmcnt(8)\n"
      "s_barrier\n"
      "s_add_u32 m0, s80, 24576\n"
      "s_add_u32 s81, s82, 24576\n"
      "buffer_load_dwordx4 v15, s[48:51], s81 offen offset:0 sc1 lds\n"
      "buffer_load_dwordx4 v15, s[48:51], s81 offen offset:1024 sc1 lds\n"
      "buffer_load_dwordx4 v15, s[48:51], s81 offen offset:2048 sc1 lds\n"
      "buffer_load_dwordx4 v15, s[48:51], s81 offen offset:3072 sc1 lds\n"
      "s_add_u32 m0, s80, 28672\n"
      "s_add_u32 s81, s82, 28672\n"
      "buffer_load_dwordx4 v15, s[48:51], s81 offen offset:0 sc1 lds\n"
      "buffer_load_dwordx4 v15, s[48:51], s81 offen offset:1024 sc1 lds\n"
      "buffer_load_dwordx4 v15, s[48:51], s81 offen offset:2048 sc1 lds\n"
      "buffer_load_dwordx4 v15, s[48:51], s81 offen offset:3072 sc1 lds\n"
      "ds_read_b128 v[56:59], v15 offset:8192\n"
      "ds_read_b128 v[60:63], v15 offset:9216\n"
      "ds_read_b128 v[64:67], v15 offset:10240\n"
      "s_waitcnt lgkmcnt(4)\n"
      "v_mfma_f32_32x32x16_bf16 v[16:31], v[120:123], v[48:51], v[16:31]\n"
      "ds_read_b128 v[68:71], v15 offset:11264\n"
      "s_waitcnt lgkmcnt(4)\n"
      "v_mfma_f32_32x32x16_bf16 v[32:47], v[120:123], v[52:55], v[32:47]\n"
      "ds_read_b128 v[48:51], v15 offset:12288\n"
      "s_waitcnt lgkmcnt(4)\n"
      "v_mfma_f32_32x32x16_bf16 v[16:31], a[8:11], v[56:59], v[16:31]\n"
      "ds_read_b128 v[52:55], v15 offset:13312\n"
      "s_waitcnt lgkmcnt(4)\n"
      "v_mfma_f32_32x32x16_bf16 v[32:47], a[8:11], v[60:63], v[32:47]\n"
      "ds_read_b128 v[56:59], v15 offset:40960\n"
      "s_waitcnt lgkmcnt(4)\n"
      "v_mfma_f32_32x32x16_bf16 v[16:31], a[12:15], v[64:67], v[16:31]\n"
      "ds_read_b128 v[60:63], v15 offset:41984\n"
      "s_waitcnt lgkmcnt(4)\n"
      "v_mfma_f32_32x32x16_bf16 v[32:47], a[12:15], v[68:71], v[32:47]\n"
      "ds_read_b128 v[64:67], v15 offset:43008\n"
      "s_waitcnt lgkmcnt(4)\n"
      "v_mfma_f32_32x32x16_bf16 v[16:31], a[16:19], v[48:51], v[16:31]\n"
      "ds_read_b128 v[68:71], v15 offset:44032\n"
      "s_waitcnt lgkmcnt(4)\n"
      "v_mfma_f32_32x32x16_bf16 v[32:47], a[16:19], v[52:55], v[32:47]\n"
      "ds_read_b128 v[48:51], v15 offset:45056\n"
      "s_waitcnt lgkmcnt(4)\n"
      "v_mfma_f32_32x32x16_bf16 v[16:31], a[52:55], v[56:59], v[16:31]\n"
      "ds_read_b128 v[52:55], v15 offset:46080\n"
      "s_waitcnt lgkmcnt(4)\n"
      "v_mfma_f32_32x32x16_bf16 v[32:47], a[52:55], v[60:63], v[32:47]\n"
      "ds_read_b128 v[56:59], v14 offset:8192\n"
      "s_waitcnt lgkmcnt(4)\n"
      "v_mfma_f32_32x32x16_bf16 v[16:31], a[56:59], v[64:67], v[16:31]\n"
      "ds_read_b128 v[60:63], v14 offset:9216\n"
      "s_waitcnt lgkmcnt(4)\n"
      "v_mfma_f32_32x32x16_bf16 v[32:47], a[56:59], v[68:71], v[32:47]\n"
      "ds_read_b128 v[64:67], v14 offset:10240\n"
      "s_waitcnt lgkmcnt(4)\n"
      "v_mfma_f32_32x32x16_bf16 v[16:31], a[60:63], v[48:51], v[16:31]\n"
      "ds_read_b128 v[68:71], v14 offset:11264\n"
      "s_waitcnt lgkmcnt(4)\n"
      "v_mfma_f32_32x32x16_bf16 v[32:47], a[60:63], v[52:55], v[32:47]\n"
      "ds_read_b128 v[48:51], v14 offset:12288\n"
      "s_waitcnt lgkmcnt(4)\n"
      "v_mfma_f32_32x32x16_bf16 v[16:31], v[76:79], v[56:59], v[16:31]\n"
      "ds_read_b128 v[52:55], v14 offset:13312\n"
      "s_waitcnt lgkmcnt(4)\n"
      "v_mfma_f32_32x32x16_bf16 v[32:47], v[76:79], v[60:63], v[32:47]\n"
      "ds_read_b128 v[56:59], v14 offset:40960\n"
      "s_waitcnt lgkmcnt(4)\n"
      "v_mfma_f32_32x32x16_bf16 v[16:31], v[80:83], v[64:67], v[16:31]\n"
      "ds_read_b128 v[60:63], v14 offset:41984\n"
      "s_waitcnt lgkmcnt(4)\n"
      "v_mfma_f32_32x32x16_bf16 v[32:47], v[80:83], v[68:71], v[32:47]\n"
      "ds_read_b128 v[64:67], v14 offset:43008\n"
      "s_waitcnt lgkmcnt(4)\n"
      "v_mfma_f32_32x32x16_bf16 v[16:31], v[84:87], v[48:51], v[16:31]\n"
      "ds_read_b128 v[68:71], v14 offset:44032\n"
      "s_waitcnt lgkmcnt(4)\n"
      "v_mfma_f32_32x32x16_bf16 v[32:47], v[84:87], v[52:55], v[32:47]\n"
      "ds_read_b128 v[48:51], v14 offset:45056\n"
      "s_waitcnt lgkmcnt(4)\n"
      "v_mfma_f32_32x32x16_bf16 v[16:31], v[124:127], v[56:59], v[16:31]\n"
      "ds_read_b128 v[52:55], v14 offset:46080\n"
      "s_waitcnt lgkmcnt(4)\n"
      "v_mfma_f32_32x32x16_bf16 v[32:47], v[124:127], v[60:63], v[32:47]\n"
      "s_waitcnt lgkmcnt(3)\n"
      "v_mfma_f32_32x32x16_bf16 v[16:31], v[128:131], v[64:67], v[16:31]\n"
      "s_waitcnt lgkmcnt(2)\n"
      "v_mfma_f32_32x32x16_bf16 v[32:47], v[128:131], v[68:71], v[32:47]\n"
      "s_waitcnt vmcnt(8)\n"
      "s_barrier\n"
      "ds_read_b128 v[56:59], v15 offset:16384\n"
      "ds_read_b128 v[60:63], v15 offset:17408\n"
      "ds_read_b128 v[64:67], v15 offset:18432\n"
      "s_waitcnt lgkmcnt(4)\n"
      "v_mfma_f32_32x32x16_bf16 v[16:31], v[132:135], v[48:51], v[16:31]\n"
      "ds_read_b128 v[68:71], v15 offset:19456\n"
      "s_waitcnt lgkmcnt(4)\n"
      "v_mfma_f32_32x32x16_bf16 v[32:47], v[132:135], v[52:55], v[32:47]\n"
      "ds_read_b128 v[48:51], v15 offset:20480\n"
      "s_waitcnt lgkmcnt(4)\n"
      "v_mfma_f32_32x32x16_bf16 v[16:31], a[20:23], v[56:59], v[16:31]\n"
      "ds_read_b128 v[52:55], v15 offset:21504\n"
      "s_waitcnt lgkmcnt(4)\n"
      "v_mfma_f32_32x32x16_bf16 v[32:47], a[20:23], v[60:63], v[32:47]\n"
      "ds_read_b128 v[56:59], v15 offset:49152\n"
      "s_waitcnt lgkmcnt(4)\n"
      "v_mfma_f32_32x32x16_bf16 v[16:31], a[24:27], v[64:67], v[16:31]\n"
      "ds_read_b128 v[60:63], v15 offset:50176\n"
      "s_waitcnt lgkmcnt(4)\n"
      "v_mfma_f32_32x32x16_bf16 v[32:47], a[24:27], v[68:71], v[32:47]\n"
      "ds_read_b128 v[64:67], v15 offset:51200\n"
      "s_waitcnt lgkmcnt(4)\n"
      "v_mfma_f32_32x32x16_bf16 v[16:31], a[28:31], v[48:51], v[16:31]\n"
      "ds_read_b128 v[68:71], v15 offset:52224\n"
      "s_waitcnt lgkmcnt(4)\n"
      "v_mfma_f32_32x32x16_bf16 v[32:47], a[28:31], v[52:55], v[32:47]\n"
      "ds_read_b128 v[48:51], v15 offset:53248\n"
      "s_waitcnt lgkmcnt(4)\n"
      "v_mfma_f32_32x32x16_bf16 v[16:31], a[64:67], v[56:59], v[16:31]\n"
      "ds_read_b128 v[52:55], v15 offset:54272\n"
      "s_waitcnt lgkmcnt(4)\n"
      "v_mfma_f32_32x32x16_bf16 v[32:47], a[64:67], v[60:63], v[32:47]\n"
      "ds_read_b128 v[56:59], v14 offset:16384\n"
      "s_waitcnt lgkmcnt(4)\n"
      "v_mfma_f32_32x32x16_bf16 v[16:31], a[68:71], v[64:67], v[16:31]\n"
      "ds_read_b128 v[60:63], v14 offset:17408\n"
      "s_waitcnt lgkmcnt(4)\n"
      "v_mfma_f32_32x32x16_bf16 v[32:47], a[68:71], v[68:71], v[32:47]\n"
      "ds_read_b128 v[64:67], v14 offset:18432\n"
      "s_waitcnt lgkmcnt(4)\n"
      "v_mfma_f32_32x32x16_bf16 v[16:31], a[72:75], v[48:51], v[16:31]\n"
      "ds_read_b128 v[68:71], v14 offset:19456\n"
      "s_waitcnt lgkmcnt(4)\n"
      "v_mfma_f32_32x32x16_bf16 v[32:47], a[72:75], v[52:55], v[32:47]\n"
      "ds_read_b128 v[48:51], v14 offset:20480\n"
      "s_waitcnt lgkmcnt(4)\n"
      "v_mfma_f32_32x32x16_bf16 v[16:31], v[88:91], v[56:59], v[16:31]\n"
      "ds_read_b128 v[52:55], v14 offset:21504\n"
      "s_waitcnt lgkmcnt(4)\n"
      "v_mfma_f32_32x32x16_bf16 v[32:47], v[88:91], v[60:63], v[32:47]\n"
      "ds_read_b128 v[56:59], v14 offset:49152\n"
      "s_waitcnt lgkmcnt(4)\n"
      "v_mfma_f32_32x32x16_bf16 v[16:31], v[92:95], v[64:67], v[16:31]\n"
      "ds_read_b128 v[60:63], v14 offset:50176\n"
      "s_waitcnt lgkmcnt(4)\n"
      "v_mfma_f32_32x32x16_bf16 v[32:47], v[92:95], v[68:71], v[32:47]\n"
      "ds_read_b128 v[64:67], v14 offset:51200\n"
      "s_waitcnt lgkmcnt(4)\n"
      "v_mfma_f32_32x32x16_bf16 v[16:31], v[96:99], v[48:51], v[16:31]\n"
      "ds_read_b128 v[68:71], v14 offset:52224\n"
      "s_waitcnt lgkmcnt(4)\n"
      "v_mfma_f32_32x32x16_bf16 v[32:47], v[96:99], v[52:55], v[32:47]\n"
      "ds_read_b128 v[48:51], v14 offset:53248\n"
      "s_waitcnt lgkmcnt(4)\n"
      "v_mfma_f32_32x32x16_bf16 v[16:31], v[136:139], v[56:59], v[16:31]\n"
      "ds_read_b128 v[52:55], v14 offset:54272\n"
      "s_waitcnt lgkmcnt(4)\n"
      "v_mfma_f32_32x32x16_bf16 v[32:47], v[136:139], v[60:63], v[32:47]\n"
      "s_waitcnt lgkmcnt(3)\n"
      "v_mfma_f32_32x32x16_bf16 v[16:31], v[140:143], v[64:67], v[16:31]\n"
      "s_waitcnt lgkmcnt(2)\n"
      "v_mfma_f32_32x32x16_bf16 v[32:47], v[140:143], v[68:71], v[32:47]\n"
      "s_waitcnt vmcnt(0)\n"
      "s_barrier\n"
      "ds_read_b128 v[56:59], v15 offset:24576\n"
      "ds_read_b128 v[60:63], v15 offset:25600\n"
      "ds_read_b128 v[64:67], v15 offset:26624\n"
      "s_waitcnt lgkmcnt(4)\n"
      "v_mfma_f32_32x32x16_bf16 v[16:31], v[144:147], v[48:51], v[16:31]\n"
      "ds_read_b128 v[68:71], v15 offset:27648\n"
      "s_waitcnt lgkmcnt(4)\n"
      "v_mfma_f32_32x32x16_bf16 v[32:47], v[144:147], v[52:55], v[32:47]\n"
      "ds_read_b128 v[48:51], v15 offset:28672\n"
      "s_waitcnt lgkmcnt(4)\n"
      "v_mfma_f32_32x32x16_bf16 v[16:31], a[32:35], v[56:59], v[16:31]\n"
      "ds_read_b128 v[52:55], v15 offset:29696\n"
      "s_waitcnt lgkmcnt(4)\n"
      "v_mfma_f32_32x32x16_bf16 v[32:47], a[32:35], v[60:63], v[32:47]\n"
      "ds_read_b128 v[56:59], v15 offset:57344\n"
      "s_waitcnt lgkmcnt(4)\n"
      "v_mfma_f32_32x32x16_bf16 v[16:31], a[36:39], v[64:67], v[16:31]\n"
      "ds_read_b128 v[60:63], v15 offset:58368\n"
      "s_waitcnt lgkmcnt(4)\n"
      "v_mfma_f32_32x32x16_bf16 v[32:47], a[36:39], v[68:71], v[32:47]\n"
      "ds_read_b128 v[64:67], v15 offset:59392\n"
      "s_waitcnt lgkmcnt(4)\n"
      "v_mfma_f32_32x32x16_bf16 v[16:31], a[40:43], v[48:51], v[16:31]\n"
      "ds_read_b128 v[68:71], v15 offset:60416\n"
      "s_waitcnt lgkmcnt(4)\n"
      "v_mfma_f32_32x32x16_bf16 v[32:47], a[40:43], v[52:55], v[32:47]\n"
      "ds_read_b128 v[48:51], v15 offset:61440\n"
      "s_waitcnt lgkmcnt(4)\n"
      "v_mfma_f32_32x32x16_bf16 v[16:31], a[76:79], v[56:59], v[16:31]\n"
      "ds_read_b128 v[52:55], v15 offset:62464\n"
      "s_waitcnt lgkmcnt(4)\n"
      "v_mfma_f32_32x32x16_bf16 v[32:47], a[76:79], v[60:63], v[32:47]\n"
      "ds_read_b128 v[56:59], v14 offset:24576\n"
      "s_waitcnt lgkmcnt(4)\n"
      "v_mfma_f32_32x32x16_bf16 v[16:31], a[80:83], v[64:67], v[16:31]\n"
      "ds_read_b128 v[60:63], v14 offset:25600\n"
      "s_waitcnt lgkmcnt(4)\n"
      "v_mfma_f32_32x32x16_bf16 v[32:47], a[80:83], v[68:71], v[32:47]\n"
      "ds_read_b128 v[64:67], v14 offset:26624\n"
      "s_waitcnt lgkmcnt(4)\n"
      "v_mfma_f32_32x32x16_bf16 v[16:31], a[84:87], v[48:51], v[16:31]\n"
      "ds_read_b128 v[68:71], v14 offset:27648\n"
      "s_waitcnt lgkmcnt(4)\n"
      "v_mfma_f32_32x32x16_bf16 v[32:47], a[84:87], v[52:55], v[32:47]\n"
      "ds_read_b128 v[48:51], v14 offset:28672\n"
      "s_waitcnt lgkmcnt(4)\n"
      "v_mfma_f32_32x32x16_bf16 v[16:31], v[100:103], v[56:59], v[16:31]\n"
      "ds_read_b128 v[52:55], v14 offset:29696\n"
      "s_waitcnt lgkmcnt(4)\n"
      "v_mfma_f32_32x32x16_bf16 v[32:47], v[100:103], v[60:63], v[32:47]\n"
      "ds_read_b128 v[56:59], v14 offset:57344\n"
      "s_waitcnt lgkmcnt(4)\n"
      "v_mfma_f32_32x32x16_bf16 v[16:31], v[104:107], v[64:67], v[16:31]\n"
      "ds_read_b128 v[60:63], v14 offset:58368\n"
      "s_waitcnt lgkmcnt(4)\n"
      "v_mfma_f32_32x32x16_bf16 v[32:47], v[104:107], v[68:71], v[32:47]\n"
      "ds_read_b128 v[64:67], v14 offset:59392\n"
      "s_waitcnt lgkmcnt(4)\n"
      "v_mfma_f32_32x32x16_bf16 v[16:31], v[108:111], v[48:51], v[16:31]\n"
      "ds_read_b128 v[68:71], v14 offset:60416\n"
      "s_waitcnt lgkmcnt(4)\n"
      "v_mfma_f32_32x32x16_bf16 v[32:47], v[108:111], v[52:55], v[32:47]\n"
      "ds_read_b128 v[48:51], v14 offset:61440\n"
      "s_waitcnt lgkmcnt(4)\n"
      "v_mfma_f32_32x32x16_bf16 v[16:31], v[148:151], v[56:59], v[16:31]\n"
      "ds_read_b128 v[52:55], v14 offset:62464\n"
      "s_waitcnt lgkmcnt(4)\n"
      "v_mfma_f32_32x32x16_bf16 v[32:47], v[148:151], v[60:63], v[32:47]\n"
      "s_waitcnt lgkmcnt(3)\n"
      "v_mfma_f32_32x32x16_bf16 v[16:31], v[152:155], v[64:67], v[16:31]\n"
      "s_waitcnt lgkmcnt(2)\n"
      "v_mfma_f32_32x32x16_bf16 v[32:47], v[152:155], v[68:71], v[32:47]\n"
      "s_waitcnt lgkmcnt(1)\n"
      "v_mfma_f32_32x32x16_bf16 v[16:31], v[156:159], v[48:51], v[16:31]\n"
      "s_waitcnt lgkmcnt(0)\n"
      "v_mfma_f32_32x32x16_bf16 v[32:47], v[156:159], v[52:55], v[32:47]\n"
      "s_nop 9\n"
      "ds_write_b128 v6, v[16:19] offset:0\n"
      "ds_write_b128 v7, v[20:23] offset:0\n"
      "ds_write_b128 v8, v[24:27] offset:0\n"
      "ds_write_b128 v9, v[28:31] offset:0\n"
      "ds_write_b128 v6, v[32:35] offset:4096\n"
      "ds_write_b128 v7, v[36:39] offset:4096\n"
      "ds_write_b128 v8, v[40:43] offset:4096\n"
      "ds_write_b128 v9, v[44:47] offset:4096\n"
      "s_waitcnt lgkmcnt(0)\n"
      "s_barrier\n"
      ".Lkend:\n"
      : "+{v0}"(tid)
      : "{s[0:1]}"(__builtin_amdgcn_kernarg_segment_ptr()), "{s2}"(blockIdx.x), "{s3}"((unsigned)(size_t)lds)
      : "v1", "v2", "v3", "v4", "v5", "v6", "v7", "v8", "v9", "v10", "v11", "v12", "v13", "v14", "v15", "v16", "v17", "v18", "v19", "v20", "v21", "v22", "v23", "v24", "v25", "v26", "v27", "v28", "v29", "v30", "v31", "v32", "v33", "v34", "v35", "v36", "v37", "v38", "v39", "v40", "v41", "v42", "v43", "v44", "v45", "v46", "v47", "v48", "v49", "v50", "v51", "v52", "v53", "v54", "v55", "v56", "v57", "v58", "v59", "v60", "v61", "v62", "v63", "v64", "v65", "v66", "v67", "v68", "v69", "v70", "v71", "v72", "v73", "v74", "v75", "v76", "v77", "v78", "v79", "v80", "v81", "v82", "v83", "v84", "v85", "v86", "v87", "v88", "v89", "v90", "v91", "v92", "v93", "v94", "v95", "v96", "v97", "v98", "v99", "v100", "v101", "v102", "v103", "v104", "v105", "v106", "v107", "v108", "v109", "v110", "v111", "v112", "v113", "v114", "v115", "v116", "v117", "v118", "v119", "v120", "v121", "v122", "v123", "v124", "v125", "v126", "v127", "v128", "v129", "v130", "v131", "v132", "v133", "v134", "v135", "v136", "v137", "v138", "v139", "v140", "v141", "v142", "v143", "v144", "v145", "v146", "v147", "v148", "v149", "v150", "v151", "v152", "v153", "v154", "v155", "v156", "v157", "v158", "v159", "a0", "a1", "a2", "a3", "a4", "a5", "a6", "a7", "a8", "a9", "a10", "a11", "a12", "a13", "a14", "a15", "a16", "a17", "a18", "a19", "a20", "a21", "a22", "a23", "a24", "a25", "a26", "a27", "a28", "a29", "a30", "a31", "a32", "a33", "a34", "a35", "a36", "a37", "a38", "a39", "a40", "a41", "a42", "a43", "a44", "a45", "a46", "a47", "a48", "a49", "a50", "a51", "a52", "a53", "a54", "a55", "a56", "a57", "a58", "a59", "a60", "a61", "a62", "a63", "a64", "a65", "a66", "a67", "a68", "a69", "a70", "a71", "a72", "a73", "a74", "a75", "a76", "a77", "a78", "a79", "a80", "a81", "a82", "a83", "a84", "a85", "a86", "a87", "a88", "a89", "a90", "a91", "a92", "a93", "a94", "a95", "s4", "s5", "s6", "s7", "s8", "s9", "s10", "s11", "s12", "s13", "s14", "s15", "s16", "s17", "s18", "s19", "s20", "s21", "s22", "s23", "s24", "s25", "s26", "s27", "s28", "s29", "s30", "s31", "s33", "s34", "s35", "s36", "s37", "s38", "s39", "s40", "s41", "s42", "s43", "s44", "s45", "s46", "s47", "s48", "s49", "s50", "s51", "s52", "s53", "s54", "s55", "s56", "s57", "s58", "s59", "s60", "s61", "s62", "s63", "s64", "s65", "s66", "s67", "s68", "s69", "s70", "s71", "s72", "s73", "s74", "s75", "s76", "s77", "s78", "s79", "s80", "s81", "s82", "s83", "s84", "s85", "s86", "s87", "s88", "s89", "s90", "s91", "s92", "s93", "s94", "s95", "s96", "s97", "s98", "s99", "vcc", "scc", "memory");
}

extern "C" void kernel_launch(void* const* d_in, const int* in_sizes, int n_in, void* d_out, int out_size, void* d_ws, size_t ws_size,
                              hipStream_t stream) {
  fused_kernel<<<256, 512, 0, stream>>>(d_in[0], d_in[1], d_in[2], d_in[3], d_in[4], d_in[6], d_in[7], d_in[8], d_in[9], d_out, d_ws);
}
```
